```python
import torch

def _softmax(x, axis=-1):
    return torch.softmax(x, dim=axis)

def _gelu(x):
    return torch.nn.functional.gelu(x, approximate='tanh')

def _silu(x):
    return torch.nn.functional.silu(x)

def _sigmoid(x):
    return torch.sigmoid(x)

def _layernorm(x, axis=-1, eps=1e-05):
    m = x.mean(dim=axis, keepdim=True)
    v = x.var(dim=axis, keepdim=True, unbiased=False)
    return (x - m) / torch.sqrt(v + eps)

def _scatter_add(tgt, idx, src, axis=0):
    return tgt.clone().index_add_(axis, idx, src)

def _segment_sum(src, seg, n):
    out = torch.zeros((n,) + src.shape[1:], dtype=src.dtype)
    return out.index_add_(0, seg, src)

def _pad(x, pw):
    flat = tuple((v for pair in reversed(pw) for v in pair))
    return torch.nn.functional.pad(x, flat)
kernel_name = 'adaLN_cond__sliding_block_cached__dense_activation_squared_relu__prompt'

def setup_inputs():

    _w = lambda a: (lambda t: t.to(torch.bfloat16) if t.is_floating_point() else t)(torch.as_tensor(a))

    def _A_setup():
        B, Sq, D = (1, 2048, 512)
        Hq, Hkv, Sk, Dh = (32, 8, 2048, 128)
        assert Hq % Hkv == 0, (Hq, Hkv)
        return {'x': _w((lambda *s: torch.randn(*s, dtype=torch.bfloat16) * 0.05)(B, Sq, D)), 'k_cache': _w((lambda *s: torch.randn(*s, dtype=torch.bfloat16) * 3.2)(B, Hkv, Sk, Dh)), 'v_cache': _w((lambda *s: torch.randn(*s, dtype=torch.bfloat16) * 0.4)(B, Hkv, Sk, Dh)), 'wq': _w((lambda *s: torch.randn(*s, dtype=torch.bfloat16) * 3.2)(D, Hq * Dh) * D ** (-0.5)), 'wo': _w((lambda *s: torch.randn(*s, dtype=torch.bfloat16) * 0.4)(Hq * Dh, D) * (Hq * Dh) ** (-0.5))}
    _A_in = _A_setup()

    def _B_setup():
        B, S, D, F = (1, 2048, 512, 1792)
        return {'x': _w((lambda *s: torch.randn(*s, dtype=torch.bfloat16) * 0.05)(B, S, D)), 'w1': _w((lambda *s: torch.randn(*s, dtype=torch.bfloat16) * 0.05)(D, F)), 'b1': _w((lambda *s: torch.randn(*s, dtype=torch.bfloat16) * 0.4)(F)), 'w2': _w((lambda *s: torch.randn(*s, dtype=torch.bfloat16) * 0.05)(F, D)), 'b2': _w((lambda *s: torch.randn(*s, dtype=torch.bfloat16) * 6.4)(D))}
    _B_in = _B_setup()
    return {'A_x': _A_in['x'], 'A_k_cache': _A_in['k_cache'], 'A_v_cache': _A_in['v_cache'], 'A_wq': _A_in['wq'], 'A_wo': _A_in['wo'], 'B_x': _B_in['x'], 'B_w1': _B_in['w1'], 'B_b1': _B_in['b1'], 'B_w2': _B_in['w2'], 'B_b2': _B_in['b2'], 'c': _w((lambda *s: torch.randn(*s, dtype=torch.bfloat16) * 0.1)(1, 512) * 8.0), 'w_mod': _w((lambda *s: torch.randn(*s, dtype=torch.bfloat16) * 0.1)(512, 6 * 512) * 512 ** (-0.5) * 8.0)}

def reference(A_x, A_k_cache, A_v_cache, A_wq, A_wo, B_x, B_w1, B_b1, B_w2, B_b2, c, w_mod):

    def _A_ref(x, k_cache, v_cache, wq, wo):
        B, Sq, D = x.shape
        Hq, Dh = (32, 128)
        Hkv, Sk = (k_cache.shape[1], k_cache.shape[2])
        W = min(4096, max(256, Sk // 4))
        rep = Hq // Hkv
        q = (lambda x, *d: x.permute(*d))((x @ wq).reshape(B, Sq, Hq, Dh), 0, 2, 1, 3)
        k = (lambda x, n, axis: torch.repeat_interleave(x, n, dim=axis))(k_cache[:, :, Sk - W:, :], rep, axis=1)
        v = (lambda x, n, axis: torch.repeat_interleave(x, n, dim=axis))(v_cache[:, :, Sk - W:, :], rep, axis=1)
        scores = torch.einsum('bhqd,bhkd->bhqk', q, k) * Dh ** (-0.5)
        p = _softmax(scores, axis=-1)
        o = torch.einsum('bhqk,bhkd->bhqd', p, v)
        return (lambda x, *d: x.permute(*d))(o, 0, 2, 1, 3).reshape(B, Sq, Hq * Dh) @ wo

    def _B_ref(x, w1, b1, w2, b2):
        h = torch.einsum('bsd,df->bsf', x, w1) + b1
        zero = torch.zeros_like(h)
        r = torch.where(h > zero, h, zero)
        h = r * r
        out = torch.einsum('bsf,fd->bsd', h, w2) + b2
        return out
    x = A_x
    mod = (_silu(c) @ w_mod).reshape(c.shape[0], 6, 1, -1)
    sa, ca, ga, sb, cb, gb = (mod[:, 0].clone(), mod[:, 1].clone(), mod[:, 2].clone(), mod[:, 3].clone(), mod[:, 4].clone(), mod[:, 5].clone())
    h = _layernorm(x, axis=-1) * (1 + ca) + sa
    x = x + ga * _A_ref(h, A_k_cache, A_v_cache, A_wq, A_wo)
    h = _layernorm(x, axis=-1) * (1 + cb) + sb
    x = x + gb * _B_ref(h, B_w1, B_b1, B_w2, B_b2)
    return x
```

```cpp
#include <hip/hip_runtime.h>
#include <hip/hip_bf16.h>
#include <cstdint>

typedef unsigned short u16;
typedef short bf16x8 __attribute__((ext_vector_type(8)));
typedef float f32x16 __attribute__((ext_vector_type(16)));
typedef float f32x4 __attribute__((ext_vector_type(4)));
typedef float f32x2 __attribute__((ext_vector_type(2)));
typedef unsigned u32x4 __attribute__((ext_vector_type(4)));
typedef __bf16 bf16x2_t __attribute__((ext_vector_type(2)));
#define DI __device__ __forceinline__
#define MFMA(a, b, c) __builtin_amdgcn_mfma_f32_32x32x16_bf16((a), (b), (c), 0, 0, 0)

constexpr int S = 2048, D = 512, HQ = 32, HKV = 8, DH = 128, WIN = 512, SK = 2048, F = 1792;
constexpr size_t OFF_MOD = 0;
constexpr size_t OFF_WQ = 65536;
constexpr size_t OFF_KT = OFF_WQ + (4u << 20);
constexpr size_t OFF_VT = OFF_KT + (1u << 20);
constexpr size_t OFF_WO = OFF_VT + (1u << 20);
constexpr size_t OFF_W1 = OFF_WO + (4u << 20);
constexpr size_t OFF_W2 = OFF_W1 + (size_t)D * F * 2;
constexpr size_t OFF_PY = OFF_W2 + (size_t)D * F * 2;
constexpr size_t OFF_X2 = OFF_PY + (16u << 20);
constexpr size_t OFF_H2 = OFF_X2 + (2u << 20);
constexpr size_t OFF_PZ = OFF_H2 + (2u << 20);

DI float bf2f(u16 v) { return __uint_as_float((unsigned)v << 16); }
DI unsigned cvtpk(float lo, float hi) { f32x2 v = {lo, hi}; bf16x2_t b = __builtin_convertvector(v, bf16x2_t); return __builtin_bit_cast(unsigned, b); }
DI float rbf(float x) { return __uint_as_float(cvtpk(x, x) << 16); }
DI float lo16(unsigned w) { return __uint_as_float(w << 16); }
DI float hi16(unsigned w) { return __uint_as_float(w & 0xffff0000u); }
DI int swap23(int r) { return (r & ~12) | ((r & 4) << 1) | ((r & 8) >> 1); }
DI int kperm(int hh, int j) { return 8 * (j >> 2) + 4 * hh + (j & 3); }
DI int rho(int m) { return (m & 3) | (((m >> 3) & 3) << 2) | (((m >> 2) & 1) << 4); }

__global__ __launch_bounds__(256) void k_prep(const u16* __restrict__ kc, const u16* __restrict__ vc, const u16* __restrict__ wq, const u16* __restrict__ wo,
                                              const u16* __restrict__ w1, const u16* __restrict__ w2, const u16* __restrict__ cvec, const u16* __restrict__ wmod,
                                              char* __restrict__ ws) {
  const int NMOD = 48;
  if (blockIdx.x < NMOD) {
    __shared__ float red[8][64];
    const int col2 = threadIdx.x & 31, kq = threadIdx.x >> 5;
    float a0 = 0.f, a1 = 0.f;
    for (int k = kq * 64; k < kq * 64 + 64; ++k) {
      const float cv = bf2f(cvec[k]);
      const float sl = rbf(cv / (1.f + __expf(-cv)));
      const unsigned w = *(const unsigned*)(wmod + (size_t)k * 3072 + blockIdx.x * 64 + col2 * 2);
      a0 += sl * lo16(w); a1 += sl * hi16(w);
    }
    red[kq][col2 * 2] = a0; red[kq][col2 * 2 + 1] = a1;
    __syncthreads();
    if (threadIdx.x < 64) { float s = 0.f; for (int i = 0; i < 8; ++i) s += red[i][threadIdx.x];
      ((float*)(ws + OFF_MOD))[blockIdx.x * 64 + threadIdx.x] = rbf(s); }
    return;
  }
  const long NWQ = 262144, NKT = 65536, NVT = 65536, NWO = 262144, NW1 = 114688, NW2 = 114688;
  for (long p = (long)(blockIdx.x - NMOD) * 256 + threadIdx.x; p < NWQ + NKT + NVT + NWO + NW1 + NW2; p += (long)(gridDim.x - NMOD) * 256) {
    long q = p; u16 v[8]; char* dst;
    const int l = q & 63, r = l & 31, hh = l >> 5;
    if (q < NWQ) {
      const int dt = (q >> 6) & 3, ks = (q >> 8) & 31, h = q >> 13;
      for (int j = 0; j < 8; ++j) v[j] = wq[(size_t)(16 * ks + 8 * hh + j) * 4096 + 128 * h + 32 * dt + swap23(r)];
      dst = ws + OFF_WQ + q * 16;
    } else if ((q -= NWQ) < NKT) {
      const int ds = (q >> 6) & 7, kt = (q >> 9) & 15, g = q >> 13;
      for (int j = 0; j < 8; ++j) v[j] = kc[((size_t)g * SK + (SK - WIN) + 32 * kt + r) * DH + 16 * ds + 8 * hh + j];
      dst = ws + OFF_KT + q * 16;
    } else if ((q -= NKT) < NVT) {
      const int dt = (q >> 6) & 3, s = (q >> 8) & 1, kt = (q >> 9) & 15, g = q >> 13;
      for (int j = 0; j < 8; ++j) v[j] = vc[((size_t)g * SK + (SK - WIN) + 32 * kt + 16 * s + kperm(hh, j)) * DH + 32 * dt + r];
      dst = ws + OFF_VT + q * 16;
    } else if ((q -= NVT) < NWO) {
      const int ks = (q >> 6) & 31, nt = (q >> 11) & 15, g = q >> 15;
      for (int j = 0; j < 8; ++j) v[j] = wo[(size_t)(512 * g + 16 * ks + kperm(hh, j)) * D + 32 * nt + rho(r)];
      dst = ws + OFF_WO + q * 16;
    } else if ((q -= NWO) < NW1) {
      const int ks = (q >> 6) & 31; const int gft = q >> 11;
      for (int j = 0; j < 8; ++j) v[j] = w1[(size_t)(16 * ks + 8 * hh + j) * F + 32 * gft + r];
      dst = ws + OFF_W1 + q * 16;
    } else {
      q -= NW1;
      const int t = q >> 6; const int ks = t % 14, nt = (t / 14) & 15, g = t / 224;
      for (int j = 0; j < 8; ++j) v[j] = w2[(size_t)(224 * g + 16 * ks + kperm(hh, j)) * D + 32 * nt + rho(r)];
      dst = ws + OFF_W2 + q * 16;
    }
    u32x4 w; for (int e = 0; e < 4; ++e) w[e] = (unsigned)v[2 * e] | ((unsigned)v[2 * e + 1] << 16);
    *(u32x4*)dst = w;
  }
}

DI u32x4 ln_mod8(const float (&x)[8], float mean, float sd, const float* __restrict__ sc, const float* __restrict__ sh) {
  float o[8];
  for (int e = 0; e < 8; ++e) { const float n = rbf(rbf(x[e] - mean) / sd); o[e] = rbf(n * rbf(1.f + sc[e])) + sh[e]; }
  u32x4 w; for (int e = 0; e < 4; ++e) w[e] = cvtpk(o[2 * e], o[2 * e + 1]);
  return w;
}

DI bf16x8 pack8(const f32x16& x, int s, float mul) {
  u32x4 w;
  for (int e = 0; e < 4; ++e) w[e] = cvtpk(x[8 * s + 2 * e] * mul, x[8 * s + 2 * e + 1] * mul);
  return __builtin_bit_cast(bf16x8, w);
}

__global__ __launch_bounds__(256, 1) void k_attn(const u16* __restrict__ x, char* __restrict__ ws) {
  extern __shared__ __attribute__((aligned(16))) char lds[];
  const int tid = threadIdx.x, lane = tid & 63, wid = __builtin_amdgcn_readfirstlane(tid >> 6);
  const int g = blockIdx.x & 7, rb = blockIdx.x >> 3;
  const float* mod = (const float*)(ws + OFF_MOD);
  for (int pass = 0; pass < 2; ++pass) {
    const int row = wid * 16 + pass * 8 + (lane & 7), c0 = lane >> 3;
    const u16* xr = x + (size_t)(rb * 64 + row) * D;
    u32x4 raw[8];
    for (int i = 0; i < 8; ++i) raw[i] = *(const u32x4*)(xr + (c0 + 8 * i) * 8);
    float s = 0.f;
    for (int i = 0; i < 8; ++i) for (int e = 0; e < 4; ++e) s += lo16(raw[i][e]) + hi16(raw[i][e]);
    s += __shfl_xor(s, 8); s += __shfl_xor(s, 16); s += __shfl_xor(s, 32);
    const float mean = s * (1.f / D);
    float v = 0.f;
    for (int i = 0; i < 8; ++i) for (int e = 0; e < 4; ++e) { float a = lo16(raw[i][e]) - mean, b = hi16(raw[i][e]) - mean; v += a * a + b * b; }
    v += __shfl_xor(v, 8); v += __shfl_xor(v, 16); v += __shfl_xor(v, 32);
    const float meanb = rbf(mean), sd = rbf(sqrtf(rbf(rbf(v * (1.f / D)) + 1e-5f)));
    for (int i = 0; i < 8; ++i) {
      const int c8 = c0 + 8 * i;
      float xv[8]; for (int e = 0; e < 4; ++e) { xv[2 * e] = lo16(raw[i][e]); xv[2 * e + 1] = hi16(raw[i][e]); }
      const u32x4 w = ln_mod8(xv, meanb, sd, mod + 512 + c8 * 8, mod + c8 * 8);
      *(u32x4*)(lds + ((row >> 5) * 64 + c8) * 512 + (row & 31) * 16) = w;
    }
  }
  __syncthreads();
  bf16x8 qf[2][8];
  {
    f32x16 acc[4][2] = {};
    const bf16x8* wp = (const bf16x8*)(ws + OFF_WQ) + (size_t)(4 * g + wid) * 8192 + lane;
#pragma unroll 4
    for (int ks = 0; ks < 32; ++ks) {
      bf16x8 a[4], b[2];
      for (int dt = 0; dt < 4; ++dt) a[dt] = wp[(ks * 4 + dt) * 64];
      for (int rt = 0; rt < 2; ++rt) b[rt] = *(const bf16x8*)(lds + (rt * 32 + ks) * 1024 + lane * 16);
      for (int dt = 0; dt < 4; ++dt) for (int rt = 0; rt < 2; ++rt) acc[dt][rt] = MFMA(a[dt], b[rt], acc[dt][rt]);
    }
    const float C = 0.088388347648318440f * 1.4426950408889634f;
#pragma unroll
    for (int rt = 0; rt < 2; ++rt)
#pragma unroll
      for (int dt = 0; dt < 4; ++dt) { qf[rt][2 * dt] = pack8(acc[dt][rt], 0, C); qf[rt][2 * dt + 1] = pack8(acc[dt][rt], 1, C); }
  }
  f32x16 o[4][2] = {};
  float m[2] = {-1e30f, -1e30f}, l[2] = {0.f, 0.f};
  const u32x4* kg = (const u32x4*)(ws + OFF_KT) + (size_t)g * 8192;
  const u32x4* vg = (const u32x4*)(ws + OFF_VT) + (size_t)g * 8192;
  char* kv = lds + 65536;
  for (int t = 0; t < 8; ++t) {
    char* kb = kv + (t & 1) * 32768; char* vb = kb + 16384;
    for (int i = 0; i < 4; ++i) { *(u32x4*)(kb + (i * 256 + tid) * 16) = kg[t * 1024 + i * 256 + tid]; *(u32x4*)(vb + (i * 256 + tid) * 16) = vg[t * 1024 + i * 256 + tid]; }
    __syncthreads();
    f32x16 s[2][2] = {};
#pragma unroll
    for (int ds = 0; ds < 8; ++ds)
#pragma unroll
      for (int kb2 = 0; kb2 < 2; ++kb2) {
        const bf16x8 a = *(const bf16x8*)(kb + (kb2 * 8 + ds) * 1024 + lane * 16);
        for (int rt = 0; rt < 2; ++rt) s[kb2][rt] = MFMA(a, qf[rt][ds], s[kb2][rt]);
      }
    bf16x8 p[2][4];
#pragma unroll
    for (int rt = 0; rt < 2; ++rt) {
      float mx = s[0][rt][0];
      for (int e = 0; e < 16; ++e) mx = fmaxf(mx, fmaxf(s[0][rt][e], s[1][rt][e]));
      mx = fmaxf(mx, __shfl_xor(mx, 32));
      const float mn = fmaxf(m[rt], mx), al = __builtin_amdgcn_exp2f(m[rt] - mn);
      m[rt] = mn;
      float ps = 0.f;
      for (int kb2 = 0; kb2 < 2; ++kb2) for (int e = 0; e < 16; ++e) { s[kb2][rt][e] = __builtin_amdgcn_exp2f(s[kb2][rt][e] - mn); ps += s[kb2][rt][e]; }
      l[rt] = l[rt] * al + ps;
      for (int dt = 0; dt < 4; ++dt) o[dt][rt] *= al;
      for (int kb2 = 0; kb2 < 2; ++kb2) { p[rt][2 * kb2] = pack8(s[kb2][rt], 0, 1.f); p[rt][2 * kb2 + 1] = pack8(s[kb2][rt], 1, 1.f); }
    }
#pragma unroll
    for (int ks = 0; ks < 4; ++ks)
#pragma unroll
      for (int dt = 0; dt < 4; ++dt) {
        const bf16x8 a = *(const bf16x8*)(vb + (ks * 4 + dt) * 1024 + lane * 16);
        for (int rt = 0; rt < 2; ++rt) o[dt][rt] = MFMA(a, p[rt][ks], o[dt][rt]);
      }
  }
#pragma unroll
  for (int rt = 0; rt < 2; ++rt) {
    const float lt = l[rt] + __shfl_xor(l[rt], 32), il = 1.f / lt;
#pragma unroll
    for (int dt = 0; dt < 4; ++dt)
#pragma unroll
      for (int s2 = 0; s2 < 2; ++s2) *(bf16x8*)(lds + (rt * 32 + wid * 8 + dt * 2 + s2) * 1024 + lane * 16) = pack8(o[dt][rt], s2, il);
  }
  __syncthreads();
  {
    f32x16 acc[4][2] = {};
    const bf16x8* wp = (const bf16x8*)(ws + OFF_WO) + (size_t)(g * 16 + wid * 4) * 2048 + lane;
#pragma unroll 4
    for (int ks = 0; ks < 32; ++ks) {
      bf16x8 a[4], b[2];
      for (int nt = 0; nt < 4; ++nt) a[nt] = wp[(nt * 32 + ks) * 64];
      for (int rt = 0; rt < 2; ++rt) b[rt] = *(const bf16x8*)(lds + (rt * 32 + ks) * 1024 + lane * 16);
      for (int nt = 0; nt < 4; ++nt) for (int rt = 0; rt < 2; ++rt) acc[nt][rt] = MFMA(a[nt], b[rt], acc[nt][rt]);
    }
    bf16x8* py = (bf16x8*)(ws + OFF_PY) + (size_t)(rb * 8 + g) * 4096 + lane;
#pragma unroll
    for (int nt = 0; nt < 4; ++nt)
#pragma unroll
      for (int rt = 0; rt < 2; ++rt)
#pragma unroll
        for (int hf = 0; hf < 2; ++hf) py[(((wid * 4 + nt) * 2 + rt) * 2 + hf) * 64] = pack8(acc[nt][rt], hf, 1.f);
  }
}

DI int chunk_off(int c8, int rt, int row32) { const int nt = c8 >> 2, hh = (c8 >> 1) & 1, hf = c8 & 1; return (((nt * 2 + rt) * 2 + hf) * 2 + hh) * 32 + row32; }

__global__ __launch_bounds__(256) void k_red1(const u16* __restrict__ x, char* __restrict__ ws) {
  __shared__ float red[2][4][8];
  const int t = threadIdx.x, gp = blockIdx.x & 7, rb = blockIdx.x >> 3, r8 = t & 7, wid = t >> 6;
  const int row = gp * 8 + r8, rt = row >> 5, row32 = row & 31;
  const float* mod = (const float*)(ws + OFF_MOD);
  const u32x4* py = (const u32x4*)(ws + OFF_PY) + (size_t)rb * 8 * 4096;
  float xv[2][8];
  for (int i = 0; i < 2; ++i) {
    const int c8 = (t >> 3) + 32 * i;
    float a[8] = {};
    for (int g = 0; g < 8; ++g) { const u32x4 w = py[g * 4096 + chunk_off(c8, rt, row32)]; for (int e = 0; e < 4; ++e) { a[2 * e] += lo16(w[e]); a[2 * e + 1] += hi16(w[e]); } }
    const u32x4 xr = *(const u32x4*)(x + (size_t)(rb * 64 + row) * D + c8 * 8);
    for (int e = 0; e < 8; ++e) { const float xi = (e & 1) ? hi16(xr[e >> 1]) : lo16(xr[e >> 1]); xv[i][e] = rbf(xi + rbf(mod[1024 + c8 * 8 + e] * rbf(a[e]))); }
    u32x4 w; for (int e = 0; e < 4; ++e) w[e] = cvtpk(xv[i][2 * e], xv[i][2 * e + 1]);
    *(u32x4*)(ws + OFF_X2 + ((size_t)(rb * 64 + row) * D + c8 * 8) * 2) = w;
  }
  float s = 0.f; for (int i = 0; i < 2; ++i) for (int e = 0; e < 8; ++e) s += xv[i][e];
  s += __shfl_xor(s, 8); s += __shfl_xor(s, 16); s += __shfl_xor(s, 32);
  if ((t & 63) < 8) red[0][wid][r8] = s;
  __syncthreads();
  const float mean = (red[0][0][r8] + red[0][1][r8] + red[0][2][r8] + red[0][3][r8]) * (1.f / D);
  float v = 0.f; for (int i = 0; i < 2; ++i) for (int e = 0; e < 8; ++e) { const float d = xv[i][e] - mean; v += d * d; }
  v += __shfl_xor(v, 8); v += __shfl_xor(v, 16); v += __shfl_xor(v, 32);
  if ((t & 63) < 8) red[1][wid][r8] = v;
  __syncthreads();
  const float var = (red[1][0][r8] + red[1][1][r8] + red[1][2][r8] + red[1][3][r8]) * (1.f / D);
  const float meanb = rbf(mean), sd = rbf(sqrtf(rbf(rbf(var) + 1e-5f)));
  for (int i = 0; i < 2; ++i) {
    const int c8 = (t >> 3) + 32 * i;
    const u32x4 w = ln_mod8(xv[i], meanb, sd, mod + 2048 + c8 * 8, mod + 1536 + c8 * 8);
    *(u32x4*)(ws + OFF_H2 + (size_t)rb * 65536 + (rt * 64 + c8) * 512 + row32 * 16) = w;
  }
}

__global__ __launch_bounds__(256, 1) void k_mlp(const u16* __restrict__ b1, char* __restrict__ ws) {
  extern __shared__ __attribute__((aligned(16))) char lds[];
  const int tid = threadIdx.x, lane = tid & 63, wid = __builtin_amdgcn_readfirstlane(tid >> 6);
  const int g = blockIdx.x & 7, rb = blockIdx.x >> 3, hh = lane >> 5;
  const u32x4* h2 = (const u32x4*)(ws + OFF_H2 + (size_t)rb * 65536);
  for (int i = 0; i < 16; ++i) *(u32x4*)(lds + (i * 256 + tid) * 16) = h2[i * 256 + tid];
  __syncthreads();
  char* h1 = lds + 65536;
  for (int ft = wid; ft < 7; ft += 4) {
    f32x16 acc[2] = {};
    const bf16x8* wp = (const bf16x8*)(ws + OFF_W1) + (size_t)(g * 7 + ft) * 2048 + lane;
#pragma unroll 8
    for (int ks = 0; ks < 32; ++ks) {
      const bf16x8 a = wp[ks * 64];
      for (int rt = 0; rt < 2; ++rt) acc[rt] = MFMA(a, *(const bf16x8*)(lds + (rt * 32 + ks) * 1024 + lane * 16), acc[rt]);
    }
    for (int e = 0; e < 16; ++e) {
      const float bias = bf2f(b1[g * 224 + ft * 32 + (e & 3) + 8 * (e >> 2) + 4 * hh]);
      for (int rt = 0; rt < 2; ++rt) { float hv = rbf(rbf(acc[rt][e]) + bias); hv = fmaxf(hv, 0.f); acc[rt][e] = hv * hv; }
    }
    for (int rt = 0; rt < 2; ++rt) for (int s2 = 0; s2 < 2; ++s2) *(bf16x8*)(h1 + (rt * 14 + ft * 2 + s2) * 1024 + lane * 16) = pack8(acc[rt], s2, 1.f);
  }
  __syncthreads();
  {
    f32x16 acc[4][2] = {};
    const bf16x8* wp = (const bf16x8*)(ws + OFF_W2) + (size_t)(g * 16 + wid * 4) * 14 * 64 + lane;
#pragma unroll 2
    for (int ks = 0; ks < 14; ++ks) {
      bf16x8 a[4], b[2];
      for (int nt = 0; nt < 4; ++nt) a[nt] = wp[(nt * 14 + ks) * 64];
      for (int rt = 0; rt < 2; ++rt) b[rt] = *(const bf16x8*)(h1 + (rt * 14 + ks) * 1024 + lane * 16);
      for (int nt = 0; nt < 4; ++nt) for (int rt = 0; rt < 2; ++rt) acc[nt][rt] = MFMA(a[nt], b[rt], acc[nt][rt]);
    }
    bf16x8* pz = (bf16x8*)(ws + OFF_PZ) + (size_t)(rb * 8 + g) * 4096 + lane;
#pragma unroll
    for (int nt = 0; nt < 4; ++nt)
#pragma unroll
      for (int rt = 0; rt < 2; ++rt)
#pragma unroll
        for (int hf = 0; hf < 2; ++hf) pz[(((wid * 4 + nt) * 2 + rt) * 2 + hf) * 64] = pack8(acc[nt][rt], hf, 1.f);
  }
}

__global__ __launch_bounds__(256) void k_red2(const u16* __restrict__ b2, const char* __restrict__ ws, u16* __restrict__ out) {
  const int t = threadIdx.x, gp = blockIdx.x & 7, rb = blockIdx.x >> 3, r8 = t & 7;
  const int row = gp * 8 + r8, rt = row >> 5, row32 = row & 31;
  const float* mod = (const float*)(ws + OFF_MOD);
  const u32x4* pz = (const u32x4*)(ws + OFF_PZ) + (size_t)rb * 8 * 4096;
  for (int i = 0; i < 2; ++i) {
    const int c8 = (t >> 3) + 32 * i;
    float a[8] = {};
    for (int g = 0; g < 8; ++g) { const u32x4 w = pz[g * 4096 + chunk_off(c8, rt, row32)]; for (int e = 0; e < 4; ++e) { a[2 * e] += lo16(w[e]); a[2 * e + 1] += hi16(w[e]); } }
    const u32x4 xr = *(const u32x4*)(ws + OFF_X2 + ((size_t)(rb * 64 + row) * D + c8 * 8) * 2);
    const u32x4 br = *(const u32x4*)(b2 + c8 * 8);
    float o[8];
    for (int e = 0; e < 8; ++e) {
      const float xi = (e & 1) ? hi16(xr[e >> 1]) : lo16(xr[e >> 1]), bi = (e & 1) ? hi16(br[e >> 1]) : lo16(br[e >> 1]);
      o[e] = xi + rbf(mod[2560 + c8 * 8 + e] * rbf(rbf(a[e]) + bi));
    }
    u32x4 w; for (int e = 0; e < 4; ++e) w[e] = cvtpk(o[2 * e], o[2 * e + 1]);
    *(u32x4*)(out + (size_t)(rb * 64 + row) * D + c8 * 8) = w;
  }
}

extern "C" void kernel_launch(void* const* d_in, const int* in_sizes, int n_in, void* d_out, int out_size, void* d_ws, size_t ws_size, hipStream_t stream) {
  const u16* x = (const u16*)d_in[0]; const u16* kc = (const u16*)d_in[1]; const u16* vc = (const u16*)d_in[2];
  const u16* wq = (const u16*)d_in[3]; const u16* wo = (const u16*)d_in[4];
  const u16* w1 = (const u16*)d_in[6]; const u16* b1 = (const u16*)d_in[7]; const u16* w2 = (const u16*)d_in[8]; const u16* b2 = (const u16*)d_in[9];
  const u16* cv = (const u16*)d_in[10]; const u16* wmod = (const u16*)d_in[11];
  char* ws = (char*)d_ws;
  hipFuncSetAttribute((const void*)k_attn, hipFuncAttributeMaxDynamicSharedMemorySize, 131072);
  hipFuncSetAttribute((const void*)k_mlp, hipFuncAttributeMaxDynamicSharedMemorySize, 98304);
  k_prep<<<48 + 2048, 256, 0, stream>>>(kc, vc, wq, wo, w1, w2, cv, wmod, ws);
  k_attn<<<256, 256, 131072, stream>>>(x, ws);
  k_red1<<<256, 256, 0, stream>>>(x, ws);
  k_mlp<<<256, 256, 98304, stream>>>(b1, ws);
  k_red2<<<256, 256, 0, stream>>>(b2, ws, (u16*)d_out);
}
```

```cpp
#include <hip/hip_runtime.h>
#include <hip/hip_bf16.h>
#include <cstdint>

__global__ __launch_bounds__(256, 1) void k_main(const char* x, const char* kc, const char* vc, const char* wq, const char* wo,
                                                 const char* w1, const char* b1, const char* w2, const char* b2,
                                                 const char* cv, const char* wm, char* ws, char* out) {
  asm volatile(
    "k_begin:\n"
    "s_load_dwordx16 s[4:19], %[ka], 0x0\n"
    "s_load_dwordx8 s[20:27], %[ka], 0x40\n"
    "s_load_dwordx2 s[28:29], %[ka], 0x60\n"
    "s_getpc_b64 s[92:93]\n"
    "s_lshr_b32 s73, %[bx], 3\n"
    "s_lshl_b32 s73, s73, 10\n"
    "v_and_b32_e64 v218, 7, %[tid]\n"
    "v_lshl_add_u32 v218, v218, 7, s73\n"
    "v_min_u32 v218, k_end-k_begin-64, v218\n"
    "global_load_dword v218, v218, s[92:93]\n"
    "v_readfirstlane_b32 s30, %[tid]\n"
    "v_and_b32 v250, 63, %[tid]\n"
    "s_mov_b32 s31, %[bx]\n"
    "s_and_b32 s3, s31, 7\n"
    "s_lshr_b32 s33, s31, 3\n"
    "s_lshr_b32 s30, s30, 6\n"
    "s_lshl_b32 s36, s3, 5\n"
    "s_add_u32 s36, s36, s33\n"
    "s_lshl_b32 s73, s33, 5\n"
    "v_add_u32 v252, s73, %[tid]\n"
    "v_and_b32 v252, 0x1ff, v252\n"
    "v_xor_b32 v102, 0x100, v252\n"
    "v_mul_u32_u24 v251, 0x1800, v252\n"
    "v_mul_u32_u24 v219, 0x1800, v102\n"
    "v_lshlrev_b32 v252, 1, v252\n"
    "v_lshlrev_b32 v102, 1, v102\n"
    "v_and_b32 v253, 7, v250\n"
    "v_lshrrev_b32 v254, 3, v250\n"
    "v_lshlrev_b32 v225, 4, v254\n"
    "v_lshl_or_b32 v225, v253, 10, v225\n"
    "s_lshl_b32 s96, s36, 3\n"
    "s_lshl_b32 s39, s33, 2\n"
    "s_add_u32 s39, s39, s30\n"
    "s_lshr_b32 s34, s33, 2\n"
    "s_and_b32 s35, s33, 3\n"
    "s_lshr_b32 s73, s3, 2\n"
    "s_lshl_b32 s73, s73, 3\n"
    "s_add_u32 s34, s34, s73\n"
    "s_bfe_u32 s73, s3, 65537\n"
    "s_lshl_b32 s73, s73, 2\n"
    "s_or_b32 s35, s35, s73\n"
    "s_andn2_b32 s73, 1, s3\n"
    "s_lshl_b32 s73, s73, 4\n"
    "s_add_u32 s34, s34, s73\n"
    "s_mov_b32 s82, s3\n"
    "s_and_b32 s73, s33, 1\n"
    "s_and_b32 s3, s82, 6\n"
    "s_or_b32 s3, s3, s73\n"
    "s_lshr_b32 s33, s33, 1\n"
    "s_lshl_b32 s81, s33, 2\n"
    "s_add_u32 s81, s81, s30\n"
    "s_and_b32 s73, s82, 1\n"
    "s_lshl_b32 s73, s73, 4\n"
    "s_or_b32 s33, s33, s73\n"
    "s_lshl_b32 s31, s33, 3\n"
    "s_or_b32 s31, s31, s3\n"
    "s_lshl_b32 s73, s33, 16\n"
    "s_lshl_b32 s90, s30, 8\n"
    "s_add_u32 s73, s73, s90\n"
    "s_mov_b32 s54, 3145728\n"
    "s_mov_b32 s55, 0x20000\n"
    "s_mov_b32 s58, 0x200000\n"
    "s_mov_b32 s59, 0x20000\n"
    "s_waitcnt lgkmcnt(0)\n"
    "s_mov_b64 s[52:53], s[24:25]\n"
    "s_mov_b64 s[56:57], s[4:5]\n"
    "global_load_ushort v64, v252, s[22:23]\n"
    "global_load_ushort v65, v102, s[22:23]\n"
    "buffer_load_dwordx2 v[66:67], v251, s[52:55], s96 offen\n"
    "buffer_load_dwordx2 v[68:69], v219, s[52:55], s96 offen\n"
    "buffer_load_dwordx4 v[0:3], v225, s[56:59], s73 offen offset:0\n"
    "buffer_load_dwordx4 v[4:7], v225, s[56:59], s73 offen offset:128\n"
    "s_add_u32 s73, s73, 8192\n"
    "buffer_load_dwordx4 v[8:11], v225, s[56:59], s73 offen offset:0\n"
    "buffer_load_dwordx4 v[12:15], v225, s[56:59], s73 offen offset:128\n"
    "s_add_u32 s73, s73, 8192\n"
    "buffer_load_dwordx4 v[16:19], v225, s[56:59], s73 offen offset:0\n"
    "buffer_load_dwordx4 v[20:23], v225, s[56:59], s73 offen offset:128\n"
    "s_add_u32 s73, s73, 8192\n"
    "buffer_load_dwordx4 v[24:27], v225, s[56:59], s73 offen offset:0\n"
    "buffer_load_dwordx4 v[28:31], v225, s[56:59], s73 offen offset:128\n"
    "s_add_u32 s73, s73, 8192\n"
    "buffer_load_dwordx4 v[32:35], v225, s[56:59], s73 offen offset:0\n"
    "buffer_load_dwordx4 v[36:39], v225, s[56:59], s73 offen offset:128\n"
    "s_add_u32 s73, s73, 8192\n"
    "buffer_load_dwordx4 v[40:43], v225, s[56:59], s73 offen offset:0\n"
    "buffer_load_dwordx4 v[44:47], v225, s[56:59], s73 offen offset:128\n"
    "s_add_u32 s73, s73, 8192\n"
    "buffer_load_dwordx4 v[48:51], v225, s[56:59], s73 offen offset:0\n"
    "buffer_load_dwordx4 v[52:55], v225, s[56:59], s73 offen offset:128\n"
    "s_add_u32 s73, s73, 8192\n"
    "buffer_load_dwordx4 v[56:59], v225, s[56:59], s73 offen offset:0\n"
    "buffer_load_dwordx4 v[60:63], v225, s[56:59], s73 offen offset:128\n"
    "s_load_dwordx2 s[94:95], s[26:27], 0x0\n"
    "v_lshrrev_b32_e64 v100, 5, v250\n"
    "v_bfe_u32 v101, v250, 4, 1\n"
    "v_bfe_u32 v102, v250, 2, 2\n"
    "v_and_b32 v103, 3, v250\n"
    "v_lshrrev_b32 v104, 4, v250\n"
    "v_and_b32 v105, 31, v250\n"
    "v_lshlrev_b32 v224, 4, v250\n"
    "v_lshlrev_b32 v226, 4, v253\n"
    "v_lshl_or_b32 v226, v254, 9, v226\n"
    "v_lshlrev_b32 v106, 4, v103\n"
    "v_lshl_or_b32 v107, v104, 6, v106\n"
    "v_and_b32 v109, 15, v250\n"
    "v_lshl_or_b32 v110, v101, 1, v100\n"
    "s_lshl_b32 s73, s30, 11\n"
    "v_lshl_or_b32 v236, v102, 8, v107\n"
    "v_or_b32 v236, s73, v236\n"
    "v_xor_b32 v233, 32, v236\n"
    "v_lshl_or_b32 v107, v101, 6, v106\n"
    "v_lshl_or_b32 v108, v100, 4, v102\n"
    "v_lshl_or_b32 v228, v108, 10, v107\n"
    "s_lshr_b32 s96, s30, 1\n"
    "v_lshl_or_b32 v108, v100, 2, v102\n"
    "v_lshl_or_b32 v227, v108, 13, v107\n"
    "v_xor_b32 v220, 32, v227\n"
    "v_mul_u32_u24 v230, 0xe00, v108\n"
    "v_add_u32 v230, v230, v107\n"
    "v_lshlrev_b32 v231, 4, v109\n"
    "v_lshl_or_b32 v231, v104, 9, v231\n"
    "v_lshlrev_b32_e64 v232, 3, v103\n"
    "v_lshl_or_b32 v232, v101, 5, v232\n"
    "v_lshl_or_b32 v232, v102, 6, v232\n"
    "v_lshl_or_b32 v232, v104, 10, v232\n"
    "v_add_u32 v238, 147456, v232\n"
    "v_xor_b32 v106, v109, v104\n"
    "s_and_b32 s97, s30, 1\n"
    "s_lshl_b32 s97, s97, 3\n"
    "v_xor_b32 v106, s97, v106\n"
    "v_lshlrev_b32 v106, 4, v106\n"
    "v_lshl_or_b32 v234, v104, 8, v106\n"
    "v_or_b32 v234, s73, v234\n"
    "v_xor_b32 v235, 64, v234\n"
    "v_xor_b32 v106, v110, v109\n"
    "v_lshlrev_b32_e64 v106, 4, v106\n"
    "v_lshl_or_b32 v237, v109, 8, v106\n"
    "v_add_u32 v237, 131072, v237\n"
    "v_lshlrev_b32 v106, 4, v105\n"
    "v_lshl_or_b32 v239, v100, 11, v106\n"
    "v_lshlrev_b32 v247, 4, v110\n"
    "v_lshl_or_b32 v247, v109, 8, v247\n"
    "v_lshlrev_b32 v242, 3, v253\n"
    "s_lshr_b32 s73, s35, 2\n"
    "s_and_b32 s97, s35, 3\n"
    "s_lshl_b32 s97, s97, 7\n"
    "s_lshl_b32 s38, s73, 15\n"
    "s_or_b32 s38, s38, s97\n"
    "s_lshr_b32 s73, s35, 1\n"
    "s_lshl_b32 s73, s73, 10\n"
    "s_and_b32 s97, s35, 1\n"
    "s_lshl_b32 s97, s97, 7\n"
    "s_or_b32 s73, s73, s97\n"
    "v_lshlrev_b32 v241, 4, v253\n"
    "v_lshl_or_b32 v241, v100, 12, v241\n"
    "v_lshl_or_b32 v241, v101, 8, v241\n"
    "v_and_b32 v107, 1, v254\n"
    "v_lshl_or_b32 v241, v107, 9, v241\n"
    "v_or_b32 v241, s73, v241\n"
    "s_lshl_b32 s73, s30, 2\n"
    "v_lshlrev_b32 v243, 5, v253\n"
    "v_lshl_or_b32 v243, v100, 4, v243\n"
    "v_add_u32 v243, s73, v243\n"
    "s_lshl_b32 s73, s35, 7\n"
    "s_lshl_b32 s97, s96, 6\n"
    "s_add_u32 s73, s73, s97\n"
    "s_and_b32 s97, s30, 1\n"
    "s_lshl_b32 s97, s97, 4\n"
    "v_lshlrev_b32 v229, 4, v104\n"
    "v_add_u32 v245, s73, v229\n"
    "v_or_b32 v107, s97, v109\n"
    "v_lshlrev_b32 v108, 4, v107\n"
    "v_lshl_add_u32 v244, v245, 5, v108\n"
    "v_lshl_add_u32 v246, v107, 10, v245\n"
    "s_lshl_b32 s73, s73, 1\n"
    "s_add_u32 s73, s73, 10240\n"
    "s_lshl_b32 s97, s30, 9\n"
    "s_add_u32 s97, s97, 8192\n"
    "v_add_u32 v240, s97, v106\n"
    "v_lshlrev_b32 v107, 4, v253\n"
    "v_add_u32_e64 v107, s73, v107\n"
    "v_cmp_lt_u32 vcc, 31, v250\n"
    "s_nop 1\n"
    "v_cndmask_b32 v240, v240, v107, vcc\n"
    "s_mov_b64 s[60:61], s[26:27]\n"
    "s_mov_b32 s62, 30932992\n"
    "s_mov_b32 s63, 0x20000\n"
    "s_mov_b32 s74, 0xffff0000\n"
    "s_mov_b32 s91, 0\n"
    "s_mov_b32 s98, 0x3f80\n"
    "s_mov_b32 s99, 0x3f800000\n"
    "s_mov_b32 s86, 0x05040100\n"
    "s_mov_b32 s87, 0x07060302\n"
    "s_waitcnt vmcnt(18)\n"
    "v_lshlrev_b32 v80, 16, v64\n"
    "v_lshlrev_b32_e64 v81, 16, v65\n"
    "v_mul_f32 v82, 0xbfb8aa3b, v80\n"
    "v_mul_f32 v83, 0xbfb8aa3b, v81\n"
    "v_exp_f32 v82, v82\n"
    "v_exp_f32 v83, v83\n"
    "s_nop 0\n"
    "v_add_f32 v82, 1.0, v82\n"
    "v_add_f32 v83, 1.0, v83\n"
    "v_rcp_f32 v82, v82\n"
    "v_rcp_f32 v83, v83\n"
    "s_nop 0\n"
    "v_mul_f32 v80, v80, v82\n"
    "v_mul_f32 v81, v81, v83\n"
    "v_cvt_pk_bf16_f32 v64, v80, v81\n"
    "v_mov_b32 v252, v64\n"
    "v_lshl_or_b32 v106, v101, 1, v100\n"
    "v_lshlrev_b32_e64 v106, 4, v106\n"
    "s_lshl_b32 s73, s30, 2\n"
    "v_add_u32 v106, s73, v106\n"
    "s_waitcnt vmcnt(16)\n"
    "v_perm_b32 v82, v68, v66, s86\n"
    "v_perm_b32 v83, v68, v66, s87\n"
    "v_perm_b32 v84, v69, v67, s86\n"
    "v_perm_b32 v85, v69, v67, s87\n"
    "v_dot2_f32_bf16 v88, v82, v64, 0\n"
    "v_dot2_f32_bf16 v89, v83, v64, 0\n"
    "v_dot2_f32_bf16 v90, v84, v64, 0\n"
    "v_dot2_f32_bf16 v91, v85, v64, 0\n"
    "s_nop 2\n"
    "v_permlane32_swap_b32 v88, v89\n"
    "v_add_f32 v88, v88, v89\n"
    "v_permlane32_swap_b32 v90, v91\n"
    "v_add_f32 v90, v90, v91\n"
    "s_nop 1\n"
    "v_permlane16_swap_b32 v88, v90\n"
    "v_add_f32 v88, v88, v90\n"
    "s_nop 1\n"
    "v_add_f32_dpp v88, v88, v88 row_ror:8 row_mask:0xf bank_mask:0xf\n"
    "s_nop 1\n"
    "v_add_f32_dpp v88, v88, v88 row_ror:4 row_mask:0xf bank_mask:0xf\n"
    "s_nop 1\n"
    "v_add_f32_dpp v88, v88, v88 row_ror:2 row_mask:0xf bank_mask:0xf\n"
    "s_nop 1\n"
    "v_add_f32_dpp v88, v88, v88 row_ror:1 row_mask:0xf bank_mask:0xf\n"
    "ds_write_b32 v106, v88 offset:6144\n"
    "s_waitcnt lgkmcnt(0)\n"
    "s_barrier\n"
    "s_add_u32 s94, s94, 1\n"
    "s_add_u32 s95, s95, 1\n"
    "s_cmp_lg_u32 s30, 0\n"
    "s_cbranch_scc1 pub1\n"
    "v_and_b32 v82, 0x70, v224\n"
    "ds_read_b128 v[88:91], v82 offset:6144\n"
    "s_lshl_b32 s73, s36, 4\n"
    "s_add_u32 s73, s73, 4096\n"
    "s_cmp_ge_u32 s36, 128\n"
    "s_cselect_b32 s96, 1.0, 0\n"
    "v_lshrrev_b32 v83, 2, v224\n"
    "v_and_b32 v83, 16, v83\n"
    "s_waitcnt lgkmcnt(0)\n"
    "v_pk_add_f32 v[88:89], v[88:89], v[90:91]\n"
    "v_add_f32 v88, v88, v89\n"
    "v_cvt_pk_bf16_f32 v88, v88, v88\n"
    "v_lshlrev_b32 v88, 16, v88\n"
    "v_add_f32 v88, s96, v88\n"
    "v_mov_b32 v92, s94\n"
    "v_mov_b32 v94, s95\n"
    "v_mov_b32_dpp v89, v88 quad_perm:[1,1,3,3] row_mask:0xf bank_mask:0xf\n"
    "v_cvt_pk_bf16_f32 v93, v88, v89\n"
    "s_nop 1\n"
    "v_mov_b32_dpp v95, v93 quad_perm:[2,2,2,2] row_mask:0xf bank_mask:0xf\n"
    "s_mov_b64 exec, 1\n"
    "buffer_store_dwordx4 v[92:95], v83, s[60:63], s73 offen sc1\n"
    "s_mov_b64 exec, -1\n"
    "pub1:\n"
    "s_mov_b64 s[40:41], s[10:11]\n"
    "s_mov_b32 s42, 4194304\n"
    "s_mov_b32 s43, 0x20000\n"
    "v_lshlrev_b32 v108, 7, v253\n"
    "s_lshl_b32 s96, s3, 10\n"
    "v_and_b32 v107, 7, v254\n"
    "v_lshl_or_b32 v107, v107, 13, v108\n"
    "s_lshl_b32 s73, s81, 16\n"
    "s_add_u32 s73, s73, s96\n"
    "v_add_u32_e64 v107, s73, v107\n"
    "s_lshl_b32 s73, s30, 8\n"
    "s_add_u32 s73, s73, s96\n"
    "s_add_u32 s40, s40, s73\n"
    "s_addc_u32 s41, s41, 0\n"
    "s_lshl_b32 s72, s30, 14\n"
    "s_add_u32 s72, s72, 65536\n"
    "global_load_dword v218, v107, s[10:11]\n"
    "s_lshl_b32 s73, s3, 19\n"
    "s_add_u32 s12, s12, s73\n"
    "s_addc_u32 s13, s13, 0\n"
    "s_add_u32 s73, s73, 393216\n"
    "s_add_u32 s6, s6, s73\n"
    "s_addc_u32 s7, s7, 0\n"
    "s_add_u32 s8, s8, s73\n"
    "s_addc_u32 s9, s9, 0\n"
    "v_and_b32 v108, 15, v250\n"
    "v_lshlrev_b32 v108, 7, v108\n"
    "s_lshl_b32 s73, s81, 11\n"
    "v_add_u32 v217, s73, v108\n"
    "v_and_b32 v107, 3, v250\n"
    "v_lshlrev_b32 v107, 7, v107\n"
    "v_mul_u32_u24 v108, 6144, v102\n"
    "v_add_u32 v221, v107, v108\n"
    "s_mul_i32 s73, s39, 24576\n"
    "s_lshl_b32 s96, s82, 9\n"
    "s_add_u32 s73, s73, s96\n"
    "v_add_u32 v221, s73, v221\n"
    "s_mov_b64 s[44:45], s[6:7]\n"
    "s_mov_b32 s46, 131072\n"
    "s_mov_b32 s47, 0x20000\n"
    "s_mov_b64 s[48:49], s[8:9]\n"
    "s_mov_b32 s50, 131072\n"
    "s_mov_b32 s51, 0x20000\n"
    "s_nop 0\n"
    "v_mov_b32 v111, 0x3f803f80\n"
    "v_accvgpr_write_b32 a192, 0\n"
    "v_accvgpr_write_b32 a204, v111\n"
    "v_accvgpr_write_b32 a193, 0\n"
    "v_accvgpr_write_b32 a205, v111\n"
    "v_accvgpr_write_b32 a194, 0\n"
    "v_accvgpr_write_b32 a206, v111\n"
    "v_accvgpr_write_b32 a195, 0\n"
    "v_accvgpr_write_b32 a207, v111\n"
    "v_lshlrev_b32 v106, 11, v250\n"
    "v_min_u32 v106, k_end-k_begin+20416, v106\n"
    "s_lshr_b32 s97, s35, 2\n"
    "s_and_b32 s96, s39, 63\n"
    "s_mul_i32 s73, s96, 28672\n"
    "s_mul_i32 s90, s97, 1792\n"
    "s_add_u32 s73, s73, s90\n"
    "v_lshlrev_b32 v107, 8, v250\n"
    "v_add_u32 v107, s73, v107\n"
    "s_mul_i32 s96, s96, 7\n"
    "v_add_u32 v108, s96, v250\n"
    "v_and_b32 v111, 1, v108\n"
    "v_lshrrev_b32 v108, 1, v108\n"
    "v_lshlrev_b32 v111, 8, v111\n"
    "v_lshl_or_b32 v108, v108, 13, v111\n"
    "s_lshl_b32 s97, s97, 9\n"
    "v_add_u32 v108, s97, v108\n"
    "s_cmp_lt_u32 s39, 64\n"
    "s_cselect_b64 vcc, -1, 0\n"
    "s_cselect_b64 s[78:79], s[14:15], s[18:19]\n"
    "s_cselect_b32 s73, 7, 2\n"
    "s_lshl_b32 s73, s73, 9\n"
    "v_cndmask_b32 v107, v108, v107, vcc\n"
    "v_mov_b32 v248, s78\n"
    "v_mov_b32 v249, s79\n"
    "v_mov_b32 v111, s73\n"
    "v_mov_b32 v108, 0x100\n"
    "v_cmp_gt_u32 vcc, 7, v250\n"
    "s_nop 1\n"
    "v_cndmask_b32 v106, v106, v107, vcc\n"
    "v_cndmask_b32 v112, v108, v111, vcc\n"
    "s_lshl_b32 s73, s81, 13\n"
    "s_add_u32 s78, s12, s73\n"
    "s_addc_u32 s79, s13, 0\n"
    "s_sub_u32 s78, s78, 14336\n"
    "s_subb_u32 s79, s79, 0\n"
    "v_cmp_le_u32 vcc, 7, v250\n"
    "v_mov_b32 v107, s78\n"
    "v_mov_b32 v108, s79\n"
    "v_cndmask_b32 v248, v248, v107, vcc\n"
    "v_cndmask_b32 v249, v249, v108, vcc\n"
    "s_add_u32 s78, s92, 0\n"
    "s_addc_u32 s79, s93, 0\n"
    "s_sub_u32 s78, s78, 22528\n"
    "s_subb_u32 s79, s79, 0\n"
    "v_cmp_le_u32 vcc, 11, v250\n"
    "v_mov_b32 v107, s78\n"
    "v_mov_b32 v108, s79\n"
    "v_cndmask_b32 v248, v248, v107, vcc\n"
    "v_cndmask_b32 v249, v249, v108, vcc\n"
    "v_mov_b32 v107, 0\n"
    "v_lshl_add_u64 v[248:249], v[106:107], 0, v[248:249]\n"
    "v_accvgpr_write_b32 a255, v112\n"
    "v_mfma_f32_32x32x16_bf16 v[64:79], a[192:195], a[192:195], 0\n"
    "v_mfma_f32_32x32x16_bf16 v[80:95], a[192:195], a[192:195], 0\n"
    "v_mfma_f32_32x32x16_bf16 v[96:111], a[192:195], a[192:195], 0\n"
    "v_mfma_f32_32x32x16_bf16 v[112:127], a[192:195], a[192:195], 0\n"
    "s_lshl_b32 s73, s30, 8\n"
    "s_add_u32 s12, s12, s73\n"
    "s_addc_u32 s13, s13, 0\n"
    "s_mul_i32 s37, s35, 448\n"
    "s_add_u32 s37, s37, 64\n"
    "s_and_b32 s37, s37, -128\n"
    "s_lshl_b32 s73, s30, 7\n"
    "s_add_u32 s37, s37, s73\n"
    "s_and_b32 s73, s35, 1\n"
    "s_add_u32 s73, s73, s30\n"
    "s_cmp_eq_u32 s73, 4\n"
    "s_cselect_b32 s9, 0, 1835008\n"
    "s_mov_b64 s[56:57], s[18:19]\n"
    "s_mov_b32 s58, 1835008\n"
    "s_mul_i32 s10, s39, 14336\n"
    "s_mul_i32 s73, s30, 458752\n"
    "s_add_u32 s18, s18, s73\n"
    "s_addc_u32 s19, s19, 0\n"
    "s_lshl_b32 s73, s35, 7\n"
    "s_add_u32 s18, s18, s73\n"
    "s_addc_u32 s19, s19, 0\n"
    "s_lshl_b32 s73, s34, 16\n"
    "s_add_u32 s28, s28, s73\n"
    "s_addc_u32 s29, s29, 0\n"
    "s_add_u32 s4, s4, s73\n"
    "s_addc_u32 s5, s5, 0\n"
    "s_lshl_b32 s73, s35, 13\n"
    "s_add_u32 s4, s4, s73\n"
    "s_addc_u32 s5, s5, 0\n"
    "s_mov_b32 s73, 0x3f803f80\n"
    "s_waitcnt vmcnt(15)\n"
    "v_dot2_f32_bf16 v192, v0, s73, 0\n"
    "v_dot2_f32_bf16 v200, v0, v0, 0\n"
    "v_dot2_f32_bf16 v192, v1, s73, v192\n"
    "v_dot2_f32_bf16 v200, v1, v1, v200\n"
    "v_dot2_f32_bf16 v192, v2, s73, v192\n"
    "v_dot2_f32_bf16 v200, v2, v2, v200\n"
    "v_dot2_f32_bf16 v192, v3, s73, v192\n"
    "v_dot2_f32_bf16 v200, v3, v3, v200\n"
    "v_dot2_f32_bf16 v192, v4, s73, v192\n"
    "v_dot2_f32_bf16 v200, v4, v4, v200\n"
    "v_dot2_f32_bf16 v192, v5, s73, v192\n"
    "v_dot2_f32_bf16 v200, v5, v5, v200\n"
    "v_dot2_f32_bf16 v192, v6, s73, v192\n"
    "v_dot2_f32_bf16 v200, v6, v6, v200\n"
    "v_dot2_f32_bf16 v192, v7, s73, v192\n"
    "v_dot2_f32_bf16 v200, v7, v7, v200\n"
    "s_waitcnt vmcnt(13)\n"
    "s_nop 0\n"
    "v_dot2_f32_bf16 v193, v8, s73, 0\n"
    "v_dot2_f32_bf16 v201, v8, v8, 0\n"
    "v_dot2_f32_bf16 v193, v9, s73, v193\n"
    "v_dot2_f32_bf16 v201, v9, v9, v201\n"
    "v_dot2_f32_bf16 v193, v10, s73, v193\n"
    "v_dot2_f32_bf16 v201, v10, v10, v201\n"
    "v_dot2_f32_bf16 v193, v11, s73, v193\n"
    "v_dot2_f32_bf16 v201, v11, v11, v201\n"
    "v_dot2_f32_bf16 v193, v12, s73, v193\n"
    "v_dot2_f32_bf16 v201, v12, v12, v201\n"
    "v_dot2_f32_bf16 v193, v13, s73, v193\n"
    "v_dot2_f32_bf16 v201, v13, v13, v201\n"
    "v_dot2_f32_bf16 v193, v14, s73, v193\n"
    "v_dot2_f32_bf16 v201, v14, v14, v201\n"
    "v_dot2_f32_bf16 v193, v15, s73, v193\n"
    "v_dot2_f32_bf16 v201, v15, v15, v201\n"
    "s_waitcnt vmcnt(11)\n"
    "s_nop 0\n"
    "v_dot2_f32_bf16 v194, v16, s73, 0\n"
    "v_dot2_f32_bf16 v202, v16, v16, 0\n"
    "v_dot2_f32_bf16 v194, v17, s73, v194\n"
    "v_dot2_f32_bf16 v202, v17, v17, v202\n"
    "v_dot2_f32_bf16 v194, v18, s73, v194\n"
    "v_dot2_f32_bf16 v202, v18, v18, v202\n"
    "v_dot2_f32_bf16 v194, v19, s73, v194\n"
    "v_dot2_f32_bf16 v202, v19, v19, v202\n"
    "v_dot2_f32_bf16 v194, v20, s73, v194\n"
    "v_dot2_f32_bf16 v202, v20, v20, v202\n"
    "v_dot2_f32_bf16 v194, v21, s73, v194\n"
    "v_dot2_f32_bf16 v202, v21, v21, v202\n"
    "v_dot2_f32_bf16 v194, v22, s73, v194\n"
    "v_dot2_f32_bf16 v202, v22, v22, v202\n"
    "v_dot2_f32_bf16 v194, v23, s73, v194\n"
    "v_dot2_f32_bf16 v202, v23, v23, v202\n"
    "s_waitcnt vmcnt(9)\n"
    "s_nop 0\n"
    "v_dot2_f32_bf16 v195, v24, s73, 0\n"
    "v_dot2_f32_bf16 v203, v24, v24, 0\n"
    "v_dot2_f32_bf16 v195, v25, s73, v195\n"
    "v_dot2_f32_bf16 v203, v25, v25, v203\n"
    "v_dot2_f32_bf16 v195, v26, s73, v195\n"
    "v_dot2_f32_bf16 v203, v26, v26, v203\n"
    "v_dot2_f32_bf16 v195, v27, s73, v195\n"
    "v_dot2_f32_bf16 v203, v27, v27, v203\n"
    "v_dot2_f32_bf16 v195, v28, s73, v195\n"
    "v_dot2_f32_bf16 v203, v28, v28, v203\n"
    "v_dot2_f32_bf16 v195, v29, s73, v195\n"
    "v_dot2_f32_bf16 v203, v29, v29, v203\n"
    "v_dot2_f32_bf16 v195, v30, s73, v195\n"
    "v_dot2_f32_bf16 v203, v30, v30, v203\n"
    "v_dot2_f32_bf16 v195, v31, s73, v195\n"
    "v_dot2_f32_bf16 v203, v31, v31, v203\n"
    "s_waitcnt vmcnt(7)\n"
    "s_nop 0\n"
    "v_dot2_f32_bf16 v196, v32, s73, 0\n"
    "v_dot2_f32_bf16 v204, v32, v32, 0\n"
    "v_dot2_f32_bf16 v196, v33, s73, v196\n"
    "v_dot2_f32_bf16 v204, v33, v33, v204\n"
    "v_dot2_f32_bf16 v196, v34, s73, v196\n"
    "v_dot2_f32_bf16 v204, v34, v34, v204\n"
    "v_dot2_f32_bf16 v196, v35, s73, v196\n"
    "v_dot2_f32_bf16 v204, v35, v35, v204\n"
    "v_dot2_f32_bf16 v196, v36, s73, v196\n"
    "v_dot2_f32_bf16 v204, v36, v36, v204\n"
    "v_dot2_f32_bf16 v196, v37, s73, v196\n"
    "v_dot2_f32_bf16 v204, v37, v37, v204\n"
    "v_dot2_f32_bf16 v196, v38, s73, v196\n"
    "v_dot2_f32_bf16 v204, v38, v38, v204\n"
    "v_dot2_f32_bf16 v196, v39, s73, v196\n"
    "v_dot2_f32_bf16 v204, v39, v39, v204\n"
    "s_waitcnt vmcnt(5)\n"
    "s_nop 0\n"
    "v_dot2_f32_bf16 v197, v40, s73, 0\n"
    "v_dot2_f32_bf16 v205, v40, v40, 0\n"
    "v_dot2_f32_bf16 v197, v41, s73, v197\n"
    "v_dot2_f32_bf16 v205, v41, v41, v205\n"
    "v_dot2_f32_bf16 v197, v42, s73, v197\n"
    "v_dot2_f32_bf16 v205, v42, v42, v205\n"
    "v_dot2_f32_bf16 v197, v43, s73, v197\n"
    "v_dot2_f32_bf16 v205, v43, v43, v205\n"
    "v_dot2_f32_bf16 v197, v44, s73, v197\n"
    "v_dot2_f32_bf16 v205, v44, v44, v205\n"
    "v_dot2_f32_bf16 v197, v45, s73, v197\n"
    "v_dot2_f32_bf16 v205, v45, v45, v205\n"
    "v_dot2_f32_bf16 v197, v46, s73, v197\n"
    "v_dot2_f32_bf16 v205, v46, v46, v205\n"
    "v_dot2_f32_bf16 v197, v47, s73, v197\n"
    "v_dot2_f32_bf16 v205, v47, v47, v205\n"
    "s_waitcnt vmcnt(3)\n"
    "s_nop 0\n"
    "v_dot2_f32_bf16 v198, v48, s73, 0\n"
    "v_dot2_f32_bf16 v206, v48, v48, 0\n"
    "v_dot2_f32_bf16 v198, v49, s73, v198\n"
    "v_dot2_f32_bf16 v206, v49, v49, v206\n"
    "v_dot2_f32_bf16 v198, v50, s73, v198\n"
    "v_dot2_f32_bf16 v206, v50, v50, v206\n"
    "v_dot2_f32_bf16 v198, v51, s73, v198\n"
    "v_dot2_f32_bf16 v206, v51, v51, v206\n"
    "v_dot2_f32_bf16 v198, v52, s73, v198\n"
    "v_dot2_f32_bf16 v206, v52, v52, v206\n"
    "v_dot2_f32_bf16 v198, v53, s73, v198\n"
    "v_dot2_f32_bf16 v206, v53, v53, v206\n"
    "v_dot2_f32_bf16 v198, v54, s73, v198\n"
    "v_dot2_f32_bf16 v206, v54, v54, v206\n"
    "v_dot2_f32_bf16 v198, v55, s73, v198\n"
    "v_dot2_f32_bf16 v206, v55, v55, v206\n"
    "s_waitcnt vmcnt(1)\n"
    "s_nop 0\n"
    "v_dot2_f32_bf16 v199, v56, s73, 0\n"
    "v_dot2_f32_bf16 v207, v56, v56, 0\n"
    "v_dot2_f32_bf16 v199, v57, s73, v199\n"
    "v_dot2_f32_bf16 v207, v57, v57, v207\n"
    "v_dot2_f32_bf16 v199, v58, s73, v199\n"
    "v_dot2_f32_bf16 v207, v58, v58, v207\n"
    "v_dot2_f32_bf16 v199, v59, s73, v199\n"
    "v_dot2_f32_bf16 v207, v59, v59, v207\n"
    "v_dot2_f32_bf16 v199, v60, s73, v199\n"
    "v_dot2_f32_bf16 v207, v60, v60, v207\n"
    "v_dot2_f32_bf16 v199, v61, s73, v199\n"
    "v_dot2_f32_bf16 v207, v61, v61, v207\n"
    "v_dot2_f32_bf16 v199, v62, s73, v199\n"
    "v_dot2_f32_bf16 v207, v62, v62, v207\n"
    "v_dot2_f32_bf16 v199, v63, s73, v199\n"
    "v_dot2_f32_bf16 v207, v63, v63, v207\n"
    "s_nop 2\n"
    "v_permlane32_swap_b32 v192, v196\n"
    "v_permlane32_swap_b32 v193, v197\n"
    "v_permlane32_swap_b32 v194, v198\n"
    "v_permlane32_swap_b32 v195, v199\n"
    "v_permlane32_swap_b32 v200, v204\n"
    "v_permlane32_swap_b32 v201, v205\n"
    "v_permlane32_swap_b32 v202, v206\n"
    "v_permlane32_swap_b32 v203, v207\n"
    "v_add_f32 v192, v192, v196\n"
    "v_add_f32 v193, v193, v197\n"
    "v_add_f32 v194, v194, v198\n"
    "v_add_f32 v195, v195, v199\n"
    "v_add_f32 v200, v200, v204\n"
    "v_add_f32 v201, v201, v205\n"
    "v_add_f32 v202, v202, v206\n"
    "v_add_f32 v203, v203, v207\n"
    "s_nop 0\n"
    "v_permlane16_swap_b32 v192, v194\n"
    "v_permlane16_swap_b32 v193, v195\n"
    "v_permlane16_swap_b32 v200, v202\n"
    "v_permlane16_swap_b32 v201, v203\n"
    "v_add_f32 v192, v192, v194\n"
    "v_add_f32 v193, v193, v195\n"
    "v_add_f32 v200, v200, v202\n"
    "v_add_f32 v201, v201, v203\n"
    "v_lshrrev_b32_e64 v210, 1, v224\n"
    "s_lshl_b32 s73, s30, 9\n"
    "s_lshl_b32 s96, s30, 13\n"
    "v_add_f32_dpp v208, v192, v192 row_ror:8 row_mask:0xf bank_mask:0x3\n"
    "v_add_f32_dpp v209, v200, v200 row_ror:8 row_mask:0xf bank_mask:0x3\n"
    "v_add_u32 v211, s73, v210\n"
    "v_add_u32 v212, s96, v210\n"
    "v_add_f32_dpp v208, v193, v193 row_ror:8 row_mask:0xf bank_mask:0xc\n"
    "v_add_f32_dpp v209, v201, v201 row_ror:8 row_mask:0xf bank_mask:0xc\n"
    "ds_write_b64 v211, v[208:209] offset:0\n"
    "ds_write_b64 v211, v[208:209] offset:8192\n"
    "ds_write_b64 v211, v[208:209] offset:16384\n"
    "ds_write_b64 v211, v[208:209] offset:24576\n"
    "s_lshl_b32 s88, s30, 11\n"
    "s_add_u32 s88, s88, 131072\n"
    "s_add_u32 s89, s88, 16384\n"
    "s_waitcnt lgkmcnt(0)\n"
    "s_barrier\n"
    "ds_read_b64 v[192:193], v212 offset:0\n"
    "ds_read_b64 v[194:195], v212 offset:512\n"
    "ds_read_b64 v[196:197], v212 offset:1024\n"
    "ds_read_b64 v[198:199], v212 offset:1536\n"
    "s_mov_b32 s65, 128\n"
    "s_mov_b32 s66, 262016\n"
    "s_mov_b32 s64, 524288\n"
    "s_mov_b32 s86, 0\n"
    "s_mov_b32 s87, 0\n"
    "s_waitcnt lgkmcnt(0)\n"
    "v_pk_add_f32 v[192:193], v[192:193], v[194:195]\n"
    "v_pk_add_f32 v[196:197], v[196:197], v[198:199]\n"
    "v_pk_add_f32 v[192:193], v[192:193], v[196:197]\n"
    "v_mul_f32 v208, 0xbb000000, v192\n"
    "v_mul_f32 v209, 0x3b000000, v193\n"
    "v_fma_f32 v209, -v208, v208, v209\n"
    "v_max_f32 v209, 0, v209\n"
    "v_cvt_pk_bf16_f32 v209, v209, v209\n"
    "v_lshlrev_b32 v209, 16, v209\n"
    "v_add_f32 v209, 0x3727c5ac, v209\n"
    "v_cvt_pk_bf16_f32 v209, v209, v209\n"
    "v_lshlrev_b32 v209, 16, v209\n"
    "v_sqrt_f32 v209, v209\n"
    "v_cvt_pk_bf16_f32 v208, v208, v208\n"
    "v_lshlrev_b32 v208, 16, v208\n"
    "v_cvt_pk_bf16_f32 v209, v209, v209\n"
    "v_lshlrev_b32 v209, 16, v209\n"
    "v_rcp_f32 v209, v209\n"
    "v_and_b32 v211, 56, v210\n"
    "v_add_u32_e64 v211, s96, v211\n"
    "ds_write_b64 v212, v[208:209] offset:2048\n"
    "ds_read_b64 v[192:193], v211 offset:2048\n"
    "ds_read_b64 v[194:195], v211 offset:2112\n"
    "ds_read_b64 v[196:197], v211 offset:2176\n"
    "ds_read_b64 v[198:199], v211 offset:2240\n"
    "ds_read_b64 v[200:201], v211 offset:2304\n"
    "ds_read_b64 v[202:203], v211 offset:2368\n"
    "ds_read_b64 v[204:205], v211 offset:2432\n"
    "ds_read_b64 v[206:207], v211 offset:2496\n"
    "v_mfma_f32_32x32x16_bf16 v[128:143], a[192:195], a[192:195], 0\n"
    "v_mfma_f32_32x32x16_bf16 v[144:159], a[192:195], a[192:195], 0\n"
    "s_waitcnt lgkmcnt(0)\n"
    "s_nop 0\n"
    "v_dot2_f32_bf16 v208, v0, s98, v192\n"
    "v_dot2_f32_bf16 v209, v0, s99, v192\n"
    "v_dot2_f32_bf16 v210, v1, s98, v192\n"
    "v_dot2_f32_bf16 v211, v1, s99, v192\n"
    "v_dot2_f32_bf16 v212, v2, s98, v192\n"
    "v_dot2_f32_bf16 v213, v2, s99, v192\n"
    "v_dot2_f32_bf16 v214, v3, s98, v192\n"
    "v_dot2_f32_bf16 v215, v3, s99, v192\n"
    "buffer_load_dword v218, v217, s[44:47], 0 offen\n"
    "v_cvt_pk_bf16_f32 v208, 0, v208\n"
    "v_cvt_pk_bf16_f32 v209, 0, v209\n"
    "v_cvt_pk_bf16_f32 v210, 0, v210\n"
    "v_cvt_pk_bf16_f32 v211, 0, v211\n"
    "v_cvt_pk_bf16_f32 v212, 0, v212\n"
    "v_cvt_pk_bf16_f32 v213, 0, v213\n"
    "v_cvt_pk_bf16_f32 v214, 0, v214\n"
    "v_cvt_pk_bf16_f32 v215, 0, v215\n"
    "buffer_load_dword v218, v217, s[48:51], 0 offen\n"
    "v_pk_mul_f32 v[208:209], v[208:209], v[192:193] op_sel:[0,1]\n"
    "v_pk_mul_f32 v[210:211], v[210:211], v[192:193] op_sel:[0,1]\n"
    "v_pk_mul_f32 v[212:213], v[212:213], v[192:193] op_sel:[0,1]\n"
    "v_pk_mul_f32 v[214:215], v[214:215], v[192:193] op_sel:[0,1]\n"
    "v_cvt_pk_bf16_f32 v0, v208, v209\n"
    "v_cvt_pk_bf16_f32 v1, v210, v211\n"
    "v_cvt_pk_bf16_f32 v2, v212, v213\n"
    "v_cvt_pk_bf16_f32 v3, v214, v215\n"
    "v_dot2_f32_bf16 v208, v4, s98, v192\n"
    "v_dot2_f32_bf16 v209, v4, s99, v192\n"
    "v_dot2_f32_bf16 v210, v5, s98, v192\n"
    "v_dot2_f32_bf16 v211, v5, s99, v192\n"
    "v_dot2_f32_bf16 v212, v6, s98, v192\n"
    "v_dot2_f32_bf16 v213, v6, s99, v192\n"
    "v_dot2_f32_bf16 v214, v7, s98, v192\n"
    "v_dot2_f32_bf16 v215, v7, s99, v192\n"
    "s_add_u32 m0, s72, 0\n"
    "s_mov_b32 s73, 0\n"
    "buffer_load_dwordx4 v227, s[40:43], s73 offen offset:0 lds\n"
    "v_cvt_pk_bf16_f32 v208, 0, v208\n"
    "v_cvt_pk_bf16_f32 v209, 0, v209\n"
    "v_cvt_pk_bf16_f32 v210, 0, v210\n"
    "v_cvt_pk_bf16_f32 v211, 0, v211\n"
    "v_cvt_pk_bf16_f32 v212, 0, v212\n"
    "v_cvt_pk_bf16_f32 v213, 0, v213\n"
    "v_cvt_pk_bf16_f32 v214, 0, v214\n"
    "v_cvt_pk_bf16_f32 v215, 0, v215\n"
    "s_mov_b32 s73, 64512\n"
    "buffer_load_dwordx4 v220, s[40:43], s73 offen offset:1024 lds\n"
    "v_pk_mul_f32 v[208:209], v[208:209], v[192:193] op_sel:[0,1]\n"
    "v_pk_mul_f32 v[210:211], v[210:211], v[192:193] op_sel:[0,1]\n"
    "v_pk_mul_f32 v[212:213], v[212:213], v[192:193] op_sel:[0,1]\n"
    "v_pk_mul_f32 v[214:215], v[214:215], v[192:193] op_sel:[0,1]\n"
    "v_cvt_pk_bf16_f32 v4, v208, v209\n"
    "v_cvt_pk_bf16_f32 v5, v210, v211\n"
    "v_cvt_pk_bf16_f32 v6, v212, v213\n"
    "v_cvt_pk_bf16_f32 v7, v214, v215\n"
    "v_dot2_f32_bf16 v208, v8, s98, v194\n"
    "v_dot2_f32_bf16 v209, v8, s99, v194\n"
    "v_dot2_f32_bf16 v210, v9, s98, v194\n"
    "v_dot2_f32_bf16 v211, v9, s99, v194\n"
    "v_dot2_f32_bf16 v212, v10, s98, v194\n"
    "v_dot2_f32_bf16 v213, v10, s99, v194\n"
    "v_dot2_f32_bf16 v214, v11, s98, v194\n"
    "v_dot2_f32_bf16 v215, v11, s99, v194\n"
    "s_mov_b32 s73, 129024\n"
    "buffer_load_dwordx4 v227, s[40:43], s73 offen offset:2048 lds\n"
    "v_cvt_pk_bf16_f32 v208, 0, v208\n"
    "v_cvt_pk_bf16_f32 v209, 0, v209\n"
    "v_cvt_pk_bf16_f32 v210, 0, v210\n"
    "v_cvt_pk_bf16_f32 v211, 0, v211\n"
    "v_cvt_pk_bf16_f32 v212, 0, v212\n"
    "v_cvt_pk_bf16_f32 v213, 0, v213\n"
    "v_cvt_pk_bf16_f32 v214, 0, v214\n"
    "v_cvt_pk_bf16_f32 v215, 0, v215\n"
    "s_mov_b32 s73, 193536\n"
    "buffer_load_dwordx4 v220, s[40:43], s73 offen offset:3072 lds\n"
    "v_pk_mul_f32 v[208:209], v[208:209], v[194:195] op_sel:[0,1]\n"
    "v_pk_mul_f32 v[210:211], v[210:211], v[194:195] op_sel:[0,1]\n"
    "v_pk_mul_f32 v[212:213], v[212:213], v[194:195] op_sel:[0,1]\n"
    "v_pk_mul_f32 v[214:215], v[214:215], v[194:195] op_sel:[0,1]\n"
    "v_cvt_pk_bf16_f32 v8, v208, v209\n"
    "v_cvt_pk_bf16_f32 v9, v210, v211\n"
    "v_cvt_pk_bf16_f32 v10, v212, v213\n"
    "v_cvt_pk_bf16_f32 v11, v214, v215\n"
    "v_dot2_f32_bf16 v208, v12, s98, v194\n"
    "v_dot2_f32_bf16 v209, v12, s99, v194\n"
    "v_dot2_f32_bf16 v210, v13, s98, v194\n"
    "v_dot2_f32_bf16 v211, v13, s99, v194\n"
    "v_dot2_f32_bf16 v212, v14, s98, v194\n"
    "v_dot2_f32_bf16 v213, v14, s99, v194\n"
    "v_dot2_f32_bf16 v214, v15, s98, v194\n"
    "v_dot2_f32_bf16 v215, v15, s99, v194\n"
    "s_add_u32 m0, s72, 4096\n"
    "s_mov_b32 s73, 128\n"
    "buffer_load_dwordx4 v227, s[40:43], s73 offen offset:0 lds\n"
    "v_cvt_pk_bf16_f32 v208, 0, v208\n"
    "v_cvt_pk_bf16_f32 v209, 0, v209\n"
    "v_cvt_pk_bf16_f32 v210, 0, v210\n"
    "v_cvt_pk_bf16_f32 v211, 0, v211\n"
    "v_cvt_pk_bf16_f32 v212, 0, v212\n"
    "v_cvt_pk_bf16_f32 v213, 0, v213\n"
    "v_cvt_pk_bf16_f32 v214, 0, v214\n"
    "v_cvt_pk_bf16_f32 v215, 0, v215\n"
    "s_mov_b32 s73, 64640\n"
    "buffer_load_dwordx4 v220, s[40:43], s73 offen offset:1024 lds\n"
    "v_pk_mul_f32 v[208:209], v[208:209], v[194:195] op_sel:[0,1]\n"
    "v_pk_mul_f32 v[210:211], v[210:211], v[194:195] op_sel:[0,1]\n"
    "v_pk_mul_f32 v[212:213], v[212:213], v[194:195] op_sel:[0,1]\n"
    "v_pk_mul_f32 v[214:215], v[214:215], v[194:195] op_sel:[0,1]\n"
    "v_cvt_pk_bf16_f32 v12, v208, v209\n"
    "v_cvt_pk_bf16_f32 v13, v210, v211\n"
    "v_cvt_pk_bf16_f32 v14, v212, v213\n"
    "v_cvt_pk_bf16_f32 v15, v214, v215\n"
    "v_dot2_f32_bf16 v208, v16, s98, v196\n"
    "v_dot2_f32_bf16 v209, v16, s99, v196\n"
    "v_dot2_f32_bf16 v210, v17, s98, v196\n"
    "v_dot2_f32_bf16 v211, v17, s99, v196\n"
    "v_dot2_f32_bf16 v212, v18, s98, v196\n"
    "v_dot2_f32_bf16 v213, v18, s99, v196\n"
    "v_dot2_f32_bf16 v214, v19, s98, v196\n"
    "v_dot2_f32_bf16 v215, v19, s99, v196\n"
    "s_mov_b32 s73, 129152\n"
    "buffer_load_dwordx4 v227, s[40:43], s73 offen offset:2048 lds\n"
    "v_cvt_pk_bf16_f32 v208, 0, v208\n"
    "v_cvt_pk_bf16_f32 v209, 0, v209\n"
    "v_cvt_pk_bf16_f32 v210, 0, v210\n"
    "v_cvt_pk_bf16_f32 v211, 0, v211\n"
    "v_cvt_pk_bf16_f32 v212, 0, v212\n"
    "v_cvt_pk_bf16_f32 v213, 0, v213\n"
    "v_cvt_pk_bf16_f32 v214, 0, v214\n"
    "v_cvt_pk_bf16_f32 v215, 0, v215\n"
    "s_mov_b32 s73, 193664\n"
    "buffer_load_dwordx4 v220, s[40:43], s73 offen offset:3072 lds\n"
    "v_pk_mul_f32 v[208:209], v[208:209], v[196:197] op_sel:[0,1]\n"
    "v_pk_mul_f32 v[210:211], v[210:211], v[196:197] op_sel:[0,1]\n"
    "v_pk_mul_f32 v[212:213], v[212:213], v[196:197] op_sel:[0,1]\n"
    "v_pk_mul_f32 v[214:215], v[214:215], v[196:197] op_sel:[0,1]\n"
    "v_cvt_pk_bf16_f32 v16, v208, v209\n"
    "v_cvt_pk_bf16_f32 v17, v210, v211\n"
    "v_cvt_pk_bf16_f32 v18, v212, v213\n"
    "v_cvt_pk_bf16_f32 v19, v214, v215\n"
    "v_dot2_f32_bf16 v208, v20, s98, v196\n"
    "v_dot2_f32_bf16 v209, v20, s99, v196\n"
    "v_dot2_f32_bf16 v210, v21, s98, v196\n"
    "v_dot2_f32_bf16 v211, v21, s99, v196\n"
    "v_dot2_f32_bf16 v212, v22, s98, v196\n"
    "v_dot2_f32_bf16 v213, v22, s99, v196\n"
    "v_dot2_f32_bf16 v214, v23, s98, v196\n"
    "v_dot2_f32_bf16 v215, v23, s99, v196\n"
    "s_add_u32 m0, s72, 8192\n"
    "s_mov_b32 s73, 262144\n"
    "buffer_load_dwordx4 v227, s[40:43], s73 offen offset:0 lds\n"
    "v_cvt_pk_bf16_f32 v208, 0, v208\n"
    "v_cvt_pk_bf16_f32 v209, 0, v209\n"
    "v_cvt_pk_bf16_f32 v210, 0, v210\n"
    "v_cvt_pk_bf16_f32 v211, 0, v211\n"
    "v_cvt_pk_bf16_f32 v212, 0, v212\n"
    "v_cvt_pk_bf16_f32 v213, 0, v213\n"
    "v_cvt_pk_bf16_f32 v214, 0, v214\n"
    "v_cvt_pk_bf16_f32 v215, 0, v215\n"
    "s_mov_b32 s73, 326656\n"
    "buffer_load_dwordx4 v220, s[40:43], s73 offen offset:1024 lds\n"
    "v_pk_mul_f32 v[208:209], v[208:209], v[196:197] op_sel:[0,1]\n"
    "v_pk_mul_f32 v[210:211], v[210:211], v[196:197] op_sel:[0,1]\n"
    "v_pk_mul_f32 v[212:213], v[212:213], v[196:197] op_sel:[0,1]\n"
    "v_pk_mul_f32 v[214:215], v[214:215], v[196:197] op_sel:[0,1]\n"
    "v_cvt_pk_bf16_f32 v20, v208, v209\n"
    "v_cvt_pk_bf16_f32 v21, v210, v211\n"
    "v_cvt_pk_bf16_f32 v22, v212, v213\n"
    "v_cvt_pk_bf16_f32 v23, v214, v215\n"
    "v_dot2_f32_bf16 v208, v24, s98, v198\n"
    "v_dot2_f32_bf16 v209, v24, s99, v198\n"
    "v_dot2_f32_bf16 v210, v25, s98, v198\n"
    "v_dot2_f32_bf16 v211, v25, s99, v198\n"
    "v_dot2_f32_bf16 v212, v26, s98, v198\n"
    "v_dot2_f32_bf16 v213, v26, s99, v198\n"
    "v_dot2_f32_bf16 v214, v27, s98, v198\n"
    "v_dot2_f32_bf16 v215, v27, s99, v198\n"
    "s_mov_b32 s73, 391168\n"
    "buffer_load_dwordx4 v227, s[40:43], s73 offen offset:2048 lds\n"
    "v_cvt_pk_bf16_f32 v208, 0, v208\n"
    "v_cvt_pk_bf16_f32 v209, 0, v209\n"
    "v_cvt_pk_bf16_f32 v210, 0, v210\n"
    "v_cvt_pk_bf16_f32 v211, 0, v211\n"
    "v_cvt_pk_bf16_f32 v212, 0, v212\n"
    "v_cvt_pk_bf16_f32 v213, 0, v213\n"
    "v_cvt_pk_bf16_f32 v214, 0, v214\n"
    "v_cvt_pk_bf16_f32 v215, 0, v215\n"
    "s_mov_b32 s73, 455680\n"
    "buffer_load_dwordx4 v220, s[40:43], s73 offen offset:3072 lds\n"
    "v_pk_mul_f32 v[208:209], v[208:209], v[198:199] op_sel:[0,1]\n"
    "v_pk_mul_f32 v[210:211], v[210:211], v[198:199] op_sel:[0,1]\n"
    "v_pk_mul_f32 v[212:213], v[212:213], v[198:199] op_sel:[0,1]\n"
    "v_pk_mul_f32 v[214:215], v[214:215], v[198:199] op_sel:[0,1]\n"
    "v_cvt_pk_bf16_f32 v24, v208, v209\n"
    "v_cvt_pk_bf16_f32 v25, v210, v211\n"
    "v_cvt_pk_bf16_f32 v26, v212, v213\n"
    "v_cvt_pk_bf16_f32 v27, v214, v215\n"
    "v_dot2_f32_bf16 v208, v28, s98, v198\n"
    "v_dot2_f32_bf16 v209, v28, s99, v198\n"
    "v_dot2_f32_bf16 v210, v29, s98, v198\n"
    "v_dot2_f32_bf16 v211, v29, s99, v198\n"
    "v_dot2_f32_bf16 v212, v30, s98, v198\n"
    "v_dot2_f32_bf16 v213, v30, s99, v198\n"
    "v_dot2_f32_bf16 v214, v31, s98, v198\n"
    "v_dot2_f32_bf16 v215, v31, s99, v198\n"
    "s_add_u32 m0, s72, 12288\n"
    "s_mov_b32 s73, 262272\n"
    "buffer_load_dwordx4 v227, s[40:43], s73 offen offset:0 lds\n"
    "v_cvt_pk_bf16_f32 v208, 0, v208\n"
    "v_cvt_pk_bf16_f32 v209, 0, v209\n"
    "v_cvt_pk_bf16_f32 v210, 0, v210\n"
    "v_cvt_pk_bf16_f32 v211, 0, v211\n"
    "v_cvt_pk_bf16_f32 v212, 0, v212\n"
    "v_cvt_pk_bf16_f32 v213, 0, v213\n"
    "v_cvt_pk_bf16_f32 v214, 0, v214\n"
    "v_cvt_pk_bf16_f32 v215, 0, v215\n"
    "s_mov_b32 s73, 326784\n"
    "buffer_load_dwordx4 v220, s[40:43], s73 offen offset:1024 lds\n"
    "v_pk_mul_f32 v[208:209], v[208:209], v[198:199] op_sel:[0,1]\n"
    "v_pk_mul_f32 v[210:211], v[210:211], v[198:199] op_sel:[0,1]\n"
    "v_pk_mul_f32 v[212:213], v[212:213], v[198:199] op_sel:[0,1]\n"
    "v_pk_mul_f32 v[214:215], v[214:215], v[198:199] op_sel:[0,1]\n"
    "v_cvt_pk_bf16_f32 v28, v208, v209\n"
    "v_cvt_pk_bf16_f32 v29, v210, v211\n"
    "v_cvt_pk_bf16_f32 v30, v212, v213\n"
    "v_cvt_pk_bf16_f32 v31, v214, v215\n"
    "v_dot2_f32_bf16 v208, v32, s98, v200\n"
    "v_dot2_f32_bf16 v209, v32, s99, v200\n"
    "v_dot2_f32_bf16 v210, v33, s98, v200\n"
    "v_dot2_f32_bf16 v211, v33, s99, v200\n"
    "v_dot2_f32_bf16 v212, v34, s98, v200\n"
    "v_dot2_f32_bf16 v213, v34, s99, v200\n"
    "v_dot2_f32_bf16 v214, v35, s98, v200\n"
    "v_dot2_f32_bf16 v215, v35, s99, v200\n"
    "s_mov_b32 s73, 391296\n"
    "buffer_load_dwordx4 v227, s[40:43], s73 offen offset:2048 lds\n"
    "v_cvt_pk_bf16_f32 v208, 0, v208\n"
    "v_cvt_pk_bf16_f32 v209, 0, v209\n"
    "v_cvt_pk_bf16_f32 v210, 0, v210\n"
    "v_cvt_pk_bf16_f32 v211, 0, v211\n"
    "v_cvt_pk_bf16_f32 v212, 0, v212\n"
    "v_cvt_pk_bf16_f32 v213, 0, v213\n"
    "v_cvt_pk_bf16_f32 v214, 0, v214\n"
    "v_cvt_pk_bf16_f32 v215, 0, v215\n"
    "s_mov_b32 s73, 455808\n"
    "buffer_load_dwordx4 v220, s[40:43], s73 offen offset:3072 lds\n"
    "v_pk_mul_f32 v[208:209], v[208:209], v[200:201] op_sel:[0,1]\n"
    "v_pk_mul_f32 v[210:211], v[210:211], v[200:201] op_sel:[0,1]\n"
    "v_pk_mul_f32 v[212:213], v[212:213], v[200:201] op_sel:[0,1]\n"
    "v_pk_mul_f32 v[214:215], v[214:215], v[200:201] op_sel:[0,1]\n"
    "v_cvt_pk_bf16_f32 v32, v208, v209\n"
    "v_cvt_pk_bf16_f32 v33, v210, v211\n"
    "v_cvt_pk_bf16_f32 v34, v212, v213\n"
    "v_cvt_pk_bf16_f32 v35, v214, v215\n"
    "v_dot2_f32_bf16 v208, v36, s98, v200\n"
    "v_dot2_f32_bf16 v209, v36, s99, v200\n"
    "v_dot2_f32_bf16 v210, v37, s98, v200\n"
    "v_dot2_f32_bf16 v211, v37, s99, v200\n"
    "v_dot2_f32_bf16 v212, v38, s98, v200\n"
    "v_dot2_f32_bf16 v213, v38, s99, v200\n"
    "v_dot2_f32_bf16 v214, v39, s98, v200\n"
    "v_dot2_f32_bf16 v215, v39, s99, v200\n"
    "s_add_u32 m0, s88, 0\n"
    "s_nop 0\n"
    "buffer_load_dwordx4 v234, s[44:47], s86 offen lds\n"
    "v_cvt_pk_bf16_f32 v208, 0, v208\n"
    "v_cvt_pk_bf16_f32 v209, 0, v209\n"
    "v_cvt_pk_bf16_f32 v210, 0, v210\n"
    "v_cvt_pk_bf16_f32 v211, 0, v211\n"
    "v_cvt_pk_bf16_f32 v212, 0, v212\n"
    "v_cvt_pk_bf16_f32 v213, 0, v213\n"
    "v_cvt_pk_bf16_f32 v214, 0, v214\n"
    "v_cvt_pk_bf16_f32 v215, 0, v215\n"
    "buffer_load_dwordx4 v235, s[44:47], s86 offen offset:1024 lds\n"
    "s_add_u32 s86, s86, 8192\n"
    "v_pk_mul_f32 v[208:209], v[208:209], v[200:201] op_sel:[0,1]\n"
    "v_pk_mul_f32 v[210:211], v[210:211], v[200:201] op_sel:[0,1]\n"
    "v_pk_mul_f32 v[212:213], v[212:213], v[200:201] op_sel:[0,1]\n"
    "v_pk_mul_f32 v[214:215], v[214:215], v[200:201] op_sel:[0,1]\n"
    "v_cvt_pk_bf16_f32 v36, v208, v209\n"
    "v_cvt_pk_bf16_f32 v37, v210, v211\n"
    "v_cvt_pk_bf16_f32 v38, v212, v213\n"
    "v_cvt_pk_bf16_f32 v39, v214, v215\n"
    "v_dot2_f32_bf16 v208, v40, s98, v202\n"
    "v_dot2_f32_bf16 v209, v40, s99, v202\n"
    "v_dot2_f32_bf16 v210, v41, s98, v202\n"
    "v_dot2_f32_bf16 v211, v41, s99, v202\n"
    "v_dot2_f32_bf16 v212, v42, s98, v202\n"
    "v_dot2_f32_bf16 v213, v42, s99, v202\n"
    "v_dot2_f32_bf16 v214, v43, s98, v202\n"
    "v_dot2_f32_bf16 v215, v43, s99, v202\n"
    "s_add_u32 m0, s88, 8192\n"
    "s_nop 0\n"
    "buffer_load_dwordx4 v234, s[44:47], s86 offen lds\n"
    "s_nop 0\n"
    "v_cvt_pk_bf16_f32 v208, 0, v208\n"
    "v_cvt_pk_bf16_f32 v209, 0, v209\n"
    "v_cvt_pk_bf16_f32 v210, 0, v210\n"
    "v_cvt_pk_bf16_f32 v211, 0, v211\n"
    "v_cvt_pk_bf16_f32 v212, 0, v212\n"
    "v_cvt_pk_bf16_f32 v213, 0, v213\n"
    "v_cvt_pk_bf16_f32 v214, 0, v214\n"
    "v_cvt_pk_bf16_f32 v215, 0, v215\n"
    "buffer_load_dwordx4 v235, s[44:47], s86 offen offset:1024 lds\n"
    "s_add_u32 s86, s86, 8192\n"
    "v_pk_mul_f32 v[208:209], v[208:209], v[202:203] op_sel:[0,1]\n"
    "v_pk_mul_f32 v[210:211], v[210:211], v[202:203] op_sel:[0,1]\n"
    "v_pk_mul_f32 v[212:213], v[212:213], v[202:203] op_sel:[0,1]\n"
    "v_pk_mul_f32 v[214:215], v[214:215], v[202:203] op_sel:[0,1]\n"
    "v_cvt_pk_bf16_f32 v40, v208, v209\n"
    "v_cvt_pk_bf16_f32 v41, v210, v211\n"
    "v_cvt_pk_bf16_f32 v42, v212, v213\n"
    "v_cvt_pk_bf16_f32 v43, v214, v215\n"
    "v_dot2_f32_bf16 v208, v44, s98, v202\n"
    "v_dot2_f32_bf16 v209, v44, s99, v202\n"
    "v_dot2_f32_bf16 v210, v45, s98, v202\n"
    "v_dot2_f32_bf16 v211, v45, s99, v202\n"
    "v_dot2_f32_bf16 v212, v46, s98, v202\n"
    "v_dot2_f32_bf16 v213, v46, s99, v202\n"
    "v_dot2_f32_bf16 v214, v47, s98, v202\n"
    "v_dot2_f32_bf16 v215, v47, s99, v202\n"
    "s_add_u32 m0, s89, 0\n"
    "s_nop 0\n"
    "buffer_load_dwordx4 v236, s[48:51], s87 offen lds\n"
    "v_cvt_pk_bf16_f32 v208, 0, v208\n"
    "v_cvt_pk_bf16_f32 v209, 0, v209\n"
    "v_cvt_pk_bf16_f32 v210, 0, v210\n"
    "v_cvt_pk_bf16_f32 v211, 0, v211\n"
    "v_cvt_pk_bf16_f32 v212, 0, v212\n"
    "v_cvt_pk_bf16_f32 v213, 0, v213\n"
    "v_cvt_pk_bf16_f32 v214, 0, v214\n"
    "v_cvt_pk_bf16_f32 v215, 0, v215\n"
    "buffer_load_dwordx4 v233, s[48:51], s87 offen offset:1024 lds\n"
    "s_add_u32 s87, s87, 8192\n"
    "v_pk_mul_f32 v[208:209], v[208:209], v[202:203] op_sel:[0,1]\n"
    "v_pk_mul_f32 v[210:211], v[210:211], v[202:203] op_sel:[0,1]\n"
    "v_pk_mul_f32 v[212:213], v[212:213], v[202:203] op_sel:[0,1]\n"
    "v_pk_mul_f32 v[214:215], v[214:215], v[202:203] op_sel:[0,1]\n"
    "v_cvt_pk_bf16_f32 v44, v208, v209\n"
    "v_cvt_pk_bf16_f32 v45, v210, v211\n"
    "v_cvt_pk_bf16_f32 v46, v212, v213\n"
    "v_cvt_pk_bf16_f32 v47, v214, v215\n"
    "v_dot2_f32_bf16 v208, v48, s98, v204\n"
    "v_dot2_f32_bf16 v209, v48, s99, v204\n"
    "v_dot2_f32_bf16 v210, v49, s98, v204\n"
    "v_dot2_f32_bf16 v211, v49, s99, v204\n"
    "v_dot2_f32_bf16 v212, v50, s98, v204\n"
    "v_dot2_f32_bf16 v213, v50, s99, v204\n"
    "v_dot2_f32_bf16 v214, v51, s98, v204\n"
    "v_dot2_f32_bf16 v215, v51, s99, v204\n"
    "s_add_u32 m0, s89, 8192\n"
    "s_nop 0\n"
    "buffer_load_dwordx4 v236, s[48:51], s87 offen lds\n"
    "s_nop 0\n"
    "v_cvt_pk_bf16_f32 v208, 0, v208\n"
    "v_cvt_pk_bf16_f32 v209, 0, v209\n"
    "v_cvt_pk_bf16_f32 v210, 0, v210\n"
    "v_cvt_pk_bf16_f32 v211, 0, v211\n"
    "v_cvt_pk_bf16_f32 v212, 0, v212\n"
    "v_cvt_pk_bf16_f32 v213, 0, v213\n"
    "v_cvt_pk_bf16_f32 v214, 0, v214\n"
    "v_cvt_pk_bf16_f32 v215, 0, v215\n"
    "buffer_load_dwordx4 v233, s[48:51], s87 offen offset:1024 lds\n"
    "s_add_u32 s87, s87, 8192\n"
    "v_pk_mul_f32 v[208:209], v[208:209], v[204:205] op_sel:[0,1]\n"
    "v_pk_mul_f32 v[210:211], v[210:211], v[204:205] op_sel:[0,1]\n"
    "v_pk_mul_f32 v[212:213], v[212:213], v[204:205] op_sel:[0,1]\n"
    "v_pk_mul_f32 v[214:215], v[214:215], v[204:205] op_sel:[0,1]\n"
    "v_cvt_pk_bf16_f32 v48, v208, v209\n"
    "v_cvt_pk_bf16_f32 v49, v210, v211\n"
    "v_cvt_pk_bf16_f32 v50, v212, v213\n"
    "v_cvt_pk_bf16_f32 v51, v214, v215\n"
    "v_dot2_f32_bf16 v208, v52, s98, v204\n"
    "v_dot2_f32_bf16 v209, v52, s99, v204\n"
    "v_dot2_f32_bf16 v210, v53, s98, v204\n"
    "v_dot2_f32_bf16 v211, v53, s99, v204\n"
    "v_dot2_f32_bf16 v212, v54, s98, v204\n"
    "v_dot2_f32_bf16 v213, v54, s99, v204\n"
    "v_dot2_f32_bf16 v214, v55, s98, v204\n"
    "v_dot2_f32_bf16 v215, v55, s99, v204\n"
    "s_nop 0\n"
    "s_nop 0\n"
    "v_cvt_pk_bf16_f32 v208, 0, v208\n"
    "v_cvt_pk_bf16_f32 v209, 0, v209\n"
    "v_cvt_pk_bf16_f32 v210, 0, v210\n"
    "v_cvt_pk_bf16_f32 v211, 0, v211\n"
    "v_cvt_pk_bf16_f32 v212, 0, v212\n"
    "v_cvt_pk_bf16_f32 v213, 0, v213\n"
    "v_cvt_pk_bf16_f32 v214, 0, v214\n"
    "v_cvt_pk_bf16_f32 v215, 0, v215\n"
    "s_lshl_b32 s97, s30, 9\n"
    "s_add_u32 s97, s97, 4096\n"
    "buffer_load_dwordx4 v[192:195], v239, s[60:63], s97 offen sc1\n"
    "s_nop 0\n"
    "v_pk_mul_f32 v[208:209], v[208:209], v[204:205] op_sel:[0,1]\n"
    "v_pk_mul_f32 v[210:211], v[210:211], v[204:205] op_sel:[0,1]\n"
    "v_pk_mul_f32 v[212:213], v[212:213], v[204:205] op_sel:[0,1]\n"
    "v_pk_mul_f32 v[214:215], v[214:215], v[204:205] op_sel:[0,1]\n"
    "v_cvt_pk_bf16_f32 v52, v208, v209\n"
    "v_cvt_pk_bf16_f32 v53, v210, v211\n"
    "v_cvt_pk_bf16_f32 v54, v212, v213\n"
    "v_cvt_pk_bf16_f32 v55, v214, v215\n"
    "v_dot2_f32_bf16 v208, v56, s98, v206\n"
    "v_dot2_f32_bf16 v209, v56, s99, v206\n"
    "v_dot2_f32_bf16 v210, v57, s98, v206\n"
    "v_dot2_f32_bf16 v211, v57, s99, v206\n"
    "v_dot2_f32_bf16 v212, v58, s98, v206\n"
    "v_dot2_f32_bf16 v213, v58, s99, v206\n"
    "v_dot2_f32_bf16 v214, v59, s98, v206\n"
    "v_dot2_f32_bf16 v215, v59, s99, v206\n"
    "s_nop 0\n"
    "s_nop 0\n"
    "v_cvt_pk_bf16_f32 v208, 0, v208\n"
    "v_cvt_pk_bf16_f32 v209, 0, v209\n"
    "v_cvt_pk_bf16_f32 v210, 0, v210\n"
    "v_cvt_pk_bf16_f32 v211, 0, v211\n"
    "v_cvt_pk_bf16_f32 v212, 0, v212\n"
    "v_cvt_pk_bf16_f32 v213, 0, v213\n"
    "v_cvt_pk_bf16_f32 v214, 0, v214\n"
    "v_cvt_pk_bf16_f32 v215, 0, v215\n"
    "v_pk_mul_f32 v[208:209], v[208:209], v[206:207] op_sel:[0,1]\n"
    "v_pk_mul_f32 v[210:211], v[210:211], v[206:207] op_sel:[0,1]\n"
    "v_pk_mul_f32 v[212:213], v[212:213], v[206:207] op_sel:[0,1]\n"
    "v_pk_mul_f32 v[214:215], v[214:215], v[206:207] op_sel:[0,1]\n"
    "v_cvt_pk_bf16_f32 v56, v208, v209\n"
    "v_cvt_pk_bf16_f32 v57, v210, v211\n"
    "v_cvt_pk_bf16_f32 v58, v212, v213\n"
    "v_cvt_pk_bf16_f32 v59, v214, v215\n"
    "v_dot2_f32_bf16 v208, v60, s98, v206\n"
    "v_dot2_f32_bf16 v209, v60, s99, v206\n"
    "v_dot2_f32_bf16 v210, v61, s98, v206\n"
    "v_dot2_f32_bf16 v211, v61, s99, v206\n"
    "v_dot2_f32_bf16 v212, v62, s98, v206\n"
    "v_dot2_f32_bf16 v213, v62, s99, v206\n"
    "v_dot2_f32_bf16 v214, v63, s98, v206\n"
    "v_dot2_f32_bf16 v215, v63, s99, v206\n"
    "s_nop 0\n"
    "s_nop 0\n"
    "v_cvt_pk_bf16_f32 v208, 0, v208\n"
    "v_cvt_pk_bf16_f32 v209, 0, v209\n"
    "v_cvt_pk_bf16_f32 v210, 0, v210\n"
    "v_cvt_pk_bf16_f32 v211, 0, v211\n"
    "v_cvt_pk_bf16_f32 v212, 0, v212\n"
    "v_cvt_pk_bf16_f32 v213, 0, v213\n"
    "v_cvt_pk_bf16_f32 v214, 0, v214\n"
    "v_cvt_pk_bf16_f32 v215, 0, v215\n"
    "v_pk_mul_f32 v[208:209], v[208:209], v[206:207] op_sel:[0,1]\n"
    "v_pk_mul_f32 v[210:211], v[210:211], v[206:207] op_sel:[0,1]\n"
    "v_pk_mul_f32 v[212:213], v[212:213], v[206:207] op_sel:[0,1]\n"
    "v_pk_mul_f32 v[214:215], v[214:215], v[206:207] op_sel:[0,1]\n"
    "v_cvt_pk_bf16_f32 v60, v208, v209\n"
    "v_cvt_pk_bf16_f32 v61, v210, v211\n"
    "v_cvt_pk_bf16_f32 v62, v212, v213\n"
    "v_cvt_pk_bf16_f32 v63, v214, v215\n"
    "s_mov_b32 s90, 32768\n"
    "s_branch gp1_chk\n"
    "s_mov_b32 s90, 32768\n"
    "gp1:\n"
    "buffer_load_dwordx4 v[192:195], v239, s[60:63], s97 offen sc1\n"
    "gp1_chk:\n"
    "s_waitcnt vmcnt(0)\n"
    "v_xor_b32 v192, s94, v192\n"
    "v_xor_b32 v194, s95, v194\n"
    "v_or_b32 v220, v192, v194\n"
    "v_cmp_ne_u32 vcc, 0, v220\n"
    "s_cbranch_vccz gp1_ok\n"
    "s_sub_u32 s90, s90, 1\n"
    "s_cbranch_scc1 gp1_ok\n"
    "s_sleep 2\n"
    "s_branch gp1\n"
    "gp1_ok:\n"
    "v_lshrrev_b32 v210, 1, v224\n"
    "v_add_u32 v212, s96, v210\n"
    "v_mov_b32 v194, v193\n"
    "ds_write_b64 v212, v[194:195] offset:4096\n"
    "v_lshrrev_b32 v211, 3, v224\n"
    "v_and_b32 v211, 0x70, v211\n"
    "v_add_u32 v211, s96, v211\n"
    "ds_read_b128 v[192:195], v211 offset:4096\n"
    "ds_read_b128 v[200:203], v211 offset:4352\n"
    "ds_read_b128 v[196:199], v211 offset:4224\n"
    "ds_read_b128 v[204:207], v211 offset:4480\n"
    "v_add_u32 v216, s96, v226\n"
    "s_waitcnt lgkmcnt(0)\n"
    "v_and_b32 v160, 0xffff, v200\n"
    "v_and_b32 v161, s74, v200\n"
    "v_lshlrev_b32 v176, 16, v192\n"
    "v_and_b32 v177, s74, v192\n"
    "v_and_b32 v162, 0xffff, v201\n"
    "v_and_b32 v163, s74, v201\n"
    "v_lshlrev_b32 v178, 16, v193\n"
    "v_and_b32 v179, s74, v193\n"
    "v_and_b32 v164, 0xffff, v202\n"
    "v_and_b32 v165, s74, v202\n"
    "v_lshlrev_b32 v180, 16, v194\n"
    "v_and_b32 v181, s74, v194\n"
    "v_and_b32 v166, 0xffff, v203\n"
    "v_and_b32 v167, s74, v203\n"
    "v_lshlrev_b32 v182, 16, v195\n"
    "v_and_b32 v183, s74, v195\n"
    "v_and_b32 v168, 0xffff, v204\n"
    "v_and_b32 v169, s74, v204\n"
    "v_lshlrev_b32 v184, 16, v196\n"
    "v_and_b32 v185, s74, v196\n"
    "v_and_b32 v170, 0xffff, v205\n"
    "v_and_b32 v171, s74, v205\n"
    "v_lshlrev_b32 v186, 16, v197\n"
    "v_and_b32 v187, s74, v197\n"
    "v_and_b32 v172, 0xffff, v206\n"
    "v_and_b32 v173, s74, v206\n"
    "v_lshlrev_b32 v188, 16, v198\n"
    "v_and_b32 v189, s74, v198\n"
    "v_and_b32 v174, 0xffff, v207\n"
    "v_and_b32 v175, s74, v207\n"
    "v_lshlrev_b32 v190, 16, v199\n"
    "v_and_b32 v191, s74, v199\n"
    "v_dot2_f32_bf16 v208, v0, v160, 0\n"
    "v_dot2_f32_bf16 v209, v0, v161, 0\n"
    "v_dot2_f32_bf16 v210, v1, v162, 0\n"
    "v_dot2_f32_bf16 v211, v1, v163, 0\n"
    "v_dot2_f32_bf16 v212, v2, v164, 0\n"
    "v_dot2_f32_bf16 v213, v2, v165, 0\n"
    "v_dot2_f32_bf16 v214, v3, v166, 0\n"
    "v_dot2_f32_bf16 v215, v3, v167, 0\n"
    "global_load_dword v218, v217, s[12:13]\n"
    "v_cvt_pk_bf16_f32 v0, v208, v209\n"
    "v_cvt_pk_bf16_f32 v1, v210, v211\n"
    "v_cvt_pk_bf16_f32 v2, v212, v213\n"
    "v_cvt_pk_bf16_f32 v3, v214, v215\n"
    "v_dot2_f32_bf16 v208, v0, s98, v176\n"
    "v_dot2_f32_bf16 v209, v0, s99, v177\n"
    "v_dot2_f32_bf16 v210, v1, s98, v178\n"
    "v_dot2_f32_bf16 v211, v1, s99, v179\n"
    "v_dot2_f32_bf16 v212, v2, s98, v180\n"
    "v_dot2_f32_bf16 v213, v2, s99, v181\n"
    "v_dot2_f32_bf16 v214, v3, s98, v182\n"
    "v_dot2_f32_bf16 v215, v3, s99, v183\n"
    "s_nop 0\n"
    "v_cvt_pk_bf16_f32 v0, v208, v209\n"
    "v_cvt_pk_bf16_f32 v1, v210, v211\n"
    "v_cvt_pk_bf16_f32 v2, v212, v213\n"
    "v_cvt_pk_bf16_f32 v3, v214, v215\n"
    "ds_write_b128 v216, v[0:3] offset:0\n"
    "s_nop 0\n"
    "v_dot2_f32_bf16 v208, v4, v168, 0\n"
    "v_dot2_f32_bf16 v209, v4, v169, 0\n"
    "v_dot2_f32_bf16 v210, v5, v170, 0\n"
    "v_dot2_f32_bf16 v211, v5, v171, 0\n"
    "v_dot2_f32_bf16 v212, v6, v172, 0\n"
    "v_dot2_f32_bf16 v213, v6, v173, 0\n"
    "v_dot2_f32_bf16 v214, v7, v174, 0\n"
    "v_dot2_f32_bf16 v215, v7, v175, 0\n"
    "buffer_load_dword v218, v221, s[52:55], 0 offen offset:2048\n"
    "v_cvt_pk_bf16_f32 v4, v208, v209\n"
    "v_cvt_pk_bf16_f32 v5, v210, v211\n"
    "v_cvt_pk_bf16_f32 v6, v212, v213\n"
    "v_cvt_pk_bf16_f32 v7, v214, v215\n"
    "v_dot2_f32_bf16 v208, v4, s98, v184\n"
    "v_dot2_f32_bf16 v209, v4, s99, v185\n"
    "v_dot2_f32_bf16 v210, v5, s98, v186\n"
    "v_dot2_f32_bf16 v211, v5, s99, v187\n"
    "v_dot2_f32_bf16 v212, v6, s98, v188\n"
    "v_dot2_f32_bf16 v213, v6, s99, v189\n"
    "v_dot2_f32_bf16 v214, v7, s98, v190\n"
    "v_dot2_f32_bf16 v215, v7, s99, v191\n"
    "s_nop 0\n"
    "v_cvt_pk_bf16_f32 v4, v208, v209\n"
    "v_cvt_pk_bf16_f32 v5, v210, v211\n"
    "v_cvt_pk_bf16_f32 v6, v212, v213\n"
    "v_cvt_pk_bf16_f32 v7, v214, v215\n"
    "ds_write_b128 v216, v[4:7] offset:4096\n"
    "s_nop 0\n"
    "v_dot2_f32_bf16 v208, v8, v160, 0\n"
    "v_dot2_f32_bf16 v209, v8, v161, 0\n"
    "v_dot2_f32_bf16 v210, v9, v162, 0\n"
    "v_dot2_f32_bf16 v211, v9, v163, 0\n"
    "v_dot2_f32_bf16 v212, v10, v164, 0\n"
    "v_dot2_f32_bf16 v213, v10, v165, 0\n"
    "v_dot2_f32_bf16 v214, v11, v166, 0\n"
    "v_dot2_f32_bf16 v215, v11, v167, 0\n"
    "v_min_u32 v222, 14, v250\n"
    "v_lshlrev_b32 v222, 21, v222\n"
    "buffer_load_dword v218, v222, s[60:63], 0 offen offset:128\n"
    "v_mov_b32_e64 v222, 0\n"
    "v_cvt_pk_bf16_f32 v8, v208, v209\n"
    "v_cvt_pk_bf16_f32 v9, v210, v211\n"
    "v_cvt_pk_bf16_f32 v10, v212, v213\n"
    "v_cvt_pk_bf16_f32 v11, v214, v215\n"
    "v_dot2_f32_bf16 v208, v8, s98, v176\n"
    "v_dot2_f32_bf16 v209, v8, s99, v177\n"
    "v_dot2_f32_bf16 v210, v9, s98, v178\n"
    "v_dot2_f32_bf16 v211, v9, s99, v179\n"
    "v_dot2_f32_bf16 v212, v10, s98, v180\n"
    "v_dot2_f32_bf16 v213, v10, s99, v181\n"
    "v_dot2_f32_bf16 v214, v11, s98, v182\n"
    "v_dot2_f32_bf16 v215, v11, s99, v183\n"
    "s_nop 0\n"
    "v_cvt_pk_bf16_f32 v8, v208, v209\n"
    "v_cvt_pk_bf16_f32 v9, v210, v211\n"
    "v_cvt_pk_bf16_f32 v10, v212, v213\n"
    "v_cvt_pk_bf16_f32 v11, v214, v215\n"
    "ds_write_b128 v216, v[8:11] offset:128\n"
    "s_nop 0\n"
    "v_dot2_f32_bf16 v208, v12, v168, 0\n"
    "v_dot2_f32_bf16 v209, v12, v169, 0\n"
    "v_dot2_f32_bf16 v210, v13, v170, 0\n"
    "v_dot2_f32_bf16 v211, v13, v171, 0\n"
    "v_dot2_f32_bf16 v212, v14, v172, 0\n"
    "v_dot2_f32_bf16 v213, v14, v173, 0\n"
    "v_dot2_f32_bf16 v214, v15, v174, 0\n"
    "v_dot2_f32_bf16 v215, v15, v175, 0\n"
    "global_load_dword v218, v222, s[16:17]\n"
    "v_cvt_pk_bf16_f32 v12, v208, v209\n"
    "v_cvt_pk_bf16_f32 v13, v210, v211\n"
    "v_cvt_pk_bf16_f32 v14, v212, v213\n"
    "v_cvt_pk_bf16_f32 v15, v214, v215\n"
    "v_dot2_f32_bf16 v208, v12, s98, v184\n"
    "v_dot2_f32_bf16 v209, v12, s99, v185\n"
    "v_dot2_f32_bf16 v210, v13, s98, v186\n"
    "v_dot2_f32_bf16 v211, v13, s99, v187\n"
    "v_dot2_f32_bf16 v212, v14, s98, v188\n"
    "v_dot2_f32_bf16 v213, v14, s99, v189\n"
    "v_dot2_f32_bf16 v214, v15, s98, v190\n"
    "v_dot2_f32_bf16 v215, v15, s99, v191\n"
    "s_nop 0\n"
    "v_cvt_pk_bf16_f32 v12, v208, v209\n"
    "v_cvt_pk_bf16_f32 v13, v210, v211\n"
    "v_cvt_pk_bf16_f32 v14, v212, v213\n"
    "v_cvt_pk_bf16_f32 v15, v214, v215\n"
    "ds_write_b128 v216, v[12:15] offset:4224\n"
    "s_nop 0\n"
    "v_dot2_f32_bf16 v208, v16, v160, 0\n"
    "v_dot2_f32_bf16 v209, v16, v161, 0\n"
    "v_dot2_f32_bf16 v210, v17, v162, 0\n"
    "v_dot2_f32_bf16 v211, v17, v163, 0\n"
    "v_dot2_f32_bf16 v212, v18, v164, 0\n"
    "v_dot2_f32_bf16 v213, v18, v165, 0\n"
    "v_dot2_f32_bf16 v214, v19, v166, 0\n"
    "v_dot2_f32_bf16 v215, v19, v167, 0\n"
    "global_load_dword v218, v222, s[20:21]\n"
    "v_cvt_pk_bf16_f32 v16, v208, v209\n"
    "v_cvt_pk_bf16_f32 v17, v210, v211\n"
    "v_cvt_pk_bf16_f32 v18, v212, v213\n"
    "v_cvt_pk_bf16_f32 v19, v214, v215\n"
    "v_dot2_f32_bf16 v208, v16, s98, v176\n"
    "v_dot2_f32_bf16 v209, v16, s99, v177\n"
    "v_dot2_f32_bf16 v210, v17, s98, v178\n"
    "v_dot2_f32_bf16 v211, v17, s99, v179\n"
    "v_dot2_f32_bf16 v212, v18, s98, v180\n"
    "v_dot2_f32_bf16 v213, v18, s99, v181\n"
    "v_dot2_f32_bf16 v214, v19, s98, v182\n"
    "v_dot2_f32_bf16 v215, v19, s99, v183\n"
    "s_nop 0\n"
    "v_cvt_pk_bf16_f32 v16, v208, v209\n"
    "v_cvt_pk_bf16_f32 v17, v210, v211\n"
    "v_cvt_pk_bf16_f32 v18, v212, v213\n"
    "v_cvt_pk_bf16_f32 v19, v214, v215\n"
    "ds_write_b128 v216, v[16:19] offset:256\n"
    "s_nop 0\n"
    "v_dot2_f32_bf16 v208, v20, v168, 0\n"
    "v_dot2_f32_bf16 v209, v20, v169, 0\n"
    "v_dot2_f32_bf16 v210, v21, v170, 0\n"
    "v_dot2_f32_bf16 v211, v21, v171, 0\n"
    "v_dot2_f32_bf16 v212, v22, v172, 0\n"
    "v_dot2_f32_bf16 v213, v22, v173, 0\n"
    "v_dot2_f32_bf16 v214, v23, v174, 0\n"
    "v_dot2_f32_bf16 v215, v23, v175, 0\n"
    "global_load_dword v218, v222, s[28:29]\n"
    "v_cvt_pk_bf16_f32 v20, v208, v209\n"
    "v_cvt_pk_bf16_f32 v21, v210, v211\n"
    "v_cvt_pk_bf16_f32 v22, v212, v213\n"
    "v_cvt_pk_bf16_f32 v23, v214, v215\n"
    "v_dot2_f32_bf16 v208, v20, s98, v184\n"
    "v_dot2_f32_bf16 v209, v20, s99, v185\n"
    "v_dot2_f32_bf16 v210, v21, s98, v186\n"
    "v_dot2_f32_bf16 v211, v21, s99, v187\n"
    "v_dot2_f32_bf16 v212, v22, s98, v188\n"
    "v_dot2_f32_bf16 v213, v22, s99, v189\n"
    "v_dot2_f32_bf16 v214, v23, s98, v190\n"
    "v_dot2_f32_bf16 v215, v23, s99, v191\n"
    "s_nop 0\n"
    "v_cvt_pk_bf16_f32 v20, v208, v209\n"
    "v_cvt_pk_bf16_f32 v21, v210, v211\n"
    "v_cvt_pk_bf16_f32 v22, v212, v213\n"
    "v_cvt_pk_bf16_f32 v23, v214, v215\n"
    "ds_write_b128 v216, v[20:23] offset:4352\n"
    "s_nop 0\n"
    "v_dot2_f32_bf16 v208, v24, v160, 0\n"
    "v_dot2_f32_bf16 v209, v24, v161, 0\n"
    "v_dot2_f32_bf16 v210, v25, v162, 0\n"
    "v_dot2_f32_bf16 v211, v25, v163, 0\n"
    "v_dot2_f32_bf16 v212, v26, v164, 0\n"
    "v_dot2_f32_bf16 v213, v26, v165, 0\n"
    "v_dot2_f32_bf16 v214, v27, v166, 0\n"
    "v_dot2_f32_bf16 v215, v27, v167, 0\n"
    "s_nop 0\n"
    "s_nop 0\n"
    "v_cvt_pk_bf16_f32 v24, v208, v209\n"
    "v_cvt_pk_bf16_f32 v25, v210, v211\n"
    "v_cvt_pk_bf16_f32 v26, v212, v213\n"
    "v_cvt_pk_bf16_f32 v27, v214, v215\n"
    "v_dot2_f32_bf16 v208, v24, s98, v176\n"
    "v_dot2_f32_bf16 v209, v24, s99, v177\n"
    "v_dot2_f32_bf16 v210, v25, s98, v178\n"
    "v_dot2_f32_bf16 v211, v25, s99, v179\n"
    "v_dot2_f32_bf16 v212, v26, s98, v180\n"
    "v_dot2_f32_bf16 v213, v26, s99, v181\n"
    "v_dot2_f32_bf16 v214, v27, s98, v182\n"
    "v_dot2_f32_bf16 v215, v27, s99, v183\n"
    "s_nop 0\n"
    "v_cvt_pk_bf16_f32 v24, v208, v209\n"
    "v_cvt_pk_bf16_f32 v25, v210, v211\n"
    "v_cvt_pk_bf16_f32 v26, v212, v213\n"
    "v_cvt_pk_bf16_f32 v27, v214, v215\n"
    "ds_write_b128 v216, v[24:27] offset:384\n"
    "s_nop 0\n"
    "v_dot2_f32_bf16 v208, v28, v168, 0\n"
    "v_dot2_f32_bf16 v209, v28, v169, 0\n"
    "v_dot2_f32_bf16 v210, v29, v170, 0\n"
    "v_dot2_f32_bf16 v211, v29, v171, 0\n"
    "v_dot2_f32_bf16 v212, v30, v172, 0\n"
    "v_dot2_f32_bf16 v213, v30, v173, 0\n"
    "v_dot2_f32_bf16 v214, v31, v174, 0\n"
    "v_dot2_f32_bf16 v215, v31, v175, 0\n"
    "s_nop 0\n"
    "s_nop 0\n"
    "v_cvt_pk_bf16_f32 v28, v208, v209\n"
    "v_cvt_pk_bf16_f32 v29, v210, v211\n"
    "v_cvt_pk_bf16_f32 v30, v212, v213\n"
    "v_cvt_pk_bf16_f32 v31, v214, v215\n"
    "v_dot2_f32_bf16 v208, v28, s98, v184\n"
    "v_dot2_f32_bf16 v209, v28, s99, v185\n"
    "v_dot2_f32_bf16 v210, v29, s98, v186\n"
    "v_dot2_f32_bf16 v211, v29, s99, v187\n"
    "v_dot2_f32_bf16 v212, v30, s98, v188\n"
    "v_dot2_f32_bf16 v213, v30, s99, v189\n"
    "v_dot2_f32_bf16 v214, v31, s98, v190\n"
    "v_dot2_f32_bf16 v215, v31, s99, v191\n"
    "s_nop 0\n"
    "v_cvt_pk_bf16_f32 v28, v208, v209\n"
    "v_cvt_pk_bf16_f32 v29, v210, v211\n"
    "v_cvt_pk_bf16_f32 v30, v212, v213\n"
    "v_cvt_pk_bf16_f32 v31, v214, v215\n"
    "ds_write_b128 v216, v[28:31] offset:4480\n"
    "s_nop 0\n"
    "v_dot2_f32_bf16 v208, v32, v160, 0\n"
    "v_dot2_f32_bf16 v209, v32, v161, 0\n"
    "v_dot2_f32_bf16 v210, v33, v162, 0\n"
    "v_dot2_f32_bf16 v211, v33, v163, 0\n"
    "v_dot2_f32_bf16 v212, v34, v164, 0\n"
    "v_dot2_f32_bf16 v213, v34, v165, 0\n"
    "v_dot2_f32_bf16 v214, v35, v166, 0\n"
    "v_dot2_f32_bf16 v215, v35, v167, 0\n"
    "s_nop 0\n"
    "s_nop 0\n"
    "v_cvt_pk_bf16_f32 v32, v208, v209\n"
    "v_cvt_pk_bf16_f32 v33, v210, v211\n"
    "v_cvt_pk_bf16_f32 v34, v212, v213\n"
    "v_cvt_pk_bf16_f32 v35, v214, v215\n"
    "v_dot2_f32_bf16 v208, v32, s98, v176\n"
    "v_dot2_f32_bf16 v209, v32, s99, v177\n"
    "v_dot2_f32_bf16 v210, v33, s98, v178\n"
    "v_dot2_f32_bf16 v211, v33, s99, v179\n"
    "v_dot2_f32_bf16 v212, v34, s98, v180\n"
    "v_dot2_f32_bf16 v213, v34, s99, v181\n"
    "v_dot2_f32_bf16 v214, v35, s98, v182\n"
    "v_dot2_f32_bf16 v215, v35, s99, v183\n"
    "s_nop 0\n"
    "v_cvt_pk_bf16_f32 v32, v208, v209\n"
    "v_cvt_pk_bf16_f32 v33, v210, v211\n"
    "v_cvt_pk_bf16_f32 v34, v212, v213\n"
    "v_cvt_pk_bf16_f32 v35, v214, v215\n"
    "ds_write_b128 v216, v[32:35] offset:32768\n"
    "s_nop 0\n"
    "v_dot2_f32_bf16 v208, v36, v168, 0\n"
    "v_dot2_f32_bf16 v209, v36, v169, 0\n"
    "v_dot2_f32_bf16 v210, v37, v170, 0\n"
    "v_dot2_f32_bf16 v211, v37, v171, 0\n"
    "v_dot2_f32_bf16 v212, v38, v172, 0\n"
    "v_dot2_f32_bf16 v213, v38, v173, 0\n"
    "v_dot2_f32_bf16 v214, v39, v174, 0\n"
    "v_dot2_f32_bf16 v215, v39, v175, 0\n"
    "s_nop 0\n"
    "s_nop 0\n"
    "v_cvt_pk_bf16_f32 v36, v208, v209\n"
    "v_cvt_pk_bf16_f32 v37, v210, v211\n"
    "v_cvt_pk_bf16_f32 v38, v212, v213\n"
    "v_cvt_pk_bf16_f32 v39, v214, v215\n"
    "v_dot2_f32_bf16 v208, v36, s98, v184\n"
    "v_dot2_f32_bf16 v209, v36, s99, v185\n"
    "v_dot2_f32_bf16 v210, v37, s98, v186\n"
    "v_dot2_f32_bf16 v211, v37, s99, v187\n"
    "v_dot2_f32_bf16 v212, v38, s98, v188\n"
    "v_dot2_f32_bf16 v213, v38, s99, v189\n"
    "v_dot2_f32_bf16 v214, v39, s98, v190\n"
    "v_dot2_f32_bf16 v215, v39, s99, v191\n"
    "s_nop 0\n"
    "v_cvt_pk_bf16_f32 v36, v208, v209\n"
    "v_cvt_pk_bf16_f32 v37, v210, v211\n"
    "v_cvt_pk_bf16_f32 v38, v212, v213\n"
    "v_cvt_pk_bf16_f32 v39, v214, v215\n"
    "ds_write_b128 v216, v[36:39] offset:36864\n"
    "s_nop 0\n"
    "v_dot2_f32_bf16 v208, v40, v160, 0\n"
    "v_dot2_f32_bf16 v209, v40, v161, 0\n"
    "v_dot2_f32_bf16 v210, v41, v162, 0\n"
    "v_dot2_f32_bf16 v211, v41, v163, 0\n"
    "v_dot2_f32_bf16 v212, v42, v164, 0\n"
    "v_dot2_f32_bf16 v213, v42, v165, 0\n"
    "v_dot2_f32_bf16 v214, v43, v166, 0\n"
    "v_dot2_f32_bf16 v215, v43, v167, 0\n"
    "s_nop 0\n"
    "s_nop 0\n"
    "v_cvt_pk_bf16_f32 v40, v208, v209\n"
    "v_cvt_pk_bf16_f32 v41, v210, v211\n"
    "v_cvt_pk_bf16_f32 v42, v212, v213\n"
    "v_cvt_pk_bf16_f32 v43, v214, v215\n"
    "v_dot2_f32_bf16 v208, v40, s98, v176\n"
    "v_dot2_f32_bf16 v209, v40, s99, v177\n"
    "v_dot2_f32_bf16 v210, v41, s98, v178\n"
    "v_dot2_f32_bf16 v211, v41, s99, v179\n"
    "v_dot2_f32_bf16 v212, v42, s98, v180\n"
    "v_dot2_f32_bf16 v213, v42, s99, v181\n"
    "v_dot2_f32_bf16 v214, v43, s98, v182\n"
    "v_dot2_f32_bf16 v215, v43, s99, v183\n"
    "s_nop 0\n"
    "v_cvt_pk_bf16_f32 v40, v208, v209\n"
    "v_cvt_pk_bf16_f32 v41, v210, v211\n"
    "v_cvt_pk_bf16_f32 v42, v212, v213\n"
    "v_cvt_pk_bf16_f32 v43, v214, v215\n"
    "ds_write_b128 v216, v[40:43] offset:32896\n"
    "s_nop 0\n"
    "v_dot2_f32_bf16 v208, v44, v168, 0\n"
    "v_dot2_f32_bf16 v209, v44, v169, 0\n"
    "v_dot2_f32_bf16 v210, v45, v170, 0\n"
    "v_dot2_f32_bf16 v211, v45, v171, 0\n"
    "v_dot2_f32_bf16 v212, v46, v172, 0\n"
    "v_dot2_f32_bf16 v213, v46, v173, 0\n"
    "v_dot2_f32_bf16 v214, v47, v174, 0\n"
    "v_dot2_f32_bf16 v215, v47, v175, 0\n"
    "s_nop 0\n"
    "s_nop 0\n"
    "v_cvt_pk_bf16_f32 v44, v208, v209\n"
    "v_cvt_pk_bf16_f32 v45, v210, v211\n"
    "v_cvt_pk_bf16_f32 v46, v212, v213\n"
    "v_cvt_pk_bf16_f32 v47, v214, v215\n"
    "v_dot2_f32_bf16 v208, v44, s98, v184\n"
    "v_dot2_f32_bf16 v209, v44, s99, v185\n"
    "v_dot2_f32_bf16 v210, v45, s98, v186\n"
    "v_dot2_f32_bf16 v211, v45, s99, v187\n"
    "v_dot2_f32_bf16 v212, v46, s98, v188\n"
    "v_dot2_f32_bf16 v213, v46, s99, v189\n"
    "v_dot2_f32_bf16 v214, v47, s98, v190\n"
    "v_dot2_f32_bf16 v215, v47, s99, v191\n"
    "s_nop 0\n"
    "v_cvt_pk_bf16_f32 v44, v208, v209\n"
    "v_cvt_pk_bf16_f32 v45, v210, v211\n"
    "v_cvt_pk_bf16_f32 v46, v212, v213\n"
    "v_cvt_pk_bf16_f32 v47, v214, v215\n"
    "ds_write_b128 v216, v[44:47] offset:36992\n"
    "s_nop 0\n"
    "v_dot2_f32_bf16 v208, v48, v160, 0\n"
    "v_dot2_f32_bf16 v209, v48, v161, 0\n"
    "v_dot2_f32_bf16 v210, v49, v162, 0\n"
    "v_dot2_f32_bf16 v211, v49, v163, 0\n"
    "v_dot2_f32_bf16 v212, v50, v164, 0\n"
    "v_dot2_f32_bf16 v213, v50, v165, 0\n"
    "v_dot2_f32_bf16 v214, v51, v166, 0\n"
    "v_dot2_f32_bf16 v215, v51, v167, 0\n"
    "s_nop 0\n"
    "s_nop 0\n"
    "v_cvt_pk_bf16_f32 v48, v208, v209\n"
    "v_cvt_pk_bf16_f32 v49, v210, v211\n"
    "v_cvt_pk_bf16_f32 v50, v212, v213\n"
    "v_cvt_pk_bf16_f32 v51, v214, v215\n"
    "v_dot2_f32_bf16 v208, v48, s98, v176\n"
    "v_dot2_f32_bf16 v209, v48, s99, v177\n"
    "v_dot2_f32_bf16 v210, v49, s98, v178\n"
    "v_dot2_f32_bf16 v211, v49, s99, v179\n"
    "v_dot2_f32_bf16 v212, v50, s98, v180\n"
    "v_dot2_f32_bf16 v213, v50, s99, v181\n"
    "v_dot2_f32_bf16 v214, v51, s98, v182\n"
    "v_dot2_f32_bf16 v215, v51, s99, v183\n"
    "s_nop 0\n"
    "v_cvt_pk_bf16_f32 v48, v208, v209\n"
    "v_cvt_pk_bf16_f32 v49, v210, v211\n"
    "v_cvt_pk_bf16_f32 v50, v212, v213\n"
    "v_cvt_pk_bf16_f32 v51, v214, v215\n"
    "ds_write_b128 v216, v[48:51] offset:33024\n"
    "s_nop 0\n"
    "v_dot2_f32_bf16 v208, v52, v168, 0\n"
    "v_dot2_f32_bf16 v209, v52, v169, 0\n"
    "v_dot2_f32_bf16 v210, v53, v170, 0\n"
    "v_dot2_f32_bf16 v211, v53, v171, 0\n"
    "v_dot2_f32_bf16 v212, v54, v172, 0\n"
    "v_dot2_f32_bf16 v213, v54, v173, 0\n"
    "v_dot2_f32_bf16 v214, v55, v174, 0\n"
    "v_dot2_f32_bf16 v215, v55, v175, 0\n"
    "s_nop 0\n"
    "s_nop 0\n"
    "v_cvt_pk_bf16_f32 v52, v208, v209\n"
    "v_cvt_pk_bf16_f32 v53, v210, v211\n"
    "v_cvt_pk_bf16_f32 v54, v212, v213\n"
    "v_cvt_pk_bf16_f32 v55, v214, v215\n"
    "v_dot2_f32_bf16 v208, v52, s98, v184\n"
    "v_dot2_f32_bf16 v209, v52, s99, v185\n"
    "v_dot2_f32_bf16 v210, v53, s98, v186\n"
    "v_dot2_f32_bf16 v211, v53, s99, v187\n"
    "v_dot2_f32_bf16 v212, v54, s98, v188\n"
    "v_dot2_f32_bf16 v213, v54, s99, v189\n"
    "v_dot2_f32_bf16 v214, v55, s98, v190\n"
    "v_dot2_f32_bf16 v215, v55, s99, v191\n"
    "s_nop 0\n"
    "v_cvt_pk_bf16_f32 v52, v208, v209\n"
    "v_cvt_pk_bf16_f32 v53, v210, v211\n"
    "v_cvt_pk_bf16_f32 v54, v212, v213\n"
    "v_cvt_pk_bf16_f32 v55, v214, v215\n"
    "ds_write_b128 v216, v[52:55] offset:37120\n"
    "s_nop 0\n"
    "v_dot2_f32_bf16 v208, v56, v160, 0\n"
    "v_dot2_f32_bf16 v209, v56, v161, 0\n"
    "v_dot2_f32_bf16 v210, v57, v162, 0\n"
    "v_dot2_f32_bf16 v211, v57, v163, 0\n"
    "v_dot2_f32_bf16 v212, v58, v164, 0\n"
    "v_dot2_f32_bf16 v213, v58, v165, 0\n"
    "v_dot2_f32_bf16 v214, v59, v166, 0\n"
    "v_dot2_f32_bf16 v215, v59, v167, 0\n"
    "s_nop 0\n"
    "s_nop 0\n"
    "v_cvt_pk_bf16_f32 v56, v208, v209\n"
    "v_cvt_pk_bf16_f32 v57, v210, v211\n"
    "v_cvt_pk_bf16_f32 v58, v212, v213\n"
    "v_cvt_pk_bf16_f32 v59, v214, v215\n"
    "v_dot2_f32_bf16 v208, v56, s98, v176\n"
    "v_dot2_f32_bf16 v209, v56, s99, v177\n"
    "v_dot2_f32_bf16 v210, v57, s98, v178\n"
    "v_dot2_f32_bf16 v211, v57, s99, v179\n"
    "v_dot2_f32_bf16 v212, v58, s98, v180\n"
    "v_dot2_f32_bf16 v213, v58, s99, v181\n"
    "v_dot2_f32_bf16 v214, v59, s98, v182\n"
    "v_dot2_f32_bf16 v215, v59, s99, v183\n"
    "s_nop 0\n"
    "v_cvt_pk_bf16_f32 v56, v208, v209\n"
    "v_cvt_pk_bf16_f32 v57, v210, v211\n"
    "v_cvt_pk_bf16_f32 v58, v212, v213\n"
    "v_cvt_pk_bf16_f32 v59, v214, v215\n"
    "ds_write_b128 v216, v[56:59] offset:33152\n"
    "s_nop 0\n"
    "v_dot2_f32_bf16 v208, v60, v168, 0\n"
    "v_dot2_f32_bf16 v209, v60, v169, 0\n"
    "v_dot2_f32_bf16 v210, v61, v170, 0\n"
    "v_dot2_f32_bf16 v211, v61, v171, 0\n"
    "v_dot2_f32_bf16 v212, v62, v172, 0\n"
    "v_dot2_f32_bf16 v213, v62, v173, 0\n"
    "v_dot2_f32_bf16 v214, v63, v174, 0\n"
    "v_dot2_f32_bf16 v215, v63, v175, 0\n"
    "s_nop 0\n"
    "s_nop 0\n"
    "v_cvt_pk_bf16_f32 v60, v208, v209\n"
    "v_cvt_pk_bf16_f32 v61, v210, v211\n"
    "v_cvt_pk_bf16_f32 v62, v212, v213\n"
    "v_cvt_pk_bf16_f32 v63, v214, v215\n"
    "v_dot2_f32_bf16 v208, v60, s98, v184\n"
    "v_dot2_f32_bf16 v209, v60, s99, v185\n"
    "v_dot2_f32_bf16 v210, v61, s98, v186\n"
    "v_dot2_f32_bf16 v211, v61, s99, v187\n"
    "v_dot2_f32_bf16 v212, v62, s98, v188\n"
    "v_dot2_f32_bf16 v213, v62, s99, v189\n"
    "v_dot2_f32_bf16 v214, v63, s98, v190\n"
    "v_dot2_f32_bf16 v215, v63, s99, v191\n"
    "s_nop 0\n"
    "v_cvt_pk_bf16_f32 v60, v208, v209\n"
    "v_cvt_pk_bf16_f32 v61, v210, v211\n"
    "v_cvt_pk_bf16_f32 v62, v212, v213\n"
    "v_cvt_pk_bf16_f32 v63, v214, v215\n"
    "ds_write_b128 v216, v[60:63] offset:37248\n"
    "v_mfma_f32_32x32x16_bf16 v[160:175], a[192:195], a[192:195], 0\n"
    "v_mfma_f32_32x32x16_bf16 v[176:191], a[192:195], a[192:195], 0\n"
    "v_add_u32 v208, s72, v232\n"
    "v_xor_b32 v209, 32, v208\n"
    "v_mov_b32 v201, v231\n"
    "ds_read_b64_tr_b16 a[128:129], v208 offset:0\n"
    "ds_read_b64_tr_b16 a[130:131], v208 offset:512\n"
    "ds_read_b64_tr_b16 a[132:133], v209 offset:0\n"
    "ds_read_b64_tr_b16 a[134:135], v209 offset:512\n"
    "ds_read_b64_tr_b16 a[136:137], v208 offset:256\n"
    "ds_read_b64_tr_b16 a[138:139], v208 offset:768\n"
    "ds_read_b64_tr_b16 a[140:141], v209 offset:256\n"
    "ds_read_b64_tr_b16 a[142:143], v209 offset:768\n"
    "v_xor_b32 v196, 0, v237\n"
    "v_xor_b32 v197, 64, v237\n"
    "v_xor_b32 v198, 128, v237\n"
    "v_xor_b32 v199, 192, v237\n"
    "v_xor_b32 v200, 32, v238\n"
    "v_mov_b32 v204, v227\n"
    "v_xor_b32_e64 v207, 32, v227\n"
    "v_add_u32 v205, 64512, v207\n"
    "v_add_u32 v206, 129024, v227\n"
    "v_add_u32 v207, 193536, v207\n"
    "s_mov_b32 s92, 0x3e0293ee\n"
    "s_mov_b32 s93, 0x3e0293ee\n"
    "s_waitcnt lgkmcnt(0)\n"
    "s_barrier\n"
    "s_or_b32 s73, s31, s30\n"
    "s_cbranch_scc1 ep_skip\n"
    "v_mov_b32 v220, s94\n"
    "v_mov_b32 v221, s95\n"
    "v_mov_b32 v222, 0\n"
    "s_mov_b64 exec, 1\n"
    "buffer_store_dwordx2 v[220:221], v222, s[60:63], 0 offen sc1\n"
    "s_mov_b64 exec, -1\n"
    "ep_skip:\n"
    "ds_read_b128 a[160:163], v201 offset:0\n"
    "ds_read_b128 a[164:167], v201 offset:256\n"
    "ds_read_b128 a[168:171], v201 offset:32768\n"
    "ds_read_b128 a[172:175], v201 offset:33024\n"
    "s_mov_b32 s69, 6\n"
    "s_mov_b32 s6, 0\n"
    "g4_loop:\n"
    "s_waitcnt vmcnt(8) lgkmcnt(0)\n"
    "v_mfma_f32_16x16x32_bf16 v[64:67], a[128:131], a[160:163], v[64:67]\n"
    "ds_read_b64_tr_b16 a[144:145], v208 offset:4096\n"
    "v_mfma_f32_16x16x32_bf16 v[72:75], a[128:131], a[164:167], v[72:75]\n"
    "ds_read_b64_tr_b16 a[146:147], v208 offset:4608\n"
    "s_add_u32 m0, s72, 0\n"
    "v_mfma_f32_16x16x32_bf16 v[80:83], a[128:131], a[168:171], v[80:83]\n"
    "ds_read_b64_tr_b16 a[148:149], v209 offset:4096\n"
    "v_mfma_f32_16x16x32_bf16 v[88:91], a[128:131], a[172:175], v[88:91]\n"
    "ds_read_b64_tr_b16 a[150:151], v209 offset:4608\n"
    "v_mfma_f32_16x16x32_bf16 v[68:71], a[132:135], a[160:163], v[68:71]\n"
    "ds_read_b64_tr_b16 a[152:153], v208 offset:4352\n"
    "v_mfma_f32_16x16x32_bf16 v[76:79], a[132:135], a[164:167], v[76:79]\n"
    "ds_read_b64_tr_b16 a[154:155], v208 offset:4864\n"
    "v_mfma_f32_16x16x32_bf16 v[84:87], a[132:135], a[168:171], v[84:87]\n"
    "ds_read_b64_tr_b16 a[156:157], v209 offset:4352\n"
    "v_mfma_f32_16x16x32_bf16 v[92:95], a[132:135], a[172:175], v[92:95]\n"
    "ds_read_b64_tr_b16 a[158:159], v209 offset:4864\n"
    "v_mfma_f32_16x16x32_bf16 v[96:99], a[136:139], a[160:163], v[96:99]\n"
    "ds_read_b128 a[176:179], v201 offset:2048\n"
    "v_mfma_f32_16x16x32_bf16 v[104:107], a[136:139], a[164:167], v[104:107]\n"
    "ds_read_b128 a[180:183], v201 offset:2304\n"
    "v_mfma_f32_16x16x32_bf16 v[112:115], a[136:139], a[168:171], v[112:115]\n"
    "buffer_load_dwordx4 v204, s[40:43], s64 offen offset:0 lds\n"
    "v_mfma_f32_16x16x32_bf16 v[120:123], a[136:139], a[172:175], v[120:123]\n"
    "buffer_load_dwordx4 v205, s[40:43], s64 offen offset:1024 lds\n"
    "v_mfma_f32_16x16x32_bf16 v[100:103], a[140:143], a[160:163], v[100:103]\n"
    "buffer_load_dwordx4 v206, s[40:43], s64 offen offset:2048 lds\n"
    "v_mfma_f32_16x16x32_bf16 v[108:111], a[140:143], a[164:167], v[108:111]\n"
    "buffer_load_dwordx4 v207, s[40:43], s64 offen offset:3072 lds\n"
    "v_mfma_f32_16x16x32_bf16 v[116:119], a[140:143], a[168:171], v[116:119]\n"
    "s_add_u32 s64, s64, s65\n"
    "v_mfma_f32_16x16x32_bf16 v[124:127], a[140:143], a[172:175], v[124:127]\n"
    "s_waitcnt vmcnt(8) lgkmcnt(0)\n"
    "v_mfma_f32_16x16x32_bf16 v[128:131], a[144:147], a[160:163], v[128:131]\n"
    "ds_read_b64_tr_b16 a[128:129], v208 offset:8192\n"
    "v_mfma_f32_16x16x32_bf16 v[136:139], a[144:147], a[164:167], v[136:139]\n"
    "ds_read_b64_tr_b16 a[130:131], v208 offset:8704\n"
    "s_add_u32 m0, s72, 4096\n"
    "v_mfma_f32_16x16x32_bf16 v[144:147], a[144:147], a[168:171], v[144:147]\n"
    "ds_read_b64_tr_b16 a[132:133], v209 offset:8192\n"
    "v_mfma_f32_16x16x32_bf16 v[152:155], a[144:147], a[172:175], v[152:155]\n"
    "ds_read_b64_tr_b16 a[134:135], v209 offset:8704\n"
    "v_mfma_f32_16x16x32_bf16 v[132:135], a[148:151], a[160:163], v[132:135]\n"
    "ds_read_b64_tr_b16 a[136:137], v208 offset:8448\n"
    "v_mfma_f32_16x16x32_bf16 v[140:143], a[148:151], a[164:167], v[140:143]\n"
    "ds_read_b64_tr_b16 a[138:139], v208 offset:8960\n"
    "v_mfma_f32_16x16x32_bf16 v[148:151], a[148:151], a[168:171], v[148:151]\n"
    "ds_read_b64_tr_b16 a[140:141], v209 offset:8448\n"
    "v_mfma_f32_16x16x32_bf16 v[156:159], a[148:151], a[172:175], v[156:159]\n"
    "ds_read_b64_tr_b16 a[142:143], v209 offset:8960\n"
    "v_mfma_f32_16x16x32_bf16 v[160:163], a[152:155], a[160:163], v[160:163]\n"
    "ds_read_b128 a[184:187], v201 offset:34816\n"
    "v_mfma_f32_16x16x32_bf16 v[168:171], a[152:155], a[164:167], v[168:171]\n"
    "ds_read_b128 a[188:191], v201 offset:35072\n"
    "v_mfma_f32_16x16x32_bf16 v[176:179], a[152:155], a[168:171], v[176:179]\n"
    "buffer_load_dwordx4 v204, s[40:43], s64 offen offset:0 lds\n"
    "v_mfma_f32_16x16x32_bf16 v[184:187], a[152:155], a[172:175], v[184:187]\n"
    "buffer_load_dwordx4 v205, s[40:43], s64 offen offset:1024 lds\n"
    "v_mfma_f32_16x16x32_bf16 v[164:167], a[156:159], a[160:163], v[164:167]\n"
    "buffer_load_dwordx4 v206, s[40:43], s64 offen offset:2048 lds\n"
    "v_mfma_f32_16x16x32_bf16 v[172:175], a[156:159], a[164:167], v[172:175]\n"
    "buffer_load_dwordx4 v207, s[40:43], s64 offen offset:3072 lds\n"
    "v_mfma_f32_16x16x32_bf16 v[180:183], a[156:159], a[168:171], v[180:183]\n"
    "s_add_u32 s64, s64, s66\n"
    "v_mfma_f32_16x16x32_bf16 v[188:191], a[156:159], a[172:175], v[188:191]\n"
    "s_waitcnt vmcnt(8) lgkmcnt(0)\n"
    "v_mfma_f32_16x16x32_bf16 v[64:67], a[128:131], a[176:179], v[64:67]\n"
    "ds_read_b64_tr_b16 a[144:145], v208 offset:12288\n"
    "v_mfma_f32_16x16x32_bf16 v[72:75], a[128:131], a[180:183], v[72:75]\n"
    "ds_read_b64_tr_b16 a[146:147], v208 offset:12800\n"
    "s_add_u32 m0, s72, 8192\n"
    "v_mfma_f32_16x16x32_bf16 v[80:83], a[128:131], a[184:187], v[80:83]\n"
    "ds_read_b64_tr_b16 a[148:149], v209 offset:12288\n"
    "v_mfma_f32_16x16x32_bf16 v[88:91], a[128:131], a[188:191], v[88:91]\n"
    "ds_read_b64_tr_b16 a[150:151], v209 offset:12800\n"
    "v_mfma_f32_16x16x32_bf16 v[68:71], a[132:135], a[176:179], v[68:71]\n"
    "ds_read_b64_tr_b16 a[152:153], v208 offset:12544\n"
    "v_mfma_f32_16x16x32_bf16 v[76:79], a[132:135], a[180:183], v[76:79]\n"
    "ds_read_b64_tr_b16 a[154:155], v208 offset:13056\n"
    "v_mfma_f32_16x16x32_bf16 v[84:87], a[132:135], a[184:187], v[84:87]\n"
    "ds_read_b64_tr_b16 a[156:157], v209 offset:12544\n"
    "v_mfma_f32_16x16x32_bf16 v[92:95], a[132:135], a[188:191], v[92:95]\n"
    "ds_read_b64_tr_b16 a[158:159], v209 offset:13056\n"
    "v_mfma_f32_16x16x32_bf16 v[96:99], a[136:139], a[176:179], v[96:99]\n"
    "ds_read_b128 a[160:163], v201 offset:4096\n"
    "v_mfma_f32_16x16x32_bf16 v[104:107], a[136:139], a[180:183], v[104:107]\n"
    "ds_read_b128 a[164:167], v201 offset:4352\n"
    "v_mfma_f32_16x16x32_bf16 v[112:115], a[136:139], a[184:187], v[112:115]\n"
    "buffer_load_dwordx4 v204, s[40:43], s64 offen offset:0 lds\n"
    "v_mfma_f32_16x16x32_bf16 v[120:123], a[136:139], a[188:191], v[120:123]\n"
    "buffer_load_dwordx4 v205, s[40:43], s64 offen offset:1024 lds\n"
    "v_mfma_f32_16x16x32_bf16 v[100:103], a[140:143], a[176:179], v[100:103]\n"
    "buffer_load_dwordx4 v206, s[40:43], s64 offen offset:2048 lds\n"
    "v_mfma_f32_16x16x32_bf16 v[108:111], a[140:143], a[180:183], v[108:111]\n"
    "buffer_load_dwordx4 v207, s[40:43], s64 offen offset:3072 lds\n"
    "v_mfma_f32_16x16x32_bf16 v[116:119], a[140:143], a[184:187], v[116:119]\n"
    "s_add_u32 s64, s64, s65\n"
    "v_mfma_f32_16x16x32_bf16 v[124:127], a[140:143], a[188:191], v[124:127]\n"
    "s_waitcnt vmcnt(8) lgkmcnt(0)\n"
    "v_mfma_f32_16x16x32_bf16 v[128:131], a[144:147], a[176:179], v[128:131]\n"
    "ds_read_b64_tr_b16 a[128:129], v208 offset:0\n"
    "v_mfma_f32_16x16x32_bf16 v[136:139], a[144:147], a[180:183], v[136:139]\n"
    "ds_read_b64_tr_b16 a[130:131], v208 offset:512\n"
    "s_add_u32 m0, s72, 12288\n"
    "v_mfma_f32_16x16x32_bf16 v[144:147], a[144:147], a[184:187], v[144:147]\n"
    "ds_read_b64_tr_b16 a[132:133], v209 offset:0\n"
    "v_mfma_f32_16x16x32_bf16 v[152:155], a[144:147], a[188:191], v[152:155]\n"
    "ds_read_b64_tr_b16 a[134:135], v209 offset:512\n"
    "v_mfma_f32_16x16x32_bf16 v[132:135], a[148:151], a[176:179], v[132:135]\n"
    "ds_read_b64_tr_b16 a[136:137], v208 offset:256\n"
    "v_mfma_f32_16x16x32_bf16 v[140:143], a[148:151], a[180:183], v[140:143]\n"
    "ds_read_b64_tr_b16 a[138:139], v208 offset:768\n"
    "v_mfma_f32_16x16x32_bf16 v[148:151], a[148:151], a[184:187], v[148:151]\n"
    "ds_read_b64_tr_b16 a[140:141], v209 offset:256\n"
    "v_mfma_f32_16x16x32_bf16 v[156:159], a[148:151], a[188:191], v[156:159]\n"
    "ds_read_b64_tr_b16 a[142:143], v209 offset:768\n"
    "v_mfma_f32_16x16x32_bf16 v[160:163], a[152:155], a[176:179], v[160:163]\n"
    "ds_read_b128 a[168:171], v201 offset:36864\n"
    "v_mfma_f32_16x16x32_bf16 v[168:171], a[152:155], a[180:183], v[168:171]\n"
    "ds_read_b128 a[172:175], v201 offset:37120\n"
    "v_mfma_f32_16x16x32_bf16 v[176:179], a[152:155], a[184:187], v[176:179]\n"
    "buffer_load_dwordx4 v204, s[40:43], s64 offen offset:0 lds\n"
    "v_mfma_f32_16x16x32_bf16 v[184:187], a[152:155], a[188:191], v[184:187]\n"
    "buffer_load_dwordx4 v205, s[40:43], s64 offen offset:1024 lds\n"
    "v_mfma_f32_16x16x32_bf16 v[164:167], a[156:159], a[176:179], v[164:167]\n"
    "buffer_load_dwordx4 v206, s[40:43], s64 offen offset:2048 lds\n"
    "v_mfma_f32_16x16x32_bf16 v[172:175], a[156:159], a[180:183], v[172:175]\n"
    "buffer_load_dwordx4 v207, s[40:43], s64 offen offset:3072 lds\n"
    "v_mfma_f32_16x16x32_bf16 v[180:183], a[156:159], a[184:187], v[180:183]\n"
    "s_add_u32 s64, s64, s66\n"
    "v_add_u32 v201, 4096, v201\n"
    "s_sub_u32 s69, s69, 1\n"
    "v_mfma_f32_16x16x32_bf16 v[188:191], a[156:159], a[188:191], v[188:191]\n"
    "s_cbranch_scc0 g4_loop\n"
    "s_cmp_lg_u32 s6, 0\n"
    "s_cbranch_scc1 g4_done\n"
    "s_mov_b64 s[40:41], s[12:13]\n"
    "s_mov_b32 s42, 0x80000\n"
    "s_mov_b32 s64, 0\n"
    "s_mov_b32 s65, 32768\n"
    "s_mov_b32 s66, 32768\n"
    "v_mov_b32 v204, v228\n"
    "v_xor_b32 v207, 32, v228\n"
    "v_add_u32 v205, 3072, v207\n"
    "v_add_u32 v206, 6144, v228\n"
    "v_add_u32 v207, 9216, v207\n"
    "s_mov_b32 s6, 1\n"
    "s_mov_b32 s69, 0\n"
    "s_branch g4_loop\n"
    "g4_done:\n"
    "s_waitcnt lgkmcnt(0)\n"
    "ds_read_b128 a[128:131], v196 offset:0\n"
    "ds_read_b128 a[132:135], v197 offset:0\n"
    "ds_read_b128 a[136:139], v198 offset:0\n"
    "ds_read_b128 a[140:143], v199 offset:0\n"
    "ds_read_b128 a[144:147], v196 offset:4096\n"
    "ds_read_b128 a[148:151], v197 offset:4096\n"
    "ds_read_b128 a[152:155], v198 offset:4096\n"
    "ds_read_b128 a[156:159], v199 offset:4096\n"
    "v_pk_mul_f32 v[64:65], v[64:65], s[92:93]\n"
    "v_pk_mul_f32 v[66:67], v[66:67], s[92:93]\n"
    "v_pk_mul_f32 v[68:69], v[68:69], s[92:93]\n"
    "v_pk_mul_f32 v[70:71], v[70:71], s[92:93]\n"
    "v_pk_mul_f32 v[72:73], v[72:73], s[92:93]\n"
    "v_pk_mul_f32 v[74:75], v[74:75], s[92:93]\n"
    "v_pk_mul_f32 v[76:77], v[76:77], s[92:93]\n"
    "v_pk_mul_f32 v[78:79], v[78:79], s[92:93]\n"
    "v_pk_mul_f32 v[80:81], v[80:81], s[92:93]\n"
    "v_pk_mul_f32 v[82:83], v[82:83], s[92:93]\n"
    "v_pk_mul_f32 v[84:85], v[84:85], s[92:93]\n"
    "v_pk_mul_f32 v[86:87], v[86:87], s[92:93]\n"
    "v_pk_mul_f32 v[88:89], v[88:89], s[92:93]\n"
    "v_pk_mul_f32 v[90:91], v[90:91], s[92:93]\n"
    "v_pk_mul_f32 v[92:93], v[92:93], s[92:93]\n"
    "v_pk_mul_f32 v[94:95], v[94:95], s[92:93]\n"
    "v_pk_mul_f32 v[96:97], v[96:97], s[92:93]\n"
    "v_pk_mul_f32 v[98:99], v[98:99], s[92:93]\n"
    "v_pk_mul_f32 v[100:101], v[100:101], s[92:93]\n"
    "v_pk_mul_f32 v[102:103], v[102:103], s[92:93]\n"
    "v_pk_mul_f32 v[104:105], v[104:105], s[92:93]\n"
    "v_pk_mul_f32 v[106:107], v[106:107], s[92:93]\n"
    "v_pk_mul_f32 v[108:109], v[108:109], s[92:93]\n"
    "v_pk_mul_f32 v[110:111], v[110:111], s[92:93]\n"
    "v_pk_mul_f32 v[112:113], v[112:113], s[92:93]\n"
    "v_pk_mul_f32 v[114:115], v[114:115], s[92:93]\n"
    "v_pk_mul_f32 v[116:117], v[116:117], s[92:93]\n"
    "v_pk_mul_f32 v[118:119], v[118:119], s[92:93]\n"
    "v_pk_mul_f32 v[120:121], v[120:121], s[92:93]\n"
    "v_pk_mul_f32 v[122:123], v[122:123], s[92:93]\n"
    "v_pk_mul_f32 v[124:125], v[124:125], s[92:93]\n"
    "v_pk_mul_f32 v[126:127], v[126:127], s[92:93]\n"
    "v_pk_mul_f32 v[128:129], v[128:129], s[92:93]\n"
    "v_pk_mul_f32 v[130:131], v[130:131], s[92:93]\n"
    "v_pk_mul_f32 v[132:133], v[132:133], s[92:93]\n"
    "v_pk_mul_f32 v[134:135], v[134:135], s[92:93]\n"
    "v_pk_mul_f32 v[136:137], v[136:137], s[92:93]\n"
    "v_pk_mul_f32 v[138:139], v[138:139], s[92:93]\n"
    "v_pk_mul_f32 v[140:141], v[140:141], s[92:93]\n"
    "v_pk_mul_f32 v[142:143], v[142:143], s[92:93]\n"
    "v_pk_mul_f32 v[144:145], v[144:145], s[92:93]\n"
    "v_pk_mul_f32 v[146:147], v[146:147], s[92:93]\n"
    "v_pk_mul_f32 v[148:149], v[148:149], s[92:93]\n"
    "v_pk_mul_f32 v[150:151], v[150:151], s[92:93]\n"
    "v_pk_mul_f32 v[152:153], v[152:153], s[92:93]\n"
    "v_pk_mul_f32 v[154:155], v[154:155], s[92:93]\n"
    "v_pk_mul_f32 v[156:157], v[156:157], s[92:93]\n"
    "v_pk_mul_f32 v[158:159], v[158:159], s[92:93]\n"
    "v_pk_mul_f32 v[160:161], v[160:161], s[92:93]\n"
    "v_pk_mul_f32 v[162:163], v[162:163], s[92:93]\n"
    "v_pk_mul_f32 v[164:165], v[164:165], s[92:93]\n"
    "v_pk_mul_f32 v[166:167], v[166:167], s[92:93]\n"
    "v_pk_mul_f32 v[168:169], v[168:169], s[92:93]\n"
    "v_pk_mul_f32 v[170:171], v[170:171], s[92:93]\n"
    "v_pk_mul_f32 v[172:173], v[172:173], s[92:93]\n"
    "v_pk_mul_f32 v[174:175], v[174:175], s[92:93]\n"
    "v_pk_mul_f32 v[176:177], v[176:177], s[92:93]\n"
    "v_pk_mul_f32 v[178:179], v[178:179], s[92:93]\n"
    "v_pk_mul_f32 v[180:181], v[180:181], s[92:93]\n"
    "v_pk_mul_f32 v[182:183], v[182:183], s[92:93]\n"
    "v_pk_mul_f32 v[184:185], v[184:185], s[92:93]\n"
    "v_pk_mul_f32 v[186:187], v[186:187], s[92:93]\n"
    "v_pk_mul_f32 v[188:189], v[188:189], s[92:93]\n"
    "v_pk_mul_f32 v[190:191], v[190:191], s[92:93]\n"
    "v_cvt_pk_bf16_f32 v0, v64, v65\n"
    "v_cvt_pk_bf16_f32 v1, v66, v67\n"
    "v_cvt_pk_bf16_f32 v2, v68, v69\n"
    "v_cvt_pk_bf16_f32 v3, v70, v71\n"
    "v_cvt_pk_bf16_f32 v16, v72, v73\n"
    "v_cvt_pk_bf16_f32 v17, v74, v75\n"
    "v_cvt_pk_bf16_f32 v18, v76, v77\n"
    "v_cvt_pk_bf16_f32 v19, v78, v79\n"
    "v_cvt_pk_bf16_f32 v32, v80, v81\n"
    "v_cvt_pk_bf16_f32 v33, v82, v83\n"
    "v_cvt_pk_bf16_f32 v34, v84, v85\n"
    "v_cvt_pk_bf16_f32 v35, v86, v87\n"
    "v_cvt_pk_bf16_f32 v48, v88, v89\n"
    "v_cvt_pk_bf16_f32 v49, v90, v91\n"
    "v_cvt_pk_bf16_f32 v50, v92, v93\n"
    "v_cvt_pk_bf16_f32 v51, v94, v95\n"
    "v_permlane16_swap_b32 v0, v2\n"
    "v_permlane16_swap_b32 v1, v3\n"
    "v_permlane16_swap_b32 v16, v18\n"
    "v_permlane16_swap_b32 v17, v19\n"
    "v_permlane16_swap_b32 v32, v34\n"
    "v_permlane16_swap_b32 v33, v35\n"
    "v_permlane16_swap_b32 v48, v50\n"
    "v_permlane16_swap_b32 v49, v51\n"
    "s_waitcnt lgkmcnt(0)\n"
    "s_barrier\n"
    "s_add_u32 m0, s88, 0\n"
    "s_nop 0\n"
    "buffer_load_dwordx4 v234, s[44:47], s86 offen lds\n"
    "buffer_load_dwordx4 v235, s[44:47], s86 offen offset:1024 lds\n"
    "s_add_u32 s86, s86, 8192\n"
    "v_mfma_f32_16x16x32_bf16 v[64:67], a[128:131], v[0:3], 0\n"
    "v_cvt_pk_bf16_f32 v4, v96, v97\n"
    "v_cvt_pk_bf16_f32 v5, v98, v99\n"
    "v_cvt_pk_bf16_f32 v6, v100, v101\n"
    "v_mfma_f32_16x16x32_bf16 v[68:71], a[144:147], v[0:3], 0\n"
    "v_cvt_pk_bf16_f32 v7, v102, v103\n"
    "v_cvt_pk_bf16_f32 v20, v104, v105\n"
    "v_cvt_pk_bf16_f32 v21, v106, v107\n"
    "v_mfma_f32_16x16x32_bf16 v[72:75], a[128:131], v[16:19], 0\n"
    "v_cvt_pk_bf16_f32 v22, v108, v109\n"
    "v_cvt_pk_bf16_f32 v23, v110, v111\n"
    "v_cvt_pk_bf16_f32 v36, v112, v113\n"
    "v_mfma_f32_16x16x32_bf16 v[76:79], a[144:147], v[16:19], 0\n"
    "v_cvt_pk_bf16_f32 v37, v114, v115\n"
    "v_cvt_pk_bf16_f32 v38, v116, v117\n"
    "v_cvt_pk_bf16_f32 v39, v118, v119\n"
    "v_mfma_f32_16x16x32_bf16 v[80:83], a[128:131], v[32:35], 0\n"
    "v_cvt_pk_bf16_f32 v52, v120, v121\n"
    "v_cvt_pk_bf16_f32 v53, v122, v123\n"
    "v_cvt_pk_bf16_f32 v54, v124, v125\n"
    "v_mfma_f32_16x16x32_bf16 v[84:87], a[144:147], v[32:35], 0\n"
    "v_cvt_pk_bf16_f32 v55, v126, v127\n"
    "v_permlane16_swap_b32 v4, v6\n"
    "v_permlane16_swap_b32 v5, v7\n"
    "v_mfma_f32_16x16x32_bf16 v[88:91], a[128:131], v[48:51], 0\n"
    "v_permlane16_swap_b32 v20, v22\n"
    "v_permlane16_swap_b32 v21, v23\n"
    "v_permlane16_swap_b32 v36, v38\n"
    "v_mfma_f32_16x16x32_bf16 v[92:95], a[144:147], v[48:51], 0\n"
    "v_permlane16_swap_b32 v37, v39\n"
    "v_permlane16_swap_b32 v52, v54\n"
    "v_permlane16_swap_b32 v53, v55\n"
    "v_mfma_f32_16x16x32_bf16 v[64:67], a[132:135], v[4:7], v[64:67]\n"
    "v_cvt_pk_bf16_f32 v8, v128, v129\n"
    "v_cvt_pk_bf16_f32 v9, v130, v131\n"
    "v_cvt_pk_bf16_f32 v10, v132, v133\n"
    "v_mfma_f32_16x16x32_bf16 v[68:71], a[148:151], v[4:7], v[68:71]\n"
    "v_cvt_pk_bf16_f32 v11, v134, v135\n"
    "v_cvt_pk_bf16_f32 v24, v136, v137\n"
    "v_cvt_pk_bf16_f32 v25, v138, v139\n"
    "v_mfma_f32_16x16x32_bf16 v[72:75], a[132:135], v[20:23], v[72:75]\n"
    "v_cvt_pk_bf16_f32 v26, v140, v141\n"
    "v_cvt_pk_bf16_f32 v27, v142, v143\n"
    "v_cvt_pk_bf16_f32 v40, v144, v145\n"
    "v_mfma_f32_16x16x32_bf16 v[76:79], a[148:151], v[20:23], v[76:79]\n"
    "v_cvt_pk_bf16_f32 v41, v146, v147\n"
    "v_cvt_pk_bf16_f32 v42, v148, v149\n"
    "v_cvt_pk_bf16_f32 v43, v150, v151\n"
    "v_mfma_f32_16x16x32_bf16 v[80:83], a[132:135], v[36:39], v[80:83]\n"
    "v_cvt_pk_bf16_f32 v56, v152, v153\n"
    "v_cvt_pk_bf16_f32 v57, v154, v155\n"
    "v_cvt_pk_bf16_f32 v58, v156, v157\n"
    "v_mfma_f32_16x16x32_bf16 v[84:87], a[148:151], v[36:39], v[84:87]\n"
    "v_cvt_pk_bf16_f32 v59, v158, v159\n"
    "v_permlane16_swap_b32 v8, v10\n"
    "v_permlane16_swap_b32 v9, v11\n"
    "v_mfma_f32_16x16x32_bf16 v[88:91], a[132:135], v[52:55], v[88:91]\n"
    "v_permlane16_swap_b32 v24, v26\n"
    "v_permlane16_swap_b32 v25, v27\n"
    "v_permlane16_swap_b32 v40, v42\n"
    "v_mfma_f32_16x16x32_bf16 v[92:95], a[148:151], v[52:55], v[92:95]\n"
    "v_permlane16_swap_b32 v41, v43\n"
    "v_permlane16_swap_b32 v56, v58\n"
    "v_permlane16_swap_b32 v57, v59\n"
    "v_mfma_f32_16x16x32_bf16 v[64:67], a[136:139], v[8:11], v[64:67]\n"
    "v_cvt_pk_bf16_f32 v12, v160, v161\n"
    "v_cvt_pk_bf16_f32 v13, v162, v163\n"
    "v_cvt_pk_bf16_f32 v14, v164, v165\n"
    "v_mfma_f32_16x16x32_bf16 v[68:71], a[152:155], v[8:11], v[68:71]\n"
    "v_cvt_pk_bf16_f32 v15, v166, v167\n"
    "v_cvt_pk_bf16_f32 v28, v168, v169\n"
    "v_cvt_pk_bf16_f32 v29, v170, v171\n"
    "v_mfma_f32_16x16x32_bf16 v[72:75], a[136:139], v[24:27], v[72:75]\n"
    "v_cvt_pk_bf16_f32 v30, v172, v173\n"
    "v_cvt_pk_bf16_f32 v31, v174, v175\n"
    "v_cvt_pk_bf16_f32 v44, v176, v177\n"
    "v_mfma_f32_16x16x32_bf16 v[76:79], a[152:155], v[24:27], v[76:79]\n"
    "v_cvt_pk_bf16_f32 v45, v178, v179\n"
    "v_cvt_pk_bf16_f32 v46, v180, v181\n"
    "v_cvt_pk_bf16_f32 v47, v182, v183\n"
    "v_mfma_f32_16x16x32_bf16 v[80:83], a[136:139], v[40:43], v[80:83]\n"
    "v_cvt_pk_bf16_f32 v60, v184, v185\n"
    "v_cvt_pk_bf16_f32 v61, v186, v187\n"
    "v_cvt_pk_bf16_f32 v62, v188, v189\n"
    "v_mfma_f32_16x16x32_bf16 v[84:87], a[152:155], v[40:43], v[84:87]\n"
    "v_cvt_pk_bf16_f32 v63, v190, v191\n"
    "v_permlane16_swap_b32 v12, v14\n"
    "v_permlane16_swap_b32 v13, v15\n"
    "v_mfma_f32_16x16x32_bf16 v[88:91], a[136:139], v[56:59], v[88:91]\n"
    "v_permlane16_swap_b32 v28, v30\n"
    "v_permlane16_swap_b32 v29, v31\n"
    "v_permlane16_swap_b32 v44, v46\n"
    "v_mfma_f32_16x16x32_bf16 v[92:95], a[152:155], v[56:59], v[92:95]\n"
    "v_permlane16_swap_b32 v45, v47\n"
    "v_permlane16_swap_b32 v60, v62\n"
    "v_permlane16_swap_b32 v61, v63\n"
    "v_mfma_f32_16x16x32_bf16 v[64:67], a[140:143], v[12:15], v[64:67]\n"
    "v_mfma_f32_16x16x32_bf16 v[68:71], a[156:159], v[12:15], v[68:71]\n"
    "v_mfma_f32_16x16x32_bf16 v[72:75], a[140:143], v[28:31], v[72:75]\n"
    "v_mfma_f32_16x16x32_bf16 v[76:79], a[156:159], v[28:31], v[76:79]\n"
    "v_mfma_f32_16x16x32_bf16 v[80:83], a[140:143], v[44:47], v[80:83]\n"
    "v_mfma_f32_16x16x32_bf16 v[84:87], a[156:159], v[44:47], v[84:87]\n"
    "v_mfma_f32_16x16x32_bf16 v[88:91], a[140:143], v[60:63], v[88:91]\n"
    "v_mfma_f32_16x16x32_bf16 v[92:95], a[156:159], v[60:63], v[92:95]\n"
    "attn_re:\n"
    "s_mov_b64 s[52:53], s[24:25]\n"
    "s_mov_b32 s54, 3145728\n"
    "s_lshl_b32 s97, s36, 4\n"
    "ds_read_b128 a[128:131], v196 offset:8192\n"
    "ds_read_b128 a[132:135], v197 offset:8192\n"
    "ds_read_b128 a[136:139], v198 offset:8192\n"
    "ds_read_b128 a[140:143], v199 offset:8192\n"
    "ds_read_b128 a[144:147], v196 offset:12288\n"
    "ds_read_b128 a[148:151], v197 offset:12288\n"
    "ds_read_b128 a[152:155], v198 offset:12288\n"
    "ds_read_b128 a[156:159], v199 offset:12288\n"
    "v_mov_b32 v192, 0\n"
    "s_lshl_b32 s96, s30, 13\n"
    "v_mov_b32 v214, s96\n"
    "ds_write_b32 v214, v192\n"
    "v_mov_b32 v216, v248\n"
    "v_mov_b32 v217, v249\n"
    "v_accvgpr_read_b32 v202, a255\n"
    "v_mov_b32 v203, 0\n"
    "s_cmp_lg_u32 s91, 0\n"
    "s_cbranch_scc1 attn_havem\n"
    "v_max3_f32 v210, v64, v65, v66\n"
    "v_max3_f32 v210, v210, v67, v68\n"
    "v_max3_f32 v210, v210, v69, v70\n"
    "v_max_f32_e64 v210, v210, v71\n"
    "v_max3_f32 v211, v72, v73, v74\n"
    "v_max3_f32 v211, v211, v75, v76\n"
    "v_max3_f32 v211, v211, v77, v78\n"
    "v_max_f32_e64 v211, v211, v79\n"
    "v_max3_f32 v212, v80, v81, v82\n"
    "v_max3_f32 v212, v212, v83, v84\n"
    "v_max3_f32 v212, v212, v85, v86\n"
    "v_max_f32_e64 v212, v212, v87\n"
    "v_max3_f32 v213, v88, v89, v90\n"
    "v_max3_f32 v213, v213, v91, v92\n"
    "v_max3_f32 v213, v213, v93, v94\n"
    "v_max_f32 v213, v213, v95\n"
    "v_mov_b32 v176, v210\n"
    "v_mov_b32 v177, v211\n"
    "v_mov_b32 v178, v212\n"
    "v_mov_b32 v179, v213\n"
    "v_permlane32_swap_b32 v210, v176\n"
    "v_permlane32_swap_b32 v211, v177\n"
    "v_permlane32_swap_b32 v212, v178\n"
    "v_permlane32_swap_b32 v213, v179\n"
    "v_max_f32 v210, v210, v176\n"
    "v_max_f32 v211, v211, v177\n"
    "v_max_f32 v212, v212, v178\n"
    "v_max_f32 v213, v213, v179\n"
    "v_mov_b32 v176, v210\n"
    "v_mov_b32 v177, v211\n"
    "v_mov_b32 v178, v212\n"
    "v_mov_b32 v179, v213\n"
    "v_permlane16_swap_b32 v210, v176\n"
    "v_permlane16_swap_b32 v211, v177\n"
    "v_permlane16_swap_b32 v212, v178\n"
    "v_permlane16_swap_b32 v213, v179\n"
    "v_max_f32 v210, v210, v176\n"
    "v_max_f32 v211, v211, v177\n"
    "v_max_f32 v212, v212, v178\n"
    "v_max_f32_e64 v213, v213, v179\n"
    "v_sub_f32 v210, 0xc2800000, v210\n"
    "v_sub_f32 v211, 0xc2800000, v211\n"
    "v_sub_f32 v212, 0xc2800000, v212\n"
    "v_sub_f32 v213, 0xc2800000, v213\n"
    "attn_havem:\n"
    "v_mov_b32 v160, v210\n"
    "v_mov_b32 v161, v210\n"
    "v_mov_b32 v162, v210\n"
    "v_mov_b32 v163, v210\n"
    "v_mov_b32 v164, v211\n"
    "v_mov_b32 v165, v211\n"
    "v_mov_b32 v166, v211\n"
    "v_mov_b32 v167, v211\n"
    "v_mov_b32 v168, v212\n"
    "v_mov_b32 v169, v212\n"
    "v_mov_b32 v170, v212\n"
    "v_mov_b32 v171, v212\n"
    "v_mov_b32 v172, v213\n"
    "v_mov_b32 v173, v213\n"
    "v_mov_b32 v174, v213\n"
    "v_mov_b32 v175, v213\n"
    "s_mov_b32 s90, 7\n"
    "s_waitcnt lgkmcnt(0)\n"
    "v_mfma_f32_16x16x32_bf16 v[96:99], a[128:131], v[0:3], v[160:163]\n"
    "v_add_f32 v64, v210, v64\n"
    "v_add_f32 v65, v210, v65\n"
    "v_mfma_f32_16x16x32_bf16 v[96:99], a[132:135], v[4:7], v[96:99]\n"
    "v_exp_f32 v64, v64\n"
    "v_mfma_f32_16x16x32_bf16 v[96:99], a[136:139], v[8:11], v[96:99]\n"
    "v_add_f32 v66, v210, v66\n"
    "v_exp_f32 v65, v65\n"
    "v_mfma_f32_16x16x32_bf16 v[96:99], a[140:143], v[12:15], v[96:99]\n"
    "v_add_f32 v67, v210, v67\n"
    "v_mfma_f32_16x16x32_bf16 v[104:107], a[128:131], v[16:19], v[164:167]\n"
    "v_exp_f32 v66, v66\n"
    "v_mfma_f32_16x16x32_bf16 v[104:107], a[132:135], v[20:23], v[104:107]\n"
    "v_add_f32 v68, v210, v68\n"
    "v_exp_f32_e64 v67, v67\n"
    "v_mfma_f32_16x16x32_bf16 v[104:107], a[136:139], v[24:27], v[104:107]\n"
    "v_cvt_pk_bf16_f32 v128, v64, v65\n"
    "v_mfma_f32_16x16x32_bf16 v[104:107], a[140:143], v[28:31], v[104:107]\n"
    "v_add_f32 v69, v210, v69\n"
    "v_exp_f32 v68, v68\n"
    "v_mfma_f32_16x16x32_bf16 v[112:115], a[128:131], v[32:35], v[168:171]\n"
    "v_add_f32 v70, v210, v70\n"
    "v_mfma_f32_16x16x32_bf16 v[112:115], a[132:135], v[36:39], v[112:115]\n"
    "v_exp_f32 v69, v69\n"
    "v_cvt_pk_bf16_f32 v129, v66, v67\n"
    "v_mfma_f32_16x16x32_bf16 v[112:115], a[136:139], v[40:43], v[112:115]\n"
    "v_add_f32 v71, v210, v71\n"
    "v_mfma_f32_16x16x32_bf16 v[112:115], a[140:143], v[44:47], v[112:115]\n"
    "v_exp_f32 v70, v70\n"
    "v_add_f32 v72, v211, v72\n"
    "v_mfma_f32_16x16x32_bf16 v[120:123], a[128:131], v[48:51], v[172:175]\n"
    "v_exp_f32 v71, v71\n"
    "v_mfma_f32_16x16x32_bf16 v[120:123], a[132:135], v[52:55], v[120:123]\n"
    "v_cvt_pk_bf16_f32 v130, v68, v69\n"
    "v_add_f32 v73, v211, v73\n"
    "v_mfma_f32_16x16x32_bf16 v[120:123], a[136:139], v[56:59], v[120:123]\n"
    "v_exp_f32 v72, v72\n"
    "v_mfma_f32_16x16x32_bf16 v[120:123], a[140:143], v[60:63], v[120:123]\n"
    "v_add_f32 v74, v211, v74\n"
    "v_exp_f32 v73, v73\n"
    "v_mfma_f32_16x16x32_bf16 v[100:103], a[144:147], v[0:3], v[160:163]\n"
    "v_cvt_pk_bf16_f32 v131, v70, v71\n"
    "v_mfma_f32_16x16x32_bf16 v[100:103], a[148:151], v[4:7], v[100:103]\n"
    "v_add_f32 v75, v211, v75\n"
    "v_exp_f32 v74, v74\n"
    "v_mfma_f32_16x16x32_bf16 v[100:103], a[152:155], v[8:11], v[100:103]\n"
    "v_add_f32 v76, v211, v76\n"
    "v_mfma_f32_16x16x32_bf16 v[100:103], a[156:159], v[12:15], v[100:103]\n"
    "v_exp_f32 v75, v75\n"
    "v_mfma_f32_16x16x32_bf16 v[108:111], a[144:147], v[16:19], v[164:167]\n"
    "v_cvt_pk_bf16_f32 v132, v72, v73\n"
    "v_add_f32 v77, v211, v77\n"
    "v_mfma_f32_16x16x32_bf16 v[108:111], a[148:151], v[20:23], v[108:111]\n"
    "v_exp_f32 v76, v76\n"
    "v_mfma_f32_16x16x32_bf16 v[108:111], a[152:155], v[24:27], v[108:111]\n"
    "v_add_f32 v78, v211, v78\n"
    "v_exp_f32 v77, v77\n"
    "v_mfma_f32_16x16x32_bf16 v[108:111], a[156:159], v[28:31], v[108:111]\n"
    "v_cvt_pk_bf16_f32 v133, v74, v75\n"
    "v_add_f32 v79, v211, v79\n"
    "v_mfma_f32_16x16x32_bf16 v[116:119], a[144:147], v[32:35], v[168:171]\n"
    "v_exp_f32 v78, v78\n"
    "v_mfma_f32_16x16x32_bf16 v[116:119], a[148:151], v[36:39], v[116:119]\n"
    "v_add_f32 v80, v212, v80\n"
    "v_mfma_f32_16x16x32_bf16 v[116:119], a[152:155], v[40:43], v[116:119]\n"
    "v_exp_f32 v79, v79\n"
    "v_cvt_pk_bf16_f32 v134, v76, v77\n"
    "v_mfma_f32_16x16x32_bf16 v[116:119], a[156:159], v[44:47], v[116:119]\n"
    "v_add_f32 v81, v212, v81\n"
    "v_mfma_f32_16x16x32_bf16 v[124:127], a[144:147], v[48:51], v[172:175]\n"
    "v_exp_f32 v80, v80\n"
    "v_add_f32 v82, v212, v82\n"
    "v_mfma_f32_16x16x32_bf16 v[124:127], a[148:151], v[52:55], v[124:127]\n"
    "v_exp_f32 v81, v81\n"
    "v_mfma_f32_16x16x32_bf16 v[124:127], a[152:155], v[56:59], v[124:127]\n"
    "v_cvt_pk_bf16_f32 v135, v78, v79\n"
    "v_add_f32 v83, v212, v83\n"
    "v_mfma_f32_16x16x32_bf16 v[124:127], a[156:159], v[60:63], v[124:127]\n"
    "s_waitcnt vmcnt(0) lgkmcnt(0)\n"
    "s_barrier\n"
    "v_mfma_f32_32x32x16_bf16 a[0:15], a[192:195], a[192:195], 0\n"
    "ds_read_b128 a[128:131], v196 offset:0\n"
    "ds_read_b128 a[132:135], v197 offset:0\n"
    "s_add_u32 m0, s88, 8192\n"
    "v_exp_f32 v82, v82\n"
    "v_add_f32 v84, v212, v84\n"
    "v_exp_f32 v83, v83\n"
    "v_cvt_pk_bf16_f32 v136, v80, v81\n"
    "v_add_f32_e64 v85, v212, v85\n"
    "v_mfma_f32_32x32x16_bf16 a[16:31], a[192:195], a[192:195], 0\n"
    "ds_read_b128 a[136:139], v198 offset:0\n"
    "ds_read_b128 a[140:143], v199 offset:0\n"
    "buffer_load_dwordx4 v234, s[44:47], s86 offen lds\n"
    "v_exp_f32 v84, v84\n"
    "v_add_f32 v86, v212, v86\n"
    "v_exp_f32 v85, v85\n"
    "v_cvt_pk_bf16_f32 v137, v82, v83\n"
    "v_add_f32 v87, v212, v87\n"
    "v_mfma_f32_32x32x16_bf16 a[32:47], a[192:195], a[192:195], 0\n"
    "ds_read_b128 a[144:147], v196 offset:4096\n"
    "ds_read_b128 a[148:151], v197 offset:4096\n"
    "buffer_load_dwordx4 v235, s[44:47], s86 offen offset:1024 lds\n"
    "v_exp_f32 v86, v86\n"
    "v_add_f32 v88, v213, v88\n"
    "v_exp_f32 v87, v87\n"
    "v_cvt_pk_bf16_f32 v138, v84, v85\n"
    "v_add_f32 v89, v213, v89\n"
    "v_mfma_f32_32x32x16_bf16 a[48:63], a[192:195], a[192:195], 0\n"
    "ds_read_b128 a[152:155], v198 offset:4096\n"
    "ds_read_b128 a[156:159], v199 offset:4096\n"
    "s_add_u32 s86, s86, 8192\n"
    "v_exp_f32 v88, v88\n"
    "v_add_f32 v90, v213, v90\n"
    "v_exp_f32 v89, v89\n"
    "v_cvt_pk_bf16_f32 v139, v86, v87\n"
    "v_add_f32 v91, v213, v91\n"
    "v_mfma_f32_32x32x16_bf16 a[64:79], a[192:195], a[192:195], 0\n"
    "buffer_load_dwordx4 a[196:199], v251, s[52:55], s97 offen offset:2048\n"
    "v_exp_f32 v90, v90\n"
    "v_add_f32 v92, v213, v92\n"
    "v_exp_f32 v91, v91\n"
    "v_cvt_pk_bf16_f32 v140, v88, v89\n"
    "v_add_f32 v93, v213, v93\n"
    "v_mfma_f32_32x32x16_bf16 a[80:95], a[192:195], a[192:195], 0\n"
    "buffer_load_dwordx4 a[200:203], v219, s[52:55], s97 offen offset:2048\n"
    "v_exp_f32 v92, v92\n"
    "v_add_f32 v94, v213, v94\n"
    "v_exp_f32 v93, v93\n"
    "v_cvt_pk_bf16_f32 v141, v90, v91\n"
    "v_add_f32 v95, v213, v95\n"
    "v_mfma_f32_32x32x16_bf16 a[96:111], a[192:195], a[192:195], 0\n"
    "global_load_dword v218, v[216:217], off\n"
    "v_exp_f32 v94, v94\n"
    "v_exp_f32 v95, v95\n"
    "v_cvt_pk_bf16_f32 v142, v92, v93\n"
    "v_cvt_pk_bf16_f32 v143, v94, v95\n"
    "v_mfma_f32_32x32x16_bf16 a[112:127], a[192:195], a[192:195], 0\n"
    "v_mfma_f32_32x32x16_bf16 a[208:223], a[192:195], a[192:195], 0\n"
    "s_waitcnt lgkmcnt(0)\n"
    "attn_loop:\n"
    "v_mfma_f32_16x16x32_bf16 v[64:67], a[128:131], v[0:3], v[160:163]\n"
    "v_exp_f32 v96, v96\n"
    "v_mfma_f32_16x16x32_bf16 v[64:67], a[132:135], v[4:7], v[64:67]\n"
    "ds_read_b64_tr_b16 a[160:161], v238 offset:0\n"
    "v_mfma_f32_16x16x32_bf16 v[64:67], a[136:139], v[8:11], v[64:67]\n"
    "v_exp_f32_e64 v97, v97\n"
    "v_mfma_f32_16x16x32_bf16 v[64:67], a[140:143], v[12:15], v[64:67]\n"
    "ds_read_b64_tr_b16 a[162:163], v238 offset:4096\n"
    "v_mfma_f32_16x16x32_bf16 v[72:75], a[128:131], v[16:19], v[164:167]\n"
    "v_exp_f32_e64 v98, v98\n"
    "v_mfma_f32_16x16x32_bf16 v[72:75], a[132:135], v[20:23], v[72:75]\n"
    "ds_read_b64_tr_b16 a[164:165], v200 offset:0\n"
    "v_exp_f32_e64 v99, v99\n"
    "v_mfma_f32_16x16x32_bf16 v[72:75], a[136:139], v[24:27], v[72:75]\n"
    "ds_read_b64_tr_b16 a[166:167], v200 offset:4096\n"
    "v_mfma_f32_16x16x32_bf16 v[72:75], a[140:143], v[28:31], v[72:75]\n"
    "v_cvt_pk_bf16_f32 v144, v96, v97\n"
    "ds_read_b64_tr_b16 a[168:169], v238 offset:256\n"
    "v_mfma_f32_16x16x32_bf16 v[80:83], a[128:131], v[32:35], v[168:171]\n"
    "v_exp_f32_e64 v100, v100\n"
    "v_mfma_f32_16x16x32_bf16 v[80:83], a[132:135], v[36:39], v[80:83]\n"
    "ds_read_b64_tr_b16 a[170:171], v238 offset:4352\n"
    "v_mfma_f32_16x16x32_bf16 v[80:83], a[136:139], v[40:43], v[80:83]\n"
    "v_exp_f32_e64 v101, v101\n"
    "v_mfma_f32_16x16x32_bf16 v[80:83], a[140:143], v[44:47], v[80:83]\n"
    "ds_read_b64_tr_b16 a[172:173], v200 offset:256\n"
    "v_mfma_f32_16x16x32_bf16 v[88:91], a[128:131], v[48:51], v[172:175]\n"
    "v_cvt_pk_bf16_f32 v145, v98, v99\n"
    "ds_read_b64_tr_b16 a[174:175], v200 offset:4352\n"
    "v_mfma_f32_16x16x32_bf16 v[88:91], a[132:135], v[52:55], v[88:91]\n"
    "v_exp_f32_e64 v102, v102\n"
    "v_mfma_f32_16x16x32_bf16 v[88:91], a[136:139], v[56:59], v[88:91]\n"
    "ds_read_b64_tr_b16 a[176:177], v238 offset:512\n"
    "v_mfma_f32_16x16x32_bf16 v[88:91], a[140:143], v[60:63], v[88:91]\n"
    "v_exp_f32_e64 v103, v103\n"
    "v_mfma_f32_16x16x32_bf16 v[68:71], a[144:147], v[0:3], v[160:163]\n"
    "ds_read_b64_tr_b16 a[178:179], v238 offset:4608\n"
    "v_cvt_pk_bf16_f32 v146, v100, v101\n"
    "v_mfma_f32_16x16x32_bf16 v[68:71], a[148:151], v[4:7], v[68:71]\n"
    "ds_read_b64_tr_b16 a[180:181], v200 offset:512\n"
    "v_mfma_f32_16x16x32_bf16 v[68:71], a[152:155], v[8:11], v[68:71]\n"
    "v_exp_f32_e64 v104, v104\n"
    "v_mfma_f32_16x16x32_bf16 v[68:71], a[156:159], v[12:15], v[68:71]\n"
    "ds_read_b64_tr_b16 a[182:183], v200 offset:4608\n"
    "v_mfma_f32_16x16x32_bf16 v[76:79], a[144:147], v[16:19], v[164:167]\n"
    "v_exp_f32_e64 v105, v105\n"
    "v_mfma_f32_16x16x32_bf16 v[76:79], a[148:151], v[20:23], v[76:79]\n"
    "ds_read_b64_tr_b16 a[184:185], v238 offset:768\n"
    "v_cvt_pk_bf16_f32 v147, v102, v103\n"
    "v_mfma_f32_16x16x32_bf16 v[76:79], a[152:155], v[24:27], v[76:79]\n"
    "ds_read_b64_tr_b16 a[186:187], v238 offset:4864\n"
    "v_mfma_f32_16x16x32_bf16 v[76:79], a[156:159], v[28:31], v[76:79]\n"
    "v_exp_f32_e64 v106, v106\n"
    "ds_read_b64_tr_b16 a[188:189], v200 offset:768\n"
    "v_mfma_f32_16x16x32_bf16 v[84:87], a[144:147], v[32:35], v[168:171]\n"
    "v_exp_f32_e64 v107, v107\n"
    "v_mfma_f32_16x16x32_bf16 v[84:87], a[148:151], v[36:39], v[84:87]\n"
    "v_cvt_pk_bf16_f32 v148, v104, v105\n"
    "v_mfma_f32_16x16x32_bf16 v[84:87], a[152:155], v[40:43], v[84:87]\n"
    "ds_read_b64_tr_b16 a[190:191], v200 offset:4864\n"
    "v_mfma_f32_16x16x32_bf16 v[84:87], a[156:159], v[44:47], v[84:87]\n"
    "v_exp_f32 v108, v108\n"
    "v_mfma_f32_16x16x32_bf16 v[92:95], a[144:147], v[48:51], v[172:175]\n"
    "v_exp_f32 v109, v109\n"
    "v_mfma_f32_16x16x32_bf16 v[92:95], a[148:151], v[52:55], v[92:95]\n"
    "v_cvt_pk_bf16_f32 v149, v106, v107\n"
    "v_mfma_f32_16x16x32_bf16 v[92:95], a[152:155], v[56:59], v[92:95]\n"
    "v_exp_f32 v110, v110\n"
    "v_mfma_f32_16x16x32_bf16 v[92:95], a[156:159], v[60:63], v[92:95]\n"
    "s_waitcnt vmcnt(1) lgkmcnt(0)\n"
    "s_barrier\n"
    "v_mfma_f32_16x16x32_bf16 a[0:3], a[160:163], v[128:131], a[0:3]\n"
    "s_add_u32 m0, s89, 0\n"
    "v_mfma_f32_16x16x32_bf16 a[8:11], a[160:163], v[132:135], a[8:11]\n"
    "v_exp_f32_e64 v111, v111\n"
    "ds_read_b128 a[128:131], v196 offset:8192\n"
    "v_mfma_f32_16x16x32_bf16 a[16:19], a[160:163], v[136:139], a[16:19]\n"
    "v_cvt_pk_bf16_f32 v150, v108, v109\n"
    "v_mfma_f32_16x16x32_bf16 a[24:27], a[160:163], v[140:143], a[24:27]\n"
    "v_exp_f32_e64 v112, v112\n"
    "v_mfma_f32_16x16x32_bf16 a[4:7], a[164:167], v[128:131], a[4:7]\n"
    "buffer_load_dwordx4 v236, s[48:51], s87 offen lds\n"
    "v_mfma_f32_16x16x32_bf16 a[12:15], a[164:167], v[132:135], a[12:15]\n"
    "v_exp_f32_e64 v113, v113\n"
    "ds_read_b128 a[132:135], v197 offset:8192\n"
    "v_mfma_f32_16x16x32_bf16 a[20:23], a[164:167], v[136:139], a[20:23]\n"
    "v_cvt_pk_bf16_f32 v151, v110, v111\n"
    "v_mfma_f32_16x16x32_bf16 a[28:31], a[164:167], v[140:143], a[28:31]\n"
    "buffer_load_dwordx4 v233, s[48:51], s87 offen offset:1024 lds\n"
    "v_exp_f32_e64 v114, v114\n"
    "v_mfma_f32_16x16x32_bf16 a[208:211], a[204:207], v[128:131], a[208:211]\n"
    "ds_read_b128 a[136:139], v198 offset:8192\n"
    "v_mfma_f32_16x16x32_bf16 a[32:35], a[168:171], v[128:131], a[32:35]\n"
    "v_exp_f32_e64 v115, v115\n"
    "v_mfma_f32_16x16x32_bf16 a[40:43], a[168:171], v[132:135], a[40:43]\n"
    "v_cvt_pk_bf16_f32 v152, v112, v113\n"
    "v_mfma_f32_16x16x32_bf16 a[48:51], a[168:171], v[136:139], a[48:51]\n"
    "s_add_u32 s87, s87, 8192\n"
    "v_exp_f32_e64 v116, v116\n"
    "v_mfma_f32_16x16x32_bf16 a[56:59], a[168:171], v[140:143], a[56:59]\n"
    "ds_read_b128 a[140:143], v199 offset:8192\n"
    "v_mfma_f32_16x16x32_bf16 a[36:39], a[172:175], v[128:131], a[36:39]\n"
    "v_exp_f32_e64 v117, v117\n"
    "v_mfma_f32_16x16x32_bf16 a[44:47], a[172:175], v[132:135], a[44:47]\n"
    "v_cvt_pk_bf16_f32 v153, v114, v115\n"
    "v_mfma_f32_16x16x32_bf16 a[52:55], a[172:175], v[136:139], a[52:55]\n"
    "s_add_u32 m0, s88, 0\n"
    "ds_read_b128 a[144:147], v196 offset:12288\n"
    "v_mfma_f32_16x16x32_bf16 a[60:63], a[172:175], v[140:143], a[60:63]\n"
    "v_exp_f32 v118, v118\n"
    "v_mfma_f32_16x16x32_bf16 a[212:215], a[204:207], v[132:135], a[212:215]\n"
    "v_exp_f32_e64 v119, v119\n"
    "v_mfma_f32_16x16x32_bf16 a[64:67], a[176:179], v[128:131], a[64:67]\n"
    "buffer_load_dwordx4 v234, s[44:47], s86 offen lds\n"
    "v_mfma_f32_16x16x32_bf16 a[72:75], a[176:179], v[132:135], a[72:75]\n"
    "v_cvt_pk_bf16_f32 v154, v116, v117\n"
    "ds_read_b128 a[148:151], v197 offset:12288\n"
    "v_mfma_f32_16x16x32_bf16 a[80:83], a[176:179], v[136:139], a[80:83]\n"
    "v_exp_f32 v120, v120\n"
    "v_mfma_f32_16x16x32_bf16 a[88:91], a[176:179], v[140:143], a[88:91]\n"
    "v_exp_f32 v121, v121\n"
    "v_mfma_f32_16x16x32_bf16 a[68:71], a[180:183], v[128:131], a[68:71]\n"
    "buffer_load_dwordx4 v235, s[44:47], s86 offen offset:1024 lds\n"
    "v_mfma_f32_16x16x32_bf16 a[76:79], a[180:183], v[132:135], a[76:79]\n"
    "ds_read_b128 a[152:155], v198 offset:12288\n"
    "v_cvt_pk_bf16_f32 v155, v118, v119\n"
    "v_mfma_f32_16x16x32_bf16 a[84:87], a[180:183], v[136:139], a[84:87]\n"
    "v_exp_f32 v122, v122\n"
    "v_mfma_f32_16x16x32_bf16 a[92:95], a[180:183], v[140:143], a[92:95]\n"
    "v_exp_f32 v123, v123\n"
    "v_mfma_f32_16x16x32_bf16 a[216:219], a[204:207], v[136:139], a[216:219]\n"
    "ds_read_b128 a[156:159], v199 offset:12288\n"
    "v_mfma_f32_16x16x32_bf16 a[96:99], a[184:187], v[128:131], a[96:99]\n"
    "s_add_u32 s86, s86, 8192\n"
    "v_cvt_pk_bf16_f32 v156, v120, v121\n"
    "v_mfma_f32_16x16x32_bf16 a[104:107], a[184:187], v[132:135], a[104:107]\n"
    "v_exp_f32_e64 v124, v124\n"
    "v_mfma_f32_16x16x32_bf16 a[112:115], a[184:187], v[136:139], a[112:115]\n"
    "global_load_dword v218, v[216:217], off offset:128\n"
    "v_mfma_f32_16x16x32_bf16 a[120:123], a[184:187], v[140:143], a[120:123]\n"
    "v_exp_f32_e64 v125, v125\n"
    "v_mfma_f32_16x16x32_bf16 a[100:103], a[188:191], v[128:131], a[100:103]\n"
    "v_cvt_pk_bf16_f32 v157, v122, v123\n"
    "v_exp_f32_e64 v126, v126\n"
    "v_mfma_f32_16x16x32_bf16 a[108:111], a[188:191], v[132:135], a[108:111]\n"
    "v_lshl_add_u64 v[216:217], v[202:203], 0, v[216:217]\n"
    "v_mfma_f32_16x16x32_bf16 a[116:119], a[188:191], v[136:139], a[116:119]\n"
    "v_exp_f32_e64 v127, v127\n"
    "v_mfma_f32_16x16x32_bf16 a[124:127], a[188:191], v[140:143], a[124:127]\n"
    "v_cvt_pk_bf16_f32 v158, v124, v125\n"
    "v_mfma_f32_16x16x32_bf16 a[220:223], a[204:207], v[140:143], a[220:223]\n"
    "v_cvt_pk_bf16_f32 v159, v126, v127\n"
    "s_waitcnt lgkmcnt(0)\n"
    "v_mfma_f32_16x16x32_bf16 v[96:99], a[128:131], v[0:3], v[160:163]\n"
    "v_exp_f32 v64, v64\n"
    "v_mfma_f32_16x16x32_bf16 v[96:99], a[132:135], v[4:7], v[96:99]\n"
    "ds_read_b64_tr_b16 a[160:161], v238 offset:8192\n"
    "v_mfma_f32_16x16x32_bf16 v[96:99], a[136:139], v[8:11], v[96:99]\n"
    "v_exp_f32_e64 v65, v65\n"
    "v_mfma_f32_16x16x32_bf16 v[96:99], a[140:143], v[12:15], v[96:99]\n"
    "ds_read_b64_tr_b16 a[162:163], v238 offset:12288\n"
    "v_mfma_f32_16x16x32_bf16 v[104:107], a[128:131], v[16:19], v[164:167]\n"
    "v_exp_f32_e64 v66, v66\n"
    "v_mfma_f32_16x16x32_bf16 v[104:107], a[132:135], v[20:23], v[104:107]\n"
    "ds_read_b64_tr_b16 a[164:165], v200 offset:8192\n"
    "v_exp_f32_e64 v67, v67\n"
    "v_mfma_f32_16x16x32_bf16 v[104:107], a[136:139], v[24:27], v[104:107]\n"
    "ds_read_b64_tr_b16 a[166:167], v200 offset:12288\n"
    "v_mfma_f32_16x16x32_bf16 v[104:107], a[140:143], v[28:31], v[104:107]\n"
    "v_cvt_pk_bf16_f32 v128, v64, v65\n"
    "ds_read_b64_tr_b16 a[168:169], v238 offset:8448\n"
    "v_mfma_f32_16x16x32_bf16 v[112:115], a[128:131], v[32:35], v[168:171]\n"
    "v_exp_f32_e64 v68, v68\n"
    "v_mfma_f32_16x16x32_bf16 v[112:115], a[132:135], v[36:39], v[112:115]\n"
    "ds_read_b64_tr_b16 a[170:171], v238 offset:12544\n"
    "v_mfma_f32_16x16x32_bf16 v[112:115], a[136:139], v[40:43], v[112:115]\n"
    "v_exp_f32_e64 v69, v69\n"
    "v_mfma_f32_16x16x32_bf16 v[112:115], a[140:143], v[44:47], v[112:115]\n"
    "ds_read_b64_tr_b16 a[172:173], v200 offset:8448\n"
    "v_mfma_f32_16x16x32_bf16 v[120:123], a[128:131], v[48:51], v[172:175]\n"
    "v_cvt_pk_bf16_f32 v129, v66, v67\n"
    "ds_read_b64_tr_b16 a[174:175], v200 offset:12544\n"
    "v_mfma_f32_16x16x32_bf16 v[120:123], a[132:135], v[52:55], v[120:123]\n"
    "v_exp_f32_e64 v70, v70\n"
    "v_mfma_f32_16x16x32_bf16 v[120:123], a[136:139], v[56:59], v[120:123]\n"
    "ds_read_b64_tr_b16 a[176:177], v238 offset:8704\n"
    "v_mfma_f32_16x16x32_bf16 v[120:123], a[140:143], v[60:63], v[120:123]\n"
    "v_exp_f32_e64 v71, v71\n"
    "v_mfma_f32_16x16x32_bf16 v[100:103], a[144:147], v[0:3], v[160:163]\n"
    "ds_read_b64_tr_b16 a[178:179], v238 offset:12800\n"
    "v_cvt_pk_bf16_f32 v130, v68, v69\n"
    "v_mfma_f32_16x16x32_bf16 v[100:103], a[148:151], v[4:7], v[100:103]\n"
    "ds_read_b64_tr_b16 a[180:181], v200 offset:8704\n"
    "v_mfma_f32_16x16x32_bf16 v[100:103], a[152:155], v[8:11], v[100:103]\n"
    "v_exp_f32_e64 v72, v72\n"
    "v_mfma_f32_16x16x32_bf16 v[100:103], a[156:159], v[12:15], v[100:103]\n"
    "ds_read_b64_tr_b16 a[182:183], v200 offset:12800\n"
    "v_mfma_f32_16x16x32_bf16 v[108:111], a[144:147], v[16:19], v[164:167]\n"
    "v_exp_f32_e64 v73, v73\n"
    "v_mfma_f32_16x16x32_bf16 v[108:111], a[148:151], v[20:23], v[108:111]\n"
    "ds_read_b64_tr_b16 a[184:185], v238 offset:8960\n"
    "v_cvt_pk_bf16_f32 v131, v70, v71\n"
    "v_mfma_f32_16x16x32_bf16 v[108:111], a[152:155], v[24:27], v[108:111]\n"
    "ds_read_b64_tr_b16 a[186:187], v238 offset:13056\n"
    "v_mfma_f32_16x16x32_bf16 v[108:111], a[156:159], v[28:31], v[108:111]\n"
    "v_exp_f32_e64 v74, v74\n"
    "ds_read_b64_tr_b16 a[188:189], v200 offset:8960\n"
    "v_mfma_f32_16x16x32_bf16 v[116:119], a[144:147], v[32:35], v[168:171]\n"
    "v_exp_f32_e64 v75, v75\n"
    "v_mfma_f32_16x16x32_bf16 v[116:119], a[148:151], v[36:39], v[116:119]\n"
    "v_cvt_pk_bf16_f32 v132, v72, v73\n"
    "v_mfma_f32_16x16x32_bf16 v[116:119], a[152:155], v[40:43], v[116:119]\n"
    "ds_read_b64_tr_b16 a[190:191], v200 offset:13056\n"
    "v_mfma_f32_16x16x32_bf16 v[116:119], a[156:159], v[44:47], v[116:119]\n"
    "v_exp_f32 v76, v76\n"
    "v_mfma_f32_16x16x32_bf16 v[124:127], a[144:147], v[48:51], v[172:175]\n"
    "v_exp_f32 v77, v77\n"
    "v_mfma_f32_16x16x32_bf16 v[124:127], a[148:151], v[52:55], v[124:127]\n"
    "v_cvt_pk_bf16_f32 v133, v74, v75\n"
    "v_mfma_f32_16x16x32_bf16 v[124:127], a[152:155], v[56:59], v[124:127]\n"
    "v_exp_f32 v78, v78\n"
    "v_mfma_f32_16x16x32_bf16 v[124:127], a[156:159], v[60:63], v[124:127]\n"
    "s_waitcnt vmcnt(1) lgkmcnt(0)\n"
    "s_barrier\n"
    "v_mfma_f32_16x16x32_bf16 a[0:3], a[160:163], v[144:147], a[0:3]\n"
    "s_add_u32 m0, s89, 8192\n"
    "v_mfma_f32_16x16x32_bf16 a[8:11], a[160:163], v[148:151], a[8:11]\n"
    "v_exp_f32 v79, v79\n"
    "v_mfma_f32_16x16x32_bf16 a[16:19], a[160:163], v[152:155], a[16:19]\n"
    "ds_read_b128 a[128:131], v196 offset:0\n"
    "v_cvt_pk_bf16_f32 v134, v76, v77\n"
    "v_mfma_f32_16x16x32_bf16 a[24:27], a[160:163], v[156:159], a[24:27]\n"
    "v_exp_f32_e64 v80, v80\n"
    "v_mfma_f32_16x16x32_bf16 a[4:7], a[164:167], v[144:147], a[4:7]\n"
    "buffer_load_dwordx4 v236, s[48:51], s87 offen lds\n"
    "v_mfma_f32_16x16x32_bf16 a[12:15], a[164:167], v[148:151], a[12:15]\n"
    "v_exp_f32_e64 v81, v81\n"
    "v_mfma_f32_16x16x32_bf16 a[20:23], a[164:167], v[152:155], a[20:23]\n"
    "ds_read_b128 a[132:135], v197 offset:0\n"
    "v_cvt_pk_bf16_f32 v135, v78, v79\n"
    "v_mfma_f32_16x16x32_bf16 a[28:31], a[164:167], v[156:159], a[28:31]\n"
    "v_exp_f32_e64 v82, v82\n"
    "v_mfma_f32_16x16x32_bf16 a[208:211], a[204:207], v[144:147], a[208:211]\n"
    "buffer_load_dwordx4 v233, s[48:51], s87 offen offset:1024 lds\n"
    "ds_read_b128 a[136:139], v198 offset:0\n"
    "v_mfma_f32_16x16x32_bf16 a[32:35], a[168:171], v[144:147], a[32:35]\n"
    "v_exp_f32_e64 v83, v83\n"
    "v_mfma_f32_16x16x32_bf16 a[40:43], a[168:171], v[148:151], a[40:43]\n"
    "v_cvt_pk_bf16_f32 v136, v80, v81\n"
    "v_mfma_f32_16x16x32_bf16 a[48:51], a[168:171], v[152:155], a[48:51]\n"
    "v_exp_f32_e64 v84, v84\n"
    "v_mfma_f32_16x16x32_bf16 a[56:59], a[168:171], v[156:159], a[56:59]\n"
    "ds_read_b128 a[140:143], v199 offset:0\n"
    "v_mfma_f32_16x16x32_bf16 a[36:39], a[172:175], v[144:147], a[36:39]\n"
    "s_add_u32 s87, s87, 8192\n"
    "v_exp_f32_e64 v85, v85\n"
    "v_mfma_f32_16x16x32_bf16 a[44:47], a[172:175], v[148:151], a[44:47]\n"
    "v_cvt_pk_bf16_f32 v137, v82, v83\n"
    "v_mfma_f32_16x16x32_bf16 a[52:55], a[172:175], v[152:155], a[52:55]\n"
    "ds_read_b128 a[144:147], v196 offset:4096\n"
    "v_mfma_f32_16x16x32_bf16 a[60:63], a[172:175], v[156:159], a[60:63]\n"
    "v_exp_f32_e64 v86, v86\n"
    "v_mfma_f32_16x16x32_bf16 a[212:215], a[204:207], v[148:151], a[212:215]\n"
    "s_add_u32 m0, s88, 8192\n"
    "v_exp_f32_e64 v87, v87\n"
    "v_mfma_f32_16x16x32_bf16 a[64:67], a[176:179], v[144:147], a[64:67]\n"
    "v_cvt_pk_bf16_f32 v138, v84, v85\n"
    "v_mfma_f32_16x16x32_bf16 a[72:75], a[176:179], v[148:151], a[72:75]\n"
    "ds_read_b128 a[148:151], v197 offset:4096\n"
    "v_mfma_f32_16x16x32_bf16 a[80:83], a[176:179], v[152:155], a[80:83]\n"
    "v_exp_f32_e64 v88, v88\n"
    "v_mfma_f32_16x16x32_bf16 a[88:91], a[176:179], v[156:159], a[88:91]\n"
    "buffer_load_dwordx4 v234, s[44:47], s86 offen lds\n"
    "v_mfma_f32_16x16x32_bf16 a[68:71], a[180:183], v[144:147], a[68:71]\n"
    "v_exp_f32_e64 v89, v89\n"
    "ds_read_b128 a[152:155], v198 offset:4096\n"
    "v_mfma_f32_16x16x32_bf16 a[76:79], a[180:183], v[148:151], a[76:79]\n"
    "v_cvt_pk_bf16_f32 v139, v86, v87\n"
    "v_mfma_f32_16x16x32_bf16 a[84:87], a[180:183], v[152:155], a[84:87]\n"
    "v_exp_f32_e64 v90, v90\n"
    "v_mfma_f32_16x16x32_bf16 a[92:95], a[180:183], v[156:159], a[92:95]\n"
    "buffer_load_dwordx4 v235, s[44:47], s86 offen offset:1024 lds\n"
    "v_mfma_f32_16x16x32_bf16 a[216:219], a[204:207], v[152:155], a[216:219]\n"
    "v_exp_f32_e64 v91, v91\n"
    "ds_read_b128 a[156:159], v199 offset:4096\n"
    "v_mfma_f32_16x16x32_bf16 a[96:99], a[184:187], v[144:147], a[96:99]\n"
    "v_cvt_pk_bf16_f32 v140, v88, v89\n"
    "v_mfma_f32_16x16x32_bf16 a[104:107], a[184:187], v[148:151], a[104:107]\n"
    "v_exp_f32_e64 v92, v92\n"
    "v_mfma_f32_16x16x32_bf16 a[112:115], a[184:187], v[152:155], a[112:115]\n"
    "s_add_u32 s86, s86, 8192\n"
    "v_mfma_f32_16x16x32_bf16 a[120:123], a[184:187], v[156:159], a[120:123]\n"
    "v_exp_f32_e64 v93, v93\n"
    "v_mfma_f32_16x16x32_bf16 a[100:103], a[188:191], v[144:147], a[100:103]\n"
    "v_cvt_pk_bf16_f32 v141, v90, v91\n"
    "v_exp_f32_e64 v94, v94\n"
    "v_mfma_f32_16x16x32_bf16 a[108:111], a[188:191], v[148:151], a[108:111]\n"
    "global_load_dword v218, v[216:217], off offset:0\n"
    "v_mfma_f32_16x16x32_bf16 a[116:119], a[188:191], v[152:155], a[116:119]\n"
    "v_exp_f32_e64 v95, v95\n"
    "v_mfma_f32_16x16x32_bf16 a[124:127], a[188:191], v[156:159], a[124:127]\n"
    "v_cvt_pk_bf16_f32 v142, v92, v93\n"
    "v_mfma_f32_16x16x32_bf16 a[220:223], a[204:207], v[156:159], a[220:223]\n"
    "v_cvt_pk_bf16_f32 v143, v94, v95\n"
    "s_waitcnt lgkmcnt(0)\n"
    "s_sub_u32 s90, s90, 1\n"
    "s_cmp_lg_u32 s90, 0\n"
    "s_cbranch_scc1 attn_loop\n"
    "ds_read_b64_tr_b16 a[160:161], v238 offset:0\n"
    "ds_read_b64_tr_b16 a[162:163], v238 offset:4096\n"
    "ds_read_b64_tr_b16 a[164:165], v200 offset:0\n"
    "ds_read_b64_tr_b16 a[166:167], v200 offset:4096\n"
    "ds_read_b64_tr_b16 a[168:169], v238 offset:256\n"
    "ds_read_b64_tr_b16 a[170:171], v238 offset:4352\n"
    "ds_read_b64_tr_b16 a[172:173], v200 offset:256\n"
    "ds_read_b64_tr_b16 a[174:175], v200 offset:4352\n"
    "ds_read_b64_tr_b16 a[176:177], v238 offset:512\n"
    "ds_read_b64_tr_b16 a[178:179], v238 offset:4608\n"
    "ds_read_b64_tr_b16 a[180:181], v200 offset:512\n"
    "ds_read_b64_tr_b16 a[182:183], v200 offset:4608\n"
    "ds_read_b64_tr_b16 a[184:185], v238 offset:768\n"
    "ds_read_b64_tr_b16 a[186:187], v238 offset:4864\n"
    "ds_read_b64_tr_b16 a[188:189], v200 offset:768\n"
    "ds_read_b64_tr_b16 a[190:191], v200 offset:4864\n"
    "v_exp_f32 v96, v96\n"
    "v_exp_f32 v97, v97\n"
    "v_exp_f32 v98, v98\n"
    "v_exp_f32 v99, v99\n"
    "v_cvt_pk_bf16_f32 v144, v96, v97\n"
    "v_exp_f32_e64 v100, v100\n"
    "v_accvgpr_read_b32 v176, a196\n"
    "v_accvgpr_read_b32 v177, a197\n"
    "v_accvgpr_read_b32 v178, a198\n"
    "v_accvgpr_read_b32 v179, a199\n"
    "v_accvgpr_read_b32 v180, a200\n"
    "v_accvgpr_read_b32 v181, a201\n"
    "v_accvgpr_read_b32 v182, a202\n"
    "v_accvgpr_read_b32 v183, a203\n"
    "s_mov_b32 s73, 0x05040100\n"
    "s_mov_b32 s97, 0x07060302\n"
    "v_perm_b32 v160, v180, v176, s73\n"
    "v_perm_b32 v161, v180, v176, s97\n"
    "v_perm_b32 v162, v181, v177, s73\n"
    "v_perm_b32 v163, v181, v177, s97\n"
    "v_perm_b32 v164, v182, v178, s73\n"
    "v_perm_b32 v165, v182, v178, s97\n"
    "v_perm_b32 v166, v183, v179, s73\n"
    "v_perm_b32 v167, v183, v179, s97\n"
    "v_dot2_f32_bf16 v160, v160, v252, 0\n"
    "v_dot2_f32_bf16 v161, v161, v252, 0\n"
    "v_dot2_f32_bf16 v162, v162, v252, 0\n"
    "v_dot2_f32_bf16 v163, v163, v252, 0\n"
    "v_dot2_f32_bf16 v164, v164, v252, 0\n"
    "v_dot2_f32_bf16 v165, v165, v252, 0\n"
    "v_dot2_f32_bf16 v166, v166, v252, 0\n"
    "v_dot2_f32_bf16 v167, v167, v252, 0\n"
    "v_lshlrev_b32 v177, 4, v254\n"
    "s_lshl_b32 s73, s30, 2\n"
    "v_add_u32 v177, s73, v177\n"
    "v_permlane32_swap_b32 v160, v164\n"
    "v_permlane32_swap_b32 v161, v165\n"
    "v_permlane32_swap_b32 v162, v166\n"
    "v_permlane32_swap_b32 v163, v167\n"
    "v_add_f32 v160, v160, v164\n"
    "v_add_f32 v161, v161, v165\n"
    "v_add_f32 v162, v162, v166\n"
    "v_add_f32 v163, v163, v167\n"
    "v_exp_f32 v101, v101\n"
    "v_permlane16_swap_b32 v160, v162\n"
    "v_permlane16_swap_b32 v161, v163\n"
    "v_add_f32 v160, v160, v162\n"
    "v_add_f32 v161, v161, v163\n"
    "v_cvt_pk_bf16_f32 v145, v98, v99\n"
    "v_add_f32_dpp v176, v160, v160 row_ror:8 row_mask:0xf bank_mask:0x3\n"
    "v_exp_f32 v102, v102\n"
    "v_exp_f32 v103, v103\n"
    "v_add_f32_dpp v176, v161, v161 row_ror:8 row_mask:0xf bank_mask:0xc\n"
    "v_cvt_pk_bf16_f32 v146, v100, v101\n"
    "v_exp_f32 v104, v104\n"
    "v_add_f32_dpp v176, v176, v176 row_half_mirror row_mask:0xf bank_mask:0xf\n"
    "v_exp_f32 v105, v105\n"
    "v_cvt_pk_bf16_f32 v147, v102, v103\n"
    "v_add_f32_dpp v176, v176, v176 quad_perm:[1,0,3,2] row_mask:0xf bank_mask:0xf\n"
    "v_exp_f32 v106, v106\n"
    "v_exp_f32 v107, v107\n"
    "v_add_f32_dpp v176, v176, v176 quad_perm:[2,3,0,1] row_mask:0xf bank_mask:0xf\n"
    "ds_write_b32 v177, v176 offset:14336\n"
    "s_waitcnt vmcnt(1) lgkmcnt(0)\n"
    "s_barrier\n"
    "s_lshr_b32 s97, s36, 6\n"
    "s_cmp_lg_u32 s30, 1\n"
    "s_cbranch_scc1 pub2\n"
    "v_and_b32 v176, 0x70, v224\n"
    "ds_read_b128 v[168:171], v176 offset:14336\n"
    "s_lshl_b32 s73, s36, 5\n"
    "s_add_u32 s73, s73, 8192\n"
    "s_cmp_eq_u32 s97, 2\n"
    "s_cselect_b32 s96, 1.0, 0\n"
    "v_lshrrev_b32 v177, 2, v224\n"
    "v_and_b32 v177, 16, v177\n"
    "s_waitcnt lgkmcnt(0)\n"
    "v_pk_add_f32 v[168:169], v[168:169], v[170:171]\n"
    "v_add_f32 v168, v168, v169\n"
    "v_cvt_pk_bf16_f32 v168, v168, v168\n"
    "v_lshlrev_b32 v168, 16, v168\n"
    "v_add_f32 v168, s96, v168\n"
    "v_mov_b32 v172, s94\n"
    "v_mov_b32 v174, s95\n"
    "v_mov_b32_dpp v169, v168 quad_perm:[1,1,3,3] row_mask:0xf bank_mask:0xf\n"
    "v_cvt_pk_bf16_f32 v173, v168, v169\n"
    "s_nop 1\n"
    "v_mov_b32_dpp v175, v173 quad_perm:[2,2,2,2] row_mask:0xf bank_mask:0xf\n"
    "s_mov_b64 exec, 17\n"
    "buffer_store_dwordx4 v[172:175], v177, s[60:63], s73 offen sc1\n"
    "s_mov_b64 exec, -1\n"
    "pub2:\n"
    "global_load_dword v218, v[216:217], off offset:128\n"
    "v_mfma_f32_16x16x32_bf16 a[0:3], a[160:163], v[128:131], a[0:3]\n"
    "v_cvt_pk_bf16_f32 v148, v104, v105\n"
    "ds_read_b64_tr_b16 a[128:129], v238 offset:8192\n"
    "v_mfma_f32_16x16x32_bf16 a[8:11], a[160:163], v[132:135], a[8:11]\n"
    "v_exp_f32 v108, v108\n"
    "v_mfma_f32_16x16x32_bf16 a[16:19], a[160:163], v[136:139], a[16:19]\n"
    "v_exp_f32_e64 v109, v109\n"
    "v_mfma_f32_16x16x32_bf16 a[24:27], a[160:163], v[140:143], a[24:27]\n"
    "ds_read_b64_tr_b16 a[130:131], v238 offset:12288\n"
    "v_cvt_pk_bf16_f32 v149, v106, v107\n"
    "v_mfma_f32_16x16x32_bf16 a[4:7], a[164:167], v[128:131], a[4:7]\n"
    "v_exp_f32_e64 v110, v110\n"
    "v_mfma_f32_16x16x32_bf16 a[12:15], a[164:167], v[132:135], a[12:15]\n"
    "ds_read_b64_tr_b16 a[132:133], v200 offset:8192\n"
    "v_mfma_f32_16x16x32_bf16 a[20:23], a[164:167], v[136:139], a[20:23]\n"
    "v_exp_f32_e64 v111, v111\n"
    "v_mfma_f32_16x16x32_bf16 a[28:31], a[164:167], v[140:143], a[28:31]\n"
    "v_cvt_pk_bf16_f32 v150, v108, v109\n"
    "ds_read_b64_tr_b16 a[134:135], v200 offset:12288\n"
    "v_mfma_f32_16x16x32_bf16 a[32:35], a[168:171], v[128:131], a[32:35]\n"
    "v_exp_f32 v112, v112\n"
    "v_mfma_f32_16x16x32_bf16 a[40:43], a[168:171], v[132:135], a[40:43]\n"
    "v_exp_f32 v113, v113\n"
    "v_mfma_f32_16x16x32_bf16 a[48:51], a[168:171], v[136:139], a[48:51]\n"
    "ds_read_b64_tr_b16 a[136:137], v238 offset:8448\n"
    "v_cvt_pk_bf16_f32 v151, v110, v111\n"
    "v_mfma_f32_16x16x32_bf16 a[56:59], a[168:171], v[140:143], a[56:59]\n"
    "v_exp_f32_e64 v114, v114\n"
    "v_mfma_f32_16x16x32_bf16 a[36:39], a[172:175], v[128:131], a[36:39]\n"
    "ds_read_b64_tr_b16 a[138:139], v238 offset:12544\n"
    "v_mfma_f32_16x16x32_bf16 a[44:47], a[172:175], v[132:135], a[44:47]\n"
    "v_exp_f32_e64 v115, v115\n"
    "v_mfma_f32_16x16x32_bf16 a[52:55], a[172:175], v[136:139], a[52:55]\n"
    "v_cvt_pk_bf16_f32 v152, v112, v113\n"
    "ds_read_b64_tr_b16 a[140:141], v200 offset:8448\n"
    "v_mfma_f32_16x16x32_bf16 a[60:63], a[172:175], v[140:143], a[60:63]\n"
    "v_exp_f32 v116, v116\n"
    "v_mfma_f32_16x16x32_bf16 a[64:67], a[176:179], v[128:131], a[64:67]\n"
    "v_exp_f32 v117, v117\n"
    "v_mfma_f32_16x16x32_bf16 a[72:75], a[176:179], v[132:135], a[72:75]\n"
    "ds_read_b64_tr_b16 a[142:143], v200 offset:12544\n"
    "v_cvt_pk_bf16_f32 v153, v114, v115\n"
    "v_mfma_f32_16x16x32_bf16 a[80:83], a[176:179], v[136:139], a[80:83]\n"
    "ds_read_b64_tr_b16 a[144:145], v238 offset:8704\n"
    "v_mfma_f32_16x16x32_bf16 a[88:91], a[176:179], v[140:143], a[88:91]\n"
    "v_exp_f32 v118, v118\n"
    "v_mfma_f32_16x16x32_bf16 a[68:71], a[180:183], v[128:131], a[68:71]\n"
    "v_exp_f32 v119, v119\n"
    "ds_read_b64_tr_b16 a[146:147], v238 offset:12800\n"
    "v_mfma_f32_16x16x32_bf16 a[76:79], a[180:183], v[132:135], a[76:79]\n"
    "v_cvt_pk_bf16_f32 v154, v116, v117\n"
    "v_mfma_f32_16x16x32_bf16 a[84:87], a[180:183], v[136:139], a[84:87]\n"
    "v_exp_f32_e64 v120, v120\n"
    "v_mfma_f32_16x16x32_bf16 a[92:95], a[180:183], v[140:143], a[92:95]\n"
    "ds_read_b64_tr_b16 a[148:149], v200 offset:8704\n"
    "v_exp_f32_e64 v121, v121\n"
    "v_mfma_f32_16x16x32_bf16 a[96:99], a[184:187], v[128:131], a[96:99]\n"
    "v_cvt_pk_bf16_f32 v155, v118, v119\n"
    "v_mfma_f32_16x16x32_bf16 a[104:107], a[184:187], v[132:135], a[104:107]\n"
    "ds_read_b64_tr_b16 a[150:151], v200 offset:12800\n"
    "v_exp_f32 v122, v122\n"
    "v_mfma_f32_16x16x32_bf16 a[112:115], a[184:187], v[136:139], a[112:115]\n"
    "v_exp_f32 v123, v123\n"
    "v_mfma_f32_16x16x32_bf16 a[120:123], a[184:187], v[140:143], a[120:123]\n"
    "ds_read_b64_tr_b16 a[152:153], v238 offset:8960\n"
    "v_mfma_f32_16x16x32_bf16 a[100:103], a[188:191], v[128:131], a[100:103]\n"
    "v_cvt_pk_bf16_f32 v156, v120, v121\n"
    "v_mfma_f32_16x16x32_bf16 a[108:111], a[188:191], v[132:135], a[108:111]\n"
    "v_exp_f32_e64 v124, v124\n"
    "ds_read_b64_tr_b16 a[154:155], v238 offset:13056\n"
    "v_mfma_f32_16x16x32_bf16 a[116:119], a[188:191], v[136:139], a[116:119]\n"
    "v_exp_f32_e64 v125, v125\n"
    "v_mfma_f32_16x16x32_bf16 a[124:127], a[188:191], v[140:143], a[124:127]\n"
    "v_cvt_pk_bf16_f32 v157, v122, v123\n"
    "v_mfma_f32_16x16x32_bf16 a[208:211], a[204:207], v[128:131], a[208:211]\n"
    "ds_read_b64_tr_b16 a[156:157], v200 offset:8960\n"
    "v_exp_f32 v126, v126\n"
    "v_mfma_f32_16x16x32_bf16 a[212:215], a[204:207], v[132:135], a[212:215]\n"
    "v_exp_f32 v127, v127\n"
    "v_mfma_f32_16x16x32_bf16 a[216:219], a[204:207], v[136:139], a[216:219]\n"
    "ds_read_b64_tr_b16 a[158:159], v200 offset:13056\n"
    "v_mfma_f32_16x16x32_bf16 a[220:223], a[204:207], v[140:143], a[220:223]\n"
    "v_cvt_pk_bf16_f32 v158, v124, v125\n"
    "v_cvt_pk_bf16_f32 v159, v126, v127\n"
    "s_waitcnt lgkmcnt(0)\n"
    "s_nop 0\n"
    "v_mfma_f32_16x16x32_bf16 a[208:211], a[204:207], v[144:147], a[208:211]\n"
    "v_mfma_f32_16x16x32_bf16 a[212:215], a[204:207], v[148:151], a[212:215]\n"
    "v_mfma_f32_16x16x32_bf16 a[216:219], a[204:207], v[152:155], a[216:219]\n"
    "v_mfma_f32_16x16x32_bf16 a[220:223], a[204:207], v[156:159], a[220:223]\n"
    "v_mfma_f32_16x16x32_bf16 a[0:3], a[128:131], v[144:147], a[0:3]\n"
    "v_mfma_f32_16x16x32_bf16 a[8:11], a[128:131], v[148:151], a[8:11]\n"
    "v_mfma_f32_16x16x32_bf16 a[16:19], a[128:131], v[152:155], a[16:19]\n"
    "v_mfma_f32_16x16x32_bf16 a[24:27], a[128:131], v[156:159], a[24:27]\n"
    "v_mfma_f32_16x16x32_bf16 a[4:7], a[132:135], v[144:147], a[4:7]\n"
    "v_mfma_f32_16x16x32_bf16 a[12:15], a[132:135], v[148:151], a[12:15]\n"
    "s_nop 1\n"
    "v_accvgpr_read_b32 v192, a208\n"
    "v_accvgpr_read_b32 v193, a212\n"
    "v_accvgpr_read_b32 v194, a216\n"
    "v_accvgpr_read_b32 v195, a220\n"
    "v_add_f32 v168, v192, v193\n"
    "v_add_f32 v169, v194, v195\n"
    "v_add_f32 v168, v168, v169\n"
    "v_cmp_ngt_f32 vcc, 0x6c800000, v168\n"
    "s_cbranch_vccz ov_no\n"
    "v_mov_b32 v168, 0\n"
    "v_mov_b32_e64 v169, 1\n"
    "ds_write_b32 v168, v169 offset:0\n"
    "ds_write_b32 v168, v169 offset:8192\n"
    "ds_write_b32 v168, v169 offset:16384\n"
    "ds_write_b32 v168, v169 offset:24576\n"
    "s_waitcnt lgkmcnt(0)\n"
    "ov_no:\n"
    "v_mfma_f32_16x16x32_bf16 a[20:23], a[132:135], v[152:155], a[20:23]\n"
    "v_rcp_f32 v210, v192\n"
    "v_rcp_f32 v211, v193\n"
    "v_rcp_f32 v212, v194\n"
    "v_rcp_f32 v213, v195\n"
    "s_barrier\n"
    "ds_read_b32 v215, v214\n"
    "v_mfma_f32_16x16x32_bf16 a[28:31], a[132:135], v[156:159], a[28:31]\n"
    "v_mfma_f32_16x16x32_bf16 a[32:35], a[136:139], v[144:147], a[32:35]\n"
    "s_lshl_b32 s8, s30, 13\n"
    "s_add_u32 s8, s8, 131072\n"
    "v_mfma_f32_16x16x32_bf16 a[40:43], a[136:139], v[148:151], a[40:43]\n"
    "v_add_u32 v202, s8, v232\n"
    "v_xor_b32_e64 v203, 32, v202\n"
    "v_mfma_f32_16x16x32_bf16 a[48:51], a[136:139], v[152:155], a[48:51]\n"
    "v_mfma_f32_16x16x32_bf16 a[56:59], a[136:139], v[156:159], a[56:59]\n"
    "s_waitcnt lgkmcnt(0)\n"
    "v_readfirstlane_b32 s73, v215\n"
    "v_add_u32_e64 v214, v214, v231\n"
    "s_andn2_b32 s73, s73, s91\n"
    "s_cbranch_scc1 attn_redo\n"
    "v_mfma_f32_16x16x32_bf16 a[36:39], a[140:143], v[144:147], a[36:39]\n"
    "v_accvgpr_read_b32 v64, a0\n"
    "v_accvgpr_read_b32 v65, a1\n"
    "v_accvgpr_read_b32 v66, a2\n"
    "v_mfma_f32_16x16x32_bf16 a[44:47], a[140:143], v[148:151], a[44:47]\n"
    "v_accvgpr_read_b32 v67, a3\n"
    "v_accvgpr_read_b32 v68, a4\n"
    "v_accvgpr_read_b32 v69, a5\n"
    "v_mfma_f32_16x16x32_bf16 a[52:55], a[140:143], v[152:155], a[52:55]\n"
    "v_accvgpr_read_b32 v70, a6\n"
    "v_accvgpr_read_b32 v71, a7\n"
    "v_accvgpr_read_b32 v72, a8\n"
    "v_mfma_f32_16x16x32_bf16 a[60:63], a[140:143], v[156:159], a[60:63]\n"
    "v_accvgpr_read_b32 v73, a9\n"
    "v_accvgpr_read_b32 v74, a10\n"
    "v_accvgpr_read_b32 v75, a11\n"
    "v_mfma_f32_16x16x32_bf16 a[64:67], a[144:147], v[144:147], a[64:67]\n"
    "v_accvgpr_read_b32 v76, a12\n"
    "v_accvgpr_read_b32 v77, a13\n"
    "v_accvgpr_read_b32 v78, a14\n"
    "v_mfma_f32_16x16x32_bf16 a[72:75], a[144:147], v[148:151], a[72:75]\n"
    "v_accvgpr_read_b32 v79, a15\n"
    "v_mul_f32 v64, v210, v64\n"
    "v_mul_f32 v65, v210, v65\n"
    "v_mfma_f32_16x16x32_bf16 a[80:83], a[144:147], v[152:155], a[80:83]\n"
    "v_mul_f32 v66, v210, v66\n"
    "v_mul_f32 v67, v210, v67\n"
    "v_mul_f32 v68, v210, v68\n"
    "v_mfma_f32_16x16x32_bf16 a[88:91], a[144:147], v[156:159], a[88:91]\n"
    "v_mul_f32 v69, v210, v69\n"
    "v_mul_f32 v70, v210, v70\n"
    "v_mul_f32 v71, v210, v71\n"
    "v_mfma_f32_16x16x32_bf16 a[68:71], a[148:151], v[144:147], a[68:71]\n"
    "v_mul_f32 v72, v211, v72\n"
    "v_mul_f32 v73, v211, v73\n"
    "v_mul_f32 v74, v211, v74\n"
    "v_mfma_f32_16x16x32_bf16 a[76:79], a[148:151], v[148:151], a[76:79]\n"
    "v_mul_f32 v75, v211, v75\n"
    "v_mul_f32 v76, v211, v76\n"
    "v_mul_f32 v77, v211, v77\n"
    "v_mfma_f32_16x16x32_bf16 a[84:87], a[148:151], v[152:155], a[84:87]\n"
    "v_mul_f32 v78, v211, v78\n"
    "v_mul_f32 v79, v211, v79\n"
    "v_cvt_pk_bf16_f32 v64, v64, v65\n"
    "v_mfma_f32_16x16x32_bf16 a[92:95], a[148:151], v[156:159], a[92:95]\n"
    "v_cvt_pk_bf16_f32 v65, v66, v67\n"
    "v_cvt_pk_bf16_f32 v66, v68, v69\n"
    "v_cvt_pk_bf16_f32 v67, v70, v71\n"
    "v_mfma_f32_16x16x32_bf16 a[96:99], a[152:155], v[144:147], a[96:99]\n"
    "v_cvt_pk_bf16_f32 v68, v72, v73\n"
    "v_cvt_pk_bf16_f32 v69, v74, v75\n"
    "v_cvt_pk_bf16_f32 v70, v76, v77\n"
    "v_mfma_f32_16x16x32_bf16 a[104:107], a[152:155], v[148:151], a[104:107]\n"
    "v_cvt_pk_bf16_f32 v71, v78, v79\n"
    "ds_write_b128 v214, v[64:67] offset:0\n"
    "ds_write_b128 v214, v[68:71] offset:256\n"
    "v_mfma_f32_16x16x32_bf16 a[112:115], a[152:155], v[152:155], a[112:115]\n"
    "v_accvgpr_read_b32 v80, a16\n"
    "v_accvgpr_read_b32 v81, a17\n"
    "v_accvgpr_read_b32 v82, a18\n"
    "v_mfma_f32_16x16x32_bf16 a[120:123], a[152:155], v[156:159], a[120:123]\n"
    "v_accvgpr_read_b32 v83, a19\n"
    "v_accvgpr_read_b32 v84, a20\n"
    "v_accvgpr_read_b32 v85, a21\n"
    "v_mfma_f32_16x16x32_bf16 a[100:103], a[156:159], v[144:147], a[100:103]\n"
    "v_accvgpr_read_b32 v86, a22\n"
    "v_accvgpr_read_b32 v87, a23\n"
    "v_accvgpr_read_b32 v88, a24\n"
    "v_mfma_f32_16x16x32_bf16 a[108:111], a[156:159], v[148:151], a[108:111]\n"
    "v_accvgpr_read_b32 v89, a25\n"
    "v_accvgpr_read_b32 v90, a26\n"
    "v_accvgpr_read_b32 v91, a27\n"
    "v_mfma_f32_16x16x32_bf16 a[116:119], a[156:159], v[152:155], a[116:119]\n"
    "v_accvgpr_read_b32 v92, a28\n"
    "v_accvgpr_read_b32 v93, a29\n"
    "v_accvgpr_read_b32 v94, a30\n"
    "v_mfma_f32_16x16x32_bf16 a[124:127], a[156:159], v[156:159], a[124:127]\n"
    "v_accvgpr_read_b32 v95, a31\n"
    "v_mul_f32 v80, v212, v80\n"
    "v_mul_f32 v81, v212, v81\n"
    "v_mul_f32 v82, v212, v82\n"
    "v_mul_f32 v83, v212, v83\n"
    "v_mul_f32 v84, v212, v84\n"
    "v_mul_f32 v85, v212, v85\n"
    "v_mul_f32 v86, v212, v86\n"
    "v_mul_f32 v87, v212, v87\n"
    "v_mul_f32 v88, v213, v88\n"
    "v_mul_f32 v89, v213, v89\n"
    "v_mul_f32 v90, v213, v90\n"
    "v_mul_f32 v91, v213, v91\n"
    "v_mul_f32 v92, v213, v92\n"
    "v_mul_f32 v93, v213, v93\n"
    "v_mul_f32 v94, v213, v94\n"
    "v_mul_f32 v95, v213, v95\n"
    "v_cvt_pk_bf16_f32 v80, v80, v81\n"
    "v_cvt_pk_bf16_f32 v81, v82, v83\n"
    "s_add_u32 m0, s8, 0\n"
    "s_mov_b32 s73, 131072\n"
    "buffer_load_dwordx4 v204, s[40:43], s73 offen offset:0 lds\n"
    "s_nop 0\n"
    "v_cvt_pk_bf16_f32 v82, v84, v85\n"
    "v_cvt_pk_bf16_f32 v83, v86, v87\n"
    "v_cvt_pk_bf16_f32 v84, v88, v89\n"
    "v_cvt_pk_bf16_f32 v85, v90, v91\n"
    "v_cvt_pk_bf16_f32 v86, v92, v93\n"
    "v_cvt_pk_bf16_f32 v87, v94, v95\n"
    "ds_write_b128 v214, v[80:83] offset:32768\n"
    "ds_write_b128 v214, v[84:87] offset:33024\n"
    "v_accvgpr_read_b32 v32, a32\n"
    "v_accvgpr_read_b32 v33, a33\n"
    "v_accvgpr_read_b32 v34, a34\n"
    "v_accvgpr_read_b32 v35, a35\n"
    "v_accvgpr_read_b32 v36, a36\n"
    "v_accvgpr_read_b32 v37, a37\n"
    "v_accvgpr_read_b32 v38, a38\n"
    "v_accvgpr_read_b32 v39, a39\n"
    "buffer_load_dwordx4 v205, s[40:43], s73 offen offset:1024 lds\n"
    "v_accvgpr_read_b32 v40, a40\n"
    "v_accvgpr_read_b32 v41, a41\n"
    "v_accvgpr_read_b32 v42, a42\n"
    "v_accvgpr_read_b32 v43, a43\n"
    "v_accvgpr_read_b32 v44, a44\n"
    "v_accvgpr_read_b32 v45, a45\n"
    "v_accvgpr_read_b32 v46, a46\n"
    "v_accvgpr_read_b32 v47, a47\n"
    "v_pk_mul_f32 v[32:33], v[32:33], v[210:211] op_sel_hi:[1,0]\n"
    "v_pk_mul_f32 v[34:35], v[34:35], v[210:211] op_sel_hi:[1,0]\n"
    "v_pk_mul_f32 v[36:37], v[36:37], v[210:211] op_sel_hi:[1,0]\n"
    "v_pk_mul_f32 v[38:39], v[38:39], v[210:211] op_sel_hi:[1,0]\n"
    "v_pk_mul_f32 v[40:41], v[40:41], v[210:211] op_sel:[0,1]\n"
    "v_pk_mul_f32 v[42:43], v[42:43], v[210:211] op_sel:[0,1]\n"
    "v_pk_mul_f32 v[44:45], v[44:45], v[210:211] op_sel:[0,1]\n"
    "v_pk_mul_f32 v[46:47], v[46:47], v[210:211] op_sel:[0,1]\n"
    "buffer_load_dwordx4 v206, s[40:43], s73 offen offset:2048 lds\n"
    "v_cvt_pk_bf16_f32 v32, v32, v33\n"
    "v_cvt_pk_bf16_f32 v33, v34, v35\n"
    "v_cvt_pk_bf16_f32 v34, v36, v37\n"
    "v_cvt_pk_bf16_f32 v35, v38, v39\n"
    "v_cvt_pk_bf16_f32 v36, v40, v41\n"
    "v_cvt_pk_bf16_f32 v37, v42, v43\n"
    "v_cvt_pk_bf16_f32 v38, v44, v45\n"
    "v_cvt_pk_bf16_f32 v39, v46, v47\n"
    "ds_write_b128 v214, v[32:35] offset:2048\n"
    "ds_write_b128 v214, v[36:39] offset:2304\n"
    "v_accvgpr_read_b32 v48, a48\n"
    "v_accvgpr_read_b32 v49, a49\n"
    "v_accvgpr_read_b32 v50, a50\n"
    "v_accvgpr_read_b32 v51, a51\n"
    "v_accvgpr_read_b32 v52, a52\n"
    "v_accvgpr_read_b32 v53, a53\n"
    "buffer_load_dwordx4 v207, s[40:43], s73 offen offset:3072 lds\n"
    "v_accvgpr_read_b32 v54, a54\n"
    "v_accvgpr_read_b32 v55, a55\n"
    "v_accvgpr_read_b32 v56, a56\n"
    "v_accvgpr_read_b32 v57, a57\n"
    "v_accvgpr_read_b32 v58, a58\n"
    "v_accvgpr_read_b32 v59, a59\n"
    "v_accvgpr_read_b32 v60, a60\n"
    "v_accvgpr_read_b32 v61, a61\n"
    "v_accvgpr_read_b32 v62, a62\n"
    "v_accvgpr_read_b32 v63, a63\n"
    "v_pk_mul_f32 v[48:49], v[48:49], v[212:213] op_sel_hi:[1,0]\n"
    "v_pk_mul_f32 v[50:51], v[50:51], v[212:213] op_sel_hi:[1,0]\n"
    "v_pk_mul_f32 v[52:53], v[52:53], v[212:213] op_sel_hi:[1,0]\n"
    "v_pk_mul_f32 v[54:55], v[54:55], v[212:213] op_sel_hi:[1,0]\n"
    "v_pk_mul_f32 v[56:57], v[56:57], v[212:213] op_sel:[0,1]\n"
    "v_pk_mul_f32 v[58:59], v[58:59], v[212:213] op_sel:[0,1]\n"
    "s_add_u32 m0, s8, 4096\n"
    "s_mov_b32 s73, 163840\n"
    "buffer_load_dwordx4 v204, s[40:43], s73 offen offset:0 lds\n"
    "v_pk_mul_f32 v[60:61], v[60:61], v[212:213] op_sel:[0,1]\n"
    "v_pk_mul_f32 v[62:63], v[62:63], v[212:213] op_sel:[0,1]\n"
    "v_cvt_pk_bf16_f32 v48, v48, v49\n"
    "v_cvt_pk_bf16_f32 v49, v50, v51\n"
    "v_cvt_pk_bf16_f32 v50, v52, v53\n"
    "v_cvt_pk_bf16_f32 v51, v54, v55\n"
    "v_cvt_pk_bf16_f32 v52, v56, v57\n"
    "v_cvt_pk_bf16_f32 v53, v58, v59\n"
    "v_cvt_pk_bf16_f32 v54, v60, v61\n"
    "v_cvt_pk_bf16_f32 v55, v62, v63\n"
    "ds_write_b128 v214, v[48:51] offset:34816\n"
    "ds_write_b128 v214, v[52:55] offset:35072\n"
    "v_accvgpr_read_b32 v0, a64\n"
    "v_accvgpr_read_b32 v1, a65\n"
    "v_accvgpr_read_b32 v2, a66\n"
    "v_accvgpr_read_b32 v3, a67\n"
    "buffer_load_dwordx4 v205, s[40:43], s73 offen offset:1024 lds\n"
    "v_accvgpr_read_b32 v4, a68\n"
    "v_accvgpr_read_b32 v5, a69\n"
    "v_accvgpr_read_b32 v6, a70\n"
    "v_accvgpr_read_b32 v7, a71\n"
    "v_accvgpr_read_b32 v8, a72\n"
    "v_accvgpr_read_b32 v9, a73\n"
    "v_accvgpr_read_b32 v10, a74\n"
    "v_accvgpr_read_b32 v11, a75\n"
    "v_accvgpr_read_b32 v12, a76\n"
    "v_accvgpr_read_b32 v13, a77\n"
    "v_accvgpr_read_b32 v14, a78\n"
    "v_accvgpr_read_b32 v15, a79\n"
    "v_pk_mul_f32 v[0:1], v[0:1], v[210:211] op_sel_hi:[1,0]\n"
    "v_pk_mul_f32 v[2:3], v[2:3], v[210:211] op_sel_hi:[1,0]\n"
    "v_pk_mul_f32 v[4:5], v[4:5], v[210:211] op_sel_hi:[1,0]\n"
    "v_pk_mul_f32 v[6:7], v[6:7], v[210:211] op_sel_hi:[1,0]\n"
    "buffer_load_dwordx4 v206, s[40:43], s73 offen offset:2048 lds\n"
    "v_pk_mul_f32 v[8:9], v[8:9], v[210:211] op_sel:[0,1]\n"
    "v_pk_mul_f32 v[10:11], v[10:11], v[210:211] op_sel:[0,1]\n"
    "v_pk_mul_f32 v[12:13], v[12:13], v[210:211] op_sel:[0,1]\n"
    "v_pk_mul_f32 v[14:15], v[14:15], v[210:211] op_sel:[0,1]\n"
    "v_cvt_pk_bf16_f32 v0, v0, v1\n"
    "v_cvt_pk_bf16_f32 v1, v2, v3\n"
    "v_cvt_pk_bf16_f32 v2, v4, v5\n"
    "v_cvt_pk_bf16_f32 v3, v6, v7\n"
    "v_cvt_pk_bf16_f32 v4, v8, v9\n"
    "v_cvt_pk_bf16_f32 v5, v10, v11\n"
    "v_cvt_pk_bf16_f32 v6, v12, v13\n"
    "v_cvt_pk_bf16_f32 v7, v14, v15\n"
    "ds_write_b128 v214, v[0:3] offset:4096\n"
    "ds_write_b128 v214, v[4:7] offset:4352\n"
    "v_accvgpr_read_b32 v16, a80\n"
    "v_accvgpr_read_b32 v17, a81\n"
    "buffer_load_dwordx4 v207, s[40:43], s73 offen offset:3072 lds\n"
    "v_accvgpr_read_b32 v18, a82\n"
    "v_accvgpr_read_b32 v19, a83\n"
    "v_accvgpr_read_b32 v20, a84\n"
    "v_accvgpr_read_b32 v21, a85\n"
    "v_accvgpr_read_b32 v22, a86\n"
    "v_accvgpr_read_b32 v23, a87\n"
    "v_accvgpr_read_b32 v24, a88\n"
    "v_accvgpr_read_b32 v25, a89\n"
    "v_accvgpr_read_b32 v26, a90\n"
    "v_accvgpr_read_b32 v27, a91\n"
    "v_accvgpr_read_b32 v28, a92\n"
    "v_accvgpr_read_b32 v29, a93\n"
    "v_accvgpr_read_b32 v30, a94\n"
    "v_accvgpr_read_b32 v31, a95\n"
    "v_pk_mul_f32 v[16:17], v[16:17], v[212:213] op_sel_hi:[1,0]\n"
    "v_pk_mul_f32 v[18:19], v[18:19], v[212:213] op_sel_hi:[1,0]\n"
    "s_mov_b64 s[52:53], s[4:5]\n"
    "s_mov_b32 s54, 8192\n"
    "s_lshl_b32 s69, s30, 8\n"
    "buffer_load_dwordx4 v[184:187], v225, s[52:55], s69 offen offset:0\n"
    "v_pk_mul_f32 v[20:21], v[20:21], v[212:213] op_sel_hi:[1,0]\n"
    "v_pk_mul_f32 v[22:23], v[22:23], v[212:213] op_sel_hi:[1,0]\n"
    "v_pk_mul_f32 v[24:25], v[24:25], v[212:213] op_sel:[0,1]\n"
    "v_pk_mul_f32 v[26:27], v[26:27], v[212:213] op_sel:[0,1]\n"
    "v_pk_mul_f32 v[28:29], v[28:29], v[212:213] op_sel:[0,1]\n"
    "v_pk_mul_f32 v[30:31], v[30:31], v[212:213] op_sel:[0,1]\n"
    "v_cvt_pk_bf16_f32 v16, v16, v17\n"
    "v_cvt_pk_bf16_f32 v17, v18, v19\n"
    "v_cvt_pk_bf16_f32 v18, v20, v21\n"
    "v_cvt_pk_bf16_f32 v19, v22, v23\n"
    "v_cvt_pk_bf16_f32 v20, v24, v25\n"
    "v_cvt_pk_bf16_f32 v21, v26, v27\n"
    "v_cvt_pk_bf16_f32 v22, v28, v29\n"
    "v_cvt_pk_bf16_f32 v23, v30, v31\n"
    "ds_write_b128 v214, v[16:19] offset:36864\n"
    "ds_write_b128 v214, v[20:23] offset:37120\n"
    "buffer_load_dwordx4 v[188:191], v225, s[52:55], s69 offen offset:128\n"
    "v_accvgpr_read_b32 v32, a96\n"
    "v_accvgpr_read_b32 v33, a97\n"
    "v_accvgpr_read_b32 v34, a98\n"
    "v_accvgpr_read_b32 v35, a99\n"
    "v_accvgpr_read_b32 v36, a100\n"
    "v_accvgpr_read_b32 v37, a101\n"
    "v_accvgpr_read_b32 v38, a102\n"
    "v_accvgpr_read_b32 v39, a103\n"
    "v_accvgpr_read_b32 v40, a104\n"
    "v_accvgpr_read_b32 v41, a105\n"
    "v_accvgpr_read_b32 v42, a106\n"
    "v_accvgpr_read_b32 v43, a107\n"
    "v_accvgpr_read_b32 v44, a108\n"
    "v_accvgpr_read_b32 v45, a109\n"
    "v_accvgpr_read_b32 v46, a110\n"
    "v_accvgpr_read_b32 v47, a111\n"
    "s_mov_b64 s[52:53], s[16:17]\n"
    "s_mov_b32 s54, 3584\n"
    "v_lshrrev_b32 v216, 1, v229\n"
    "buffer_load_dwordx2 v[160:161], v216, s[52:55], s37 offen offset:0\n"
    "v_pk_mul_f32 v[32:33], v[32:33], v[210:211] op_sel_hi:[1,0]\n"
    "v_pk_mul_f32 v[34:35], v[34:35], v[210:211] op_sel_hi:[1,0]\n"
    "v_pk_mul_f32 v[36:37], v[36:37], v[210:211] op_sel_hi:[1,0]\n"
    "v_pk_mul_f32 v[38:39], v[38:39], v[210:211] op_sel_hi:[1,0]\n"
    "v_pk_mul_f32 v[40:41], v[40:41], v[210:211] op_sel:[0,1]\n"
    "v_pk_mul_f32 v[42:43], v[42:43], v[210:211] op_sel:[0,1]\n"
    "v_pk_mul_f32 v[44:45], v[44:45], v[210:211] op_sel:[0,1]\n"
    "v_pk_mul_f32 v[46:47], v[46:47], v[210:211] op_sel:[0,1]\n"
    "v_cvt_pk_bf16_f32 v32, v32, v33\n"
    "v_cvt_pk_bf16_f32 v33, v34, v35\n"
    "v_cvt_pk_bf16_f32 v34, v36, v37\n"
    "v_cvt_pk_bf16_f32 v35, v38, v39\n"
    "v_cvt_pk_bf16_f32 v36, v40, v41\n"
    "v_cvt_pk_bf16_f32 v37, v42, v43\n"
    "v_cvt_pk_bf16_f32 v38, v44, v45\n"
    "v_cvt_pk_bf16_f32 v39, v46, v47\n"
    "buffer_load_dwordx2 v[162:163], v216, s[52:55], s37 offen offset:32\n"
    "ds_write_b128 v214, v[32:35] offset:6144\n"
    "ds_write_b128 v214, v[36:39] offset:6400\n"
    "v_accvgpr_read_b32 v48, a112\n"
    "v_accvgpr_read_b32 v49, a113\n"
    "v_accvgpr_read_b32 v50, a114\n"
    "v_accvgpr_read_b32 v51, a115\n"
    "v_accvgpr_read_b32 v52, a116\n"
    "v_accvgpr_read_b32 v53, a117\n"
    "v_accvgpr_read_b32 v54, a118\n"
    "v_accvgpr_read_b32 v55, a119\n"
    "v_accvgpr_read_b32 v56, a120\n"
    "v_accvgpr_read_b32 v57, a121\n"
    "v_accvgpr_read_b32 v58, a122\n"
    "v_accvgpr_read_b32 v59, a123\n"
    "v_accvgpr_read_b32 v60, a124\n"
    "v_accvgpr_read_b32 v61, a125\n"
    "buffer_load_dwordx2 v[164:165], v216, s[52:55], s37 offen offset:64\n"
    "v_accvgpr_read_b32 v62, a126\n"
    "v_accvgpr_read_b32 v63, a127\n"
    "v_pk_mul_f32 v[48:49], v[48:49], v[212:213] op_sel_hi:[1,0]\n"
    "v_pk_mul_f32 v[50:51], v[50:51], v[212:213] op_sel_hi:[1,0]\n"
    "v_pk_mul_f32 v[52:53], v[52:53], v[212:213] op_sel_hi:[1,0]\n"
    "v_pk_mul_f32 v[54:55], v[54:55], v[212:213] op_sel_hi:[1,0]\n"
    "v_pk_mul_f32 v[56:57], v[56:57], v[212:213] op_sel:[0,1]\n"
    "v_pk_mul_f32 v[58:59], v[58:59], v[212:213] op_sel:[0,1]\n"
    "v_pk_mul_f32 v[60:61], v[60:61], v[212:213] op_sel:[0,1]\n"
    "v_pk_mul_f32 v[62:63], v[62:63], v[212:213] op_sel:[0,1]\n"
    "v_cvt_pk_bf16_f32 v48, v48, v49\n"
    "v_cvt_pk_bf16_f32 v49, v50, v51\n"
    "v_cvt_pk_bf16_f32 v50, v52, v53\n"
    "v_cvt_pk_bf16_f32 v51, v54, v55\n"
    "v_cvt_pk_bf16_f32 v52, v56, v57\n"
    "v_cvt_pk_bf16_f32 v53, v58, v59\n"
    "buffer_load_dwordx2 v[166:167], v216, s[52:55], s37 offen offset:96\n"
    "v_cvt_pk_bf16_f32 v54, v60, v61\n"
    "v_cvt_pk_bf16_f32 v55, v62, v63\n"
    "ds_write_b128 v214, v[48:51] offset:38912\n"
    "ds_write_b128 v214, v[52:55] offset:39168\n"
    "ds_read_b64_tr_b16 a[224:225], v208 offset:0\n"
    "ds_read_b64_tr_b16 a[226:227], v208 offset:512\n"
    "ds_read_b64_tr_b16 a[228:229], v209 offset:0\n"
    "ds_read_b64_tr_b16 a[230:231], v209 offset:512\n"
    "ds_read_b64_tr_b16 a[232:233], v208 offset:256\n"
    "ds_read_b64_tr_b16 a[234:235], v208 offset:768\n"
    "ds_read_b64_tr_b16 a[236:237], v209 offset:256\n"
    "ds_read_b64_tr_b16 a[238:239], v209 offset:768\n"
    "s_waitcnt lgkmcnt(0)\n"
    "s_barrier\n"
    "ds_read_b128 a[0:3], v231 offset:0\n"
    "ds_read_b128 a[4:7], v231 offset:256\n"
    "ds_read_b128 a[8:11], v231 offset:32768\n"
    "ds_read_b128 a[12:15], v231 offset:33024\n"
    "s_waitcnt lgkmcnt(0)\n"
    "v_mfma_f32_16x16x32_bf16 v[64:67], a[224:227], a[0:3], 0\n"
    "ds_read_b64_tr_b16 a[240:241], v208 offset:4096\n"
    "ds_read_b64_tr_b16 a[242:243], v208 offset:4608\n"
    "v_mfma_f32_16x16x32_bf16 v[72:75], a[224:227], a[4:7], 0\n"
    "ds_read_b128 a[16:19], v231 offset:2048\n"
    "ds_read_b64_tr_b16 a[244:245], v209 offset:4096\n"
    "v_mfma_f32_16x16x32_bf16 v[80:83], a[224:227], a[8:11], 0\n"
    "ds_read_b64_tr_b16 a[246:247], v209 offset:4608\n"
    "v_mfma_f32_16x16x32_bf16 v[88:91], a[224:227], a[12:15], 0\n"
    "ds_read_b128 a[20:23], v231 offset:2304\n"
    "v_mfma_f32_16x16x32_bf16 v[68:71], a[228:231], a[0:3], 0\n"
    "ds_read_b64_tr_b16 a[248:249], v208 offset:4352\n"
    "v_mfma_f32_16x16x32_bf16 v[76:79], a[228:231], a[4:7], 0\n"
    "ds_read_b64_tr_b16 a[250:251], v208 offset:4864\n"
    "s_add_u32 m0, s72, 0\n"
    "s_mov_b32 s73, 196608\n"
    "buffer_load_dwordx4 v204, s[40:43], s73 offen offset:0 lds\n"
    "v_mfma_f32_16x16x32_bf16 v[84:87], a[228:231], a[8:11], 0\n"
    "ds_read_b128 a[24:27], v231 offset:34816\n"
    "v_mfma_f32_16x16x32_bf16 v[92:95], a[228:231], a[12:15], 0\n"
    "ds_read_b64_tr_b16 a[252:253], v209 offset:4352\n"
    "v_mfma_f32_16x16x32_bf16 v[96:99], a[232:235], a[0:3], 0\n"
    "ds_read_b64_tr_b16 a[254:255], v209 offset:4864\n"
    "buffer_load_dwordx4 v205, s[40:43], s73 offen offset:1024 lds\n"
    "v_mfma_f32_16x16x32_bf16 v[104:107], a[232:235], a[4:7], 0\n"
    "ds_read_b128 a[28:31], v231 offset:35072\n"
    "v_mfma_f32_16x16x32_bf16 v[112:115], a[232:235], a[8:11], 0\n"
    "v_mfma_f32_16x16x32_bf16 v[120:123], a[232:235], a[12:15], 0\n"
    "buffer_load_dwordx4 v206, s[40:43], s73 offen offset:2048 lds\n"
    "v_mfma_f32_16x16x32_bf16 v[100:103], a[236:239], a[0:3], 0\n"
    "v_mfma_f32_16x16x32_bf16 v[108:111], a[236:239], a[4:7], 0\n"
    "buffer_load_dwordx4 v207, s[40:43], s73 offen offset:3072 lds\n"
    "v_mfma_f32_16x16x32_bf16 v[116:119], a[236:239], a[8:11], 0\n"
    "v_mfma_f32_16x16x32_bf16 v[124:127], a[236:239], a[12:15], 0\n"
    "s_waitcnt lgkmcnt(0)\n"
    "v_mfma_f32_16x16x32_bf16 v[64:67], a[240:243], a[16:19], v[64:67]\n"
    "ds_read_b64_tr_b16 a[224:225], v208 offset:8192\n"
    "ds_read_b64_tr_b16 a[226:227], v208 offset:8704\n"
    "v_mfma_f32_16x16x32_bf16 v[72:75], a[240:243], a[20:23], v[72:75]\n"
    "ds_read_b128 a[32:35], v231 offset:4096\n"
    "ds_read_b64_tr_b16 a[228:229], v209 offset:8192\n"
    "v_mfma_f32_16x16x32_bf16 v[80:83], a[240:243], a[24:27], v[80:83]\n"
    "ds_read_b64_tr_b16 a[230:231], v209 offset:8704\n"
    "v_mfma_f32_16x16x32_bf16 v[88:91], a[240:243], a[28:31], v[88:91]\n"
    "ds_read_b128 a[36:39], v231 offset:4352\n"
    "v_mfma_f32_16x16x32_bf16 v[68:71], a[244:247], a[16:19], v[68:71]\n"
    "ds_read_b64_tr_b16 a[232:233], v208 offset:8448\n"
    "v_mfma_f32_16x16x32_bf16 v[76:79], a[244:247], a[20:23], v[76:79]\n"
    "ds_read_b64_tr_b16 a[234:235], v208 offset:8960\n"
    "s_add_u32 m0, s72, 4096\n"
    "s_mov_b32 s73, 229376\n"
    "buffer_load_dwordx4 v204, s[40:43], s73 offen offset:0 lds\n"
    "v_mfma_f32_16x16x32_bf16 v[84:87], a[244:247], a[24:27], v[84:87]\n"
    "ds_read_b128 a[40:43], v231 offset:36864\n"
    "v_mfma_f32_16x16x32_bf16 v[92:95], a[244:247], a[28:31], v[92:95]\n"
    "ds_read_b64_tr_b16 a[236:237], v209 offset:8448\n"
    "v_mfma_f32_16x16x32_bf16 v[96:99], a[248:251], a[16:19], v[96:99]\n"
    "ds_read_b64_tr_b16 a[238:239], v209 offset:8960\n"
    "buffer_load_dwordx4 v205, s[40:43], s73 offen offset:1024 lds\n"
    "v_mfma_f32_16x16x32_bf16 v[104:107], a[248:251], a[20:23], v[104:107]\n"
    "ds_read_b128 a[44:47], v231 offset:37120\n"
    "v_mfma_f32_16x16x32_bf16 v[112:115], a[248:251], a[24:27], v[112:115]\n"
    "v_mfma_f32_16x16x32_bf16 v[120:123], a[248:251], a[28:31], v[120:123]\n"
    "buffer_load_dwordx4 v206, s[40:43], s73 offen offset:2048 lds\n"
    "v_mfma_f32_16x16x32_bf16 v[100:103], a[252:255], a[16:19], v[100:103]\n"
    "v_mfma_f32_16x16x32_bf16 v[108:111], a[252:255], a[20:23], v[108:111]\n"
    "buffer_load_dwordx4 v207, s[40:43], s73 offen offset:3072 lds\n"
    "v_mfma_f32_16x16x32_bf16 v[116:119], a[252:255], a[24:27], v[116:119]\n"
    "v_mfma_f32_16x16x32_bf16 v[124:127], a[252:255], a[28:31], v[124:127]\n"
    "s_waitcnt lgkmcnt(0)\n"
    "v_mfma_f32_16x16x32_bf16 v[64:67], a[224:227], a[32:35], v[64:67]\n"
    "ds_read_b64_tr_b16 a[240:241], v208 offset:12288\n"
    "ds_read_b64_tr_b16 a[242:243], v208 offset:12800\n"
    "v_mfma_f32_16x16x32_bf16 v[72:75], a[224:227], a[36:39], v[72:75]\n"
    "ds_read_b128 a[48:51], v231 offset:6144\n"
    "ds_read_b64_tr_b16 a[244:245], v209 offset:12288\n"
    "v_mfma_f32_16x16x32_bf16 v[80:83], a[224:227], a[40:43], v[80:83]\n"
    "ds_read_b64_tr_b16 a[246:247], v209 offset:12800\n"
    "v_mfma_f32_16x16x32_bf16 v[88:91], a[224:227], a[44:47], v[88:91]\n"
    "ds_read_b128 a[52:55], v231 offset:6400\n"
    "v_mfma_f32_16x16x32_bf16 v[68:71], a[228:231], a[32:35], v[68:71]\n"
    "ds_read_b64_tr_b16 a[248:249], v208 offset:12544\n"
    "v_mfma_f32_16x16x32_bf16 v[76:79], a[228:231], a[36:39], v[76:79]\n"
    "ds_read_b64_tr_b16 a[250:251], v208 offset:13056\n"
    "s_add_u32 m0, s72, 8192\n"
    "s_mov_b32 s73, 262144\n"
    "buffer_load_dwordx4 v204, s[40:43], s73 offen offset:0 lds\n"
    "v_mfma_f32_16x16x32_bf16 v[84:87], a[228:231], a[40:43], v[84:87]\n"
    "ds_read_b128 a[56:59], v231 offset:38912\n"
    "v_mfma_f32_16x16x32_bf16 v[92:95], a[228:231], a[44:47], v[92:95]\n"
    "ds_read_b64_tr_b16 a[252:253], v209 offset:12544\n"
    "v_mfma_f32_16x16x32_bf16 v[96:99], a[232:235], a[32:35], v[96:99]\n"
    "ds_read_b64_tr_b16 a[254:255], v209 offset:13056\n"
    "buffer_load_dwordx4 v205, s[40:43], s73 offen offset:1024 lds\n"
    "v_mfma_f32_16x16x32_bf16 v[104:107], a[232:235], a[36:39], v[104:107]\n"
    "ds_read_b128 a[60:63], v231 offset:39168\n"
    "v_mfma_f32_16x16x32_bf16 v[112:115], a[232:235], a[40:43], v[112:115]\n"
    "v_mfma_f32_16x16x32_bf16 v[120:123], a[232:235], a[44:47], v[120:123]\n"
    "buffer_load_dwordx4 v206, s[40:43], s73 offen offset:2048 lds\n"
    "v_mfma_f32_16x16x32_bf16 v[100:103], a[236:239], a[32:35], v[100:103]\n"
    "v_mfma_f32_16x16x32_bf16 v[108:111], a[236:239], a[36:39], v[108:111]\n"
    "buffer_load_dwordx4 v207, s[40:43], s73 offen offset:3072 lds\n"
    "v_mfma_f32_16x16x32_bf16 v[116:119], a[236:239], a[40:43], v[116:119]\n"
    "v_mfma_f32_16x16x32_bf16 v[124:127], a[236:239], a[44:47], v[124:127]\n"
    "s_waitcnt vmcnt(22) lgkmcnt(0)\n"
    "v_mfma_f32_16x16x32_bf16 v[64:67], a[240:243], a[48:51], v[64:67]\n"
    "ds_read_b64_tr_b16 a[224:225], v202 offset:0\n"
    "ds_read_b64_tr_b16 a[226:227], v202 offset:512\n"
    "v_mfma_f32_16x16x32_bf16 v[72:75], a[240:243], a[52:55], v[72:75]\n"
    "ds_read_b128 a[64:67], v231 offset:8192\n"
    "ds_read_b64_tr_b16 a[228:229], v203 offset:0\n"
    "v_mfma_f32_16x16x32_bf16 v[80:83], a[240:243], a[56:59], v[80:83]\n"
    "ds_read_b64_tr_b16 a[230:231], v203 offset:512\n"
    "v_mfma_f32_16x16x32_bf16 v[88:91], a[240:243], a[60:63], v[88:91]\n"
    "ds_read_b128 a[68:71], v231 offset:8448\n"
    "v_mfma_f32_16x16x32_bf16 v[68:71], a[244:247], a[48:51], v[68:71]\n"
    "ds_read_b64_tr_b16 a[232:233], v202 offset:256\n"
    "v_mfma_f32_16x16x32_bf16 v[76:79], a[244:247], a[52:55], v[76:79]\n"
    "ds_read_b64_tr_b16 a[234:235], v202 offset:768\n"
    "s_add_u32 m0, s72, 12288\n"
    "s_mov_b32 s73, 294912\n"
    "buffer_load_dwordx4 v204, s[40:43], s73 offen offset:0 lds\n"
    "v_mfma_f32_16x16x32_bf16 v[84:87], a[244:247], a[56:59], v[84:87]\n"
    "ds_read_b128 a[72:75], v231 offset:40960\n"
    "v_mfma_f32_16x16x32_bf16 v[92:95], a[244:247], a[60:63], v[92:95]\n"
    "ds_read_b64_tr_b16 a[236:237], v203 offset:256\n"
    "v_mfma_f32_16x16x32_bf16 v[96:99], a[248:251], a[48:51], v[96:99]\n"
    "ds_read_b64_tr_b16 a[238:239], v203 offset:768\n"
    "buffer_load_dwordx4 v205, s[40:43], s73 offen offset:1024 lds\n"
    "v_mfma_f32_16x16x32_bf16 v[104:107], a[248:251], a[52:55], v[104:107]\n"
    "ds_read_b128 a[76:79], v231 offset:41216\n"
    "v_mfma_f32_16x16x32_bf16 v[112:115], a[248:251], a[56:59], v[112:115]\n"
    "v_mfma_f32_16x16x32_bf16 v[120:123], a[248:251], a[60:63], v[120:123]\n"
    "buffer_load_dwordx4 v206, s[40:43], s73 offen offset:2048 lds\n"
    "v_mfma_f32_16x16x32_bf16 v[100:103], a[252:255], a[48:51], v[100:103]\n"
    "v_mfma_f32_16x16x32_bf16 v[108:111], a[252:255], a[52:55], v[108:111]\n"
    "buffer_load_dwordx4 v207, s[40:43], s73 offen offset:3072 lds\n"
    "v_mfma_f32_16x16x32_bf16 v[116:119], a[252:255], a[56:59], v[116:119]\n"
    "v_mfma_f32_16x16x32_bf16 v[124:127], a[252:255], a[60:63], v[124:127]\n"
    "s_waitcnt vmcnt(22) lgkmcnt(0)\n"
    "v_mfma_f32_16x16x32_bf16 v[64:67], a[224:227], a[64:67], v[64:67]\n"
    "ds_read_b64_tr_b16 a[240:241], v202 offset:4096\n"
    "ds_read_b64_tr_b16 a[242:243], v202 offset:4608\n"
    "v_mfma_f32_16x16x32_bf16 v[72:75], a[224:227], a[68:71], v[72:75]\n"
    "ds_read_b128 a[80:83], v231 offset:10240\n"
    "ds_read_b64_tr_b16 a[244:245], v203 offset:4096\n"
    "v_mfma_f32_16x16x32_bf16 v[80:83], a[224:227], a[72:75], v[80:83]\n"
    "ds_read_b64_tr_b16 a[246:247], v203 offset:4608\n"
    "v_mfma_f32_16x16x32_bf16 v[88:91], a[224:227], a[76:79], v[88:91]\n"
    "ds_read_b128 a[84:87], v231 offset:10496\n"
    "v_mfma_f32_16x16x32_bf16 v[68:71], a[228:231], a[64:67], v[68:71]\n"
    "ds_read_b64_tr_b16 a[248:249], v202 offset:4352\n"
    "v_mfma_f32_16x16x32_bf16 v[76:79], a[228:231], a[68:71], v[76:79]\n"
    "ds_read_b64_tr_b16 a[250:251], v202 offset:4864\n"
    "s_add_u32 m0, s8, 0\n"
    "s_mov_b32 s73, 327680\n"
    "buffer_load_dwordx4 v204, s[40:43], s73 offen offset:0 lds\n"
    "v_mfma_f32_16x16x32_bf16 v[84:87], a[228:231], a[72:75], v[84:87]\n"
    "ds_read_b128 a[88:91], v231 offset:43008\n"
    "v_mfma_f32_16x16x32_bf16 v[92:95], a[228:231], a[76:79], v[92:95]\n"
    "ds_read_b64_tr_b16 a[252:253], v203 offset:4352\n"
    "v_mfma_f32_16x16x32_bf16 v[96:99], a[232:235], a[64:67], v[96:99]\n"
    "ds_read_b64_tr_b16 a[254:255], v203 offset:4864\n"
    "buffer_load_dwordx4 v205, s[40:43], s73 offen offset:1024 lds\n"
    "v_mfma_f32_16x16x32_bf16 v[104:107], a[232:235], a[68:71], v[104:107]\n"
    "ds_read_b128 a[92:95], v231 offset:43264\n"
    "v_mfma_f32_16x16x32_bf16 v[112:115], a[232:235], a[72:75], v[112:115]\n"
    "v_mfma_f32_16x16x32_bf16 v[120:123], a[232:235], a[76:79], v[120:123]\n"
    "buffer_load_dwordx4 v206, s[40:43], s73 offen offset:2048 lds\n"
    "v_mfma_f32_16x16x32_bf16 v[100:103], a[236:239], a[64:67], v[100:103]\n"
    "v_mfma_f32_16x16x32_bf16 v[108:111], a[236:239], a[68:71], v[108:111]\n"
    "buffer_load_dwordx4 v207, s[40:43], s73 offen offset:3072 lds\n"
    "v_mfma_f32_16x16x32_bf16 v[116:119], a[236:239], a[72:75], v[116:119]\n"
    "v_mfma_f32_16x16x32_bf16 v[124:127], a[236:239], a[76:79], v[124:127]\n"
    "s_waitcnt vmcnt(16) lgkmcnt(0)\n"
    "v_mfma_f32_16x16x32_bf16 v[64:67], a[240:243], a[80:83], v[64:67]\n"
    "ds_read_b64_tr_b16 a[224:225], v208 offset:0\n"
    "ds_read_b64_tr_b16 a[226:227], v208 offset:512\n"
    "v_mfma_f32_16x16x32_bf16 v[72:75], a[240:243], a[84:87], v[72:75]\n"
    "ds_read_b128 a[96:99], v231 offset:12288\n"
    "ds_read_b64_tr_b16 a[228:229], v209 offset:0\n"
    "v_mfma_f32_16x16x32_bf16 v[80:83], a[240:243], a[88:91], v[80:83]\n"
    "ds_read_b64_tr_b16 a[230:231], v209 offset:512\n"
    "v_mfma_f32_16x16x32_bf16 v[88:91], a[240:243], a[92:95], v[88:91]\n"
    "ds_read_b128 a[100:103], v231 offset:12544\n"
    "v_mfma_f32_16x16x32_bf16 v[68:71], a[244:247], a[80:83], v[68:71]\n"
    "ds_read_b64_tr_b16 a[232:233], v208 offset:256\n"
    "v_mfma_f32_16x16x32_bf16 v[76:79], a[244:247], a[84:87], v[76:79]\n"
    "ds_read_b64_tr_b16 a[234:235], v208 offset:768\n"
    "s_add_u32 m0, s8, 4096\n"
    "s_mov_b32 s73, 360448\n"
    "buffer_load_dwordx4 v204, s[40:43], s73 offen offset:0 lds\n"
    "v_mfma_f32_16x16x32_bf16 v[84:87], a[244:247], a[88:91], v[84:87]\n"
    "ds_read_b128 a[104:107], v231 offset:45056\n"
    "v_mfma_f32_16x16x32_bf16 v[92:95], a[244:247], a[92:95], v[92:95]\n"
    "ds_read_b64_tr_b16 a[236:237], v209 offset:256\n"
    "v_mfma_f32_16x16x32_bf16 v[96:99], a[248:251], a[80:83], v[96:99]\n"
    "ds_read_b64_tr_b16 a[238:239], v209 offset:768\n"
    "buffer_load_dwordx4 v205, s[40:43], s73 offen offset:1024 lds\n"
    "v_mfma_f32_16x16x32_bf16 v[104:107], a[248:251], a[84:87], v[104:107]\n"
    "ds_read_b128 a[108:111], v231 offset:45312\n"
    "v_mfma_f32_16x16x32_bf16 v[112:115], a[248:251], a[88:91], v[112:115]\n"
    "v_mfma_f32_16x16x32_bf16 v[120:123], a[248:251], a[92:95], v[120:123]\n"
    "buffer_load_dwordx4 v206, s[40:43], s73 offen offset:2048 lds\n"
    "v_mfma_f32_16x16x32_bf16 v[100:103], a[252:255], a[80:83], v[100:103]\n"
    "v_mfma_f32_16x16x32_bf16 v[108:111], a[252:255], a[84:87], v[108:111]\n"
    "buffer_load_dwordx4 v207, s[40:43], s73 offen offset:3072 lds\n"
    "v_mfma_f32_16x16x32_bf16 v[116:119], a[252:255], a[88:91], v[116:119]\n"
    "v_mfma_f32_16x16x32_bf16 v[124:127], a[252:255], a[92:95], v[124:127]\n"
    "s_waitcnt vmcnt(16) lgkmcnt(0)\n"
    "v_mfma_f32_16x16x32_bf16 v[64:67], a[224:227], a[96:99], v[64:67]\n"
    "ds_read_b64_tr_b16 a[240:241], v208 offset:4096\n"
    "ds_read_b64_tr_b16 a[242:243], v208 offset:4608\n"
    "v_mfma_f32_16x16x32_bf16 v[72:75], a[224:227], a[100:103], v[72:75]\n"
    "ds_read_b128 a[112:115], v231 offset:14336\n"
    "ds_read_b64_tr_b16 a[244:245], v209 offset:4096\n"
    "v_mfma_f32_16x16x32_bf16 v[80:83], a[224:227], a[104:107], v[80:83]\n"
    "ds_read_b64_tr_b16 a[246:247], v209 offset:4608\n"
    "v_mfma_f32_16x16x32_bf16 v[88:91], a[224:227], a[108:111], v[88:91]\n"
    "ds_read_b128 a[116:119], v231 offset:14592\n"
    "v_mfma_f32_16x16x32_bf16 v[68:71], a[228:231], a[96:99], v[68:71]\n"
    "ds_read_b64_tr_b16 a[248:249], v208 offset:4352\n"
    "v_mfma_f32_16x16x32_bf16 v[76:79], a[228:231], a[100:103], v[76:79]\n"
    "ds_read_b64_tr_b16 a[250:251], v208 offset:4864\n"
    "s_add_u32 m0, s72, 0\n"
    "s_mov_b32 s73, 393216\n"
    "buffer_load_dwordx4 v204, s[40:43], s73 offen offset:0 lds\n"
    "v_mfma_f32_16x16x32_bf16 v[84:87], a[228:231], a[104:107], v[84:87]\n"
    "ds_read_b128 a[120:123], v231 offset:47104\n"
    "v_mfma_f32_16x16x32_bf16 v[92:95], a[228:231], a[108:111], v[92:95]\n"
    "ds_read_b64_tr_b16 a[252:253], v209 offset:4352\n"
    "v_mfma_f32_16x16x32_bf16 v[96:99], a[232:235], a[96:99], v[96:99]\n"
    "ds_read_b64_tr_b16 a[254:255], v209 offset:4864\n"
    "buffer_load_dwordx4 v205, s[40:43], s73 offen offset:1024 lds\n"
    "v_mfma_f32_16x16x32_bf16 v[104:107], a[232:235], a[100:103], v[104:107]\n"
    "ds_read_b128 a[124:127], v231 offset:47360\n"
    "v_mfma_f32_16x16x32_bf16 v[112:115], a[232:235], a[104:107], v[112:115]\n"
    "v_mfma_f32_16x16x32_bf16 v[120:123], a[232:235], a[108:111], v[120:123]\n"
    "buffer_load_dwordx4 v206, s[40:43], s73 offen offset:2048 lds\n"
    "v_mfma_f32_16x16x32_bf16 v[100:103], a[236:239], a[96:99], v[100:103]\n"
    "v_mfma_f32_16x16x32_bf16 v[108:111], a[236:239], a[100:103], v[108:111]\n"
    "buffer_load_dwordx4 v207, s[40:43], s73 offen offset:3072 lds\n"
    "v_mfma_f32_16x16x32_bf16 v[116:119], a[236:239], a[104:107], v[116:119]\n"
    "v_mfma_f32_16x16x32_bf16 v[124:127], a[236:239], a[108:111], v[124:127]\n"
    "s_waitcnt vmcnt(16) lgkmcnt(0)\n"
    "v_mfma_f32_16x16x32_bf16 v[64:67], a[240:243], a[112:115], v[64:67]\n"
    "ds_read_b64_tr_b16 a[224:225], v208 offset:8192\n"
    "ds_read_b64_tr_b16 a[226:227], v208 offset:8704\n"
    "v_mfma_f32_16x16x32_bf16 v[72:75], a[240:243], a[116:119], v[72:75]\n"
    "ds_read_b128 a[128:131], v231 offset:16384\n"
    "ds_read_b64_tr_b16 a[228:229], v209 offset:8192\n"
    "v_mfma_f32_16x16x32_bf16 v[80:83], a[240:243], a[120:123], v[80:83]\n"
    "ds_read_b64_tr_b16 a[230:231], v209 offset:8704\n"
    "v_mfma_f32_16x16x32_bf16 v[88:91], a[240:243], a[124:127], v[88:91]\n"
    "ds_read_b128 a[132:135], v231 offset:16640\n"
    "v_mfma_f32_16x16x32_bf16 v[68:71], a[244:247], a[112:115], v[68:71]\n"
    "ds_read_b64_tr_b16 a[232:233], v208 offset:8448\n"
    "v_mfma_f32_16x16x32_bf16 v[76:79], a[244:247], a[116:119], v[76:79]\n"
    "ds_read_b64_tr_b16 a[234:235], v208 offset:8960\n"
    "s_add_u32 m0, s72, 4096\n"
    "s_mov_b32 s73, 425984\n"
    "buffer_load_dwordx4 v204, s[40:43], s73 offen offset:0 lds\n"
    "v_mfma_f32_16x16x32_bf16 v[84:87], a[244:247], a[120:123], v[84:87]\n"
    "ds_read_b128 a[136:139], v231 offset:49152\n"
    "v_mfma_f32_16x16x32_bf16 v[92:95], a[244:247], a[124:127], v[92:95]\n"
    "ds_read_b64_tr_b16 a[236:237], v209 offset:8448\n"
    "v_mfma_f32_16x16x32_bf16 v[96:99], a[248:251], a[112:115], v[96:99]\n"
    "ds_read_b64_tr_b16 a[238:239], v209 offset:8960\n"
    "buffer_load_dwordx4 v205, s[40:43], s73 offen offset:1024 lds\n"
    "v_mfma_f32_16x16x32_bf16 v[104:107], a[248:251], a[116:119], v[104:107]\n"
    "ds_read_b128 a[140:143], v231 offset:49408\n"
    "v_mfma_f32_16x16x32_bf16 v[112:115], a[248:251], a[120:123], v[112:115]\n"
    "v_mfma_f32_16x16x32_bf16 v[120:123], a[248:251], a[124:127], v[120:123]\n"
    "buffer_load_dwordx4 v206, s[40:43], s73 offen offset:2048 lds\n"
    "v_mfma_f32_16x16x32_bf16 v[100:103], a[252:255], a[112:115], v[100:103]\n"
    "v_mfma_f32_16x16x32_bf16 v[108:111], a[252:255], a[116:119], v[108:111]\n"
    "buffer_load_dwordx4 v207, s[40:43], s73 offen offset:3072 lds\n"
    "v_mfma_f32_16x16x32_bf16 v[116:119], a[252:255], a[120:123], v[116:119]\n"
    "v_mfma_f32_16x16x32_bf16 v[124:127], a[252:255], a[124:127], v[124:127]\n"
    "s_waitcnt vmcnt(16) lgkmcnt(0)\n"
    "v_mfma_f32_16x16x32_bf16 v[64:67], a[224:227], a[128:131], v[64:67]\n"
    "ds_read_b64_tr_b16 a[240:241], v208 offset:12288\n"
    "ds_read_b64_tr_b16 a[242:243], v208 offset:12800\n"
    "v_mfma_f32_16x16x32_bf16 v[72:75], a[224:227], a[132:135], v[72:75]\n"
    "ds_read_b128 a[144:147], v231 offset:18432\n"
    "ds_read_b64_tr_b16 a[244:245], v209 offset:12288\n"
    "v_mfma_f32_16x16x32_bf16 v[80:83], a[224:227], a[136:139], v[80:83]\n"
    "ds_read_b64_tr_b16 a[246:247], v209 offset:12800\n"
    "v_mfma_f32_16x16x32_bf16 v[88:91], a[224:227], a[140:143], v[88:91]\n"
    "ds_read_b128 a[148:151], v231 offset:18688\n"
    "v_mfma_f32_16x16x32_bf16 v[68:71], a[228:231], a[128:131], v[68:71]\n"
    "ds_read_b64_tr_b16 a[248:249], v208 offset:12544\n"
    "v_mfma_f32_16x16x32_bf16 v[76:79], a[228:231], a[132:135], v[76:79]\n"
    "ds_read_b64_tr_b16 a[250:251], v208 offset:13056\n"
    "s_add_u32 m0, s72, 8192\n"
    "s_mov_b32 s73, 458752\n"
    "buffer_load_dwordx4 v204, s[40:43], s73 offen offset:0 lds\n"
    "v_mfma_f32_16x16x32_bf16 v[84:87], a[228:231], a[136:139], v[84:87]\n"
    "ds_read_b128 a[152:155], v231 offset:51200\n"
    "v_mfma_f32_16x16x32_bf16 v[92:95], a[228:231], a[140:143], v[92:95]\n"
    "ds_read_b64_tr_b16 a[252:253], v209 offset:12544\n"
    "v_mfma_f32_16x16x32_bf16 v[96:99], a[232:235], a[128:131], v[96:99]\n"
    "ds_read_b64_tr_b16 a[254:255], v209 offset:13056\n"
    "buffer_load_dwordx4 v205, s[40:43], s73 offen offset:1024 lds\n"
    "v_mfma_f32_16x16x32_bf16 v[104:107], a[232:235], a[132:135], v[104:107]\n"
    "ds_read_b128 a[156:159], v231 offset:51456\n"
    "v_mfma_f32_16x16x32_bf16 v[112:115], a[232:235], a[136:139], v[112:115]\n"
    "v_mfma_f32_16x16x32_bf16 v[120:123], a[232:235], a[140:143], v[120:123]\n"
    "buffer_load_dwordx4 v206, s[40:43], s73 offen offset:2048 lds\n"
    "v_mfma_f32_16x16x32_bf16 v[100:103], a[236:239], a[128:131], v[100:103]\n"
    "v_mfma_f32_16x16x32_bf16 v[108:111], a[236:239], a[132:135], v[108:111]\n"
    "buffer_load_dwordx4 v207, s[40:43], s73 offen offset:3072 lds\n"
    "v_mfma_f32_16x16x32_bf16 v[116:119], a[236:239], a[136:139], v[116:119]\n"
    "v_mfma_f32_16x16x32_bf16 v[124:127], a[236:239], a[140:143], v[124:127]\n"
    "s_waitcnt vmcnt(16) lgkmcnt(0)\n"
    "v_mfma_f32_16x16x32_bf16 v[64:67], a[240:243], a[144:147], v[64:67]\n"
    "ds_read_b64_tr_b16 a[224:225], v202 offset:0\n"
    "ds_read_b64_tr_b16 a[226:227], v202 offset:512\n"
    "v_mfma_f32_16x16x32_bf16 v[72:75], a[240:243], a[148:151], v[72:75]\n"
    "ds_read_b128 a[160:163], v231 offset:20480\n"
    "ds_read_b64_tr_b16 a[228:229], v203 offset:0\n"
    "v_mfma_f32_16x16x32_bf16 v[80:83], a[240:243], a[152:155], v[80:83]\n"
    "ds_read_b64_tr_b16 a[230:231], v203 offset:512\n"
    "v_mfma_f32_16x16x32_bf16 v[88:91], a[240:243], a[156:159], v[88:91]\n"
    "ds_read_b128 a[164:167], v231 offset:20736\n"
    "v_mfma_f32_16x16x32_bf16 v[68:71], a[244:247], a[144:147], v[68:71]\n"
    "ds_read_b64_tr_b16 a[232:233], v202 offset:256\n"
    "v_mfma_f32_16x16x32_bf16 v[76:79], a[244:247], a[148:151], v[76:79]\n"
    "ds_read_b64_tr_b16 a[234:235], v202 offset:768\n"
    "s_add_u32 m0, s72, 12288\n"
    "s_mov_b32 s73, 491520\n"
    "buffer_load_dwordx4 v204, s[40:43], s73 offen offset:0 lds\n"
    "v_mfma_f32_16x16x32_bf16 v[84:87], a[244:247], a[152:155], v[84:87]\n"
    "ds_read_b128 a[168:171], v231 offset:53248\n"
    "v_mfma_f32_16x16x32_bf16 v[92:95], a[244:247], a[156:159], v[92:95]\n"
    "ds_read_b64_tr_b16 a[236:237], v203 offset:256\n"
    "v_mfma_f32_16x16x32_bf16 v[96:99], a[248:251], a[144:147], v[96:99]\n"
    "ds_read_b64_tr_b16 a[238:239], v203 offset:768\n"
    "buffer_load_dwordx4 v205, s[40:43], s73 offen offset:1024 lds\n"
    "v_mfma_f32_16x16x32_bf16 v[104:107], a[248:251], a[148:151], v[104:107]\n"
    "ds_read_b128 a[172:175], v231 offset:53504\n"
    "v_mfma_f32_16x16x32_bf16 v[112:115], a[248:251], a[152:155], v[112:115]\n"
    "v_mfma_f32_16x16x32_bf16 v[120:123], a[248:251], a[156:159], v[120:123]\n"
    "buffer_load_dwordx4 v206, s[40:43], s73 offen offset:2048 lds\n"
    "v_mfma_f32_16x16x32_bf16 v[100:103], a[252:255], a[144:147], v[100:103]\n"
    "v_mfma_f32_16x16x32_bf16 v[108:111], a[252:255], a[148:151], v[108:111]\n"
    "buffer_load_dwordx4 v207, s[40:43], s73 offen offset:3072 lds\n"
    "v_mfma_f32_16x16x32_bf16 v[116:119], a[252:255], a[152:155], v[116:119]\n"
    "v_mfma_f32_16x16x32_bf16 v[124:127], a[252:255], a[156:159], v[124:127]\n"
    "s_waitcnt vmcnt(16) lgkmcnt(0)\n"
    "v_mfma_f32_16x16x32_bf16 v[64:67], a[224:227], a[160:163], v[64:67]\n"
    "ds_read_b64_tr_b16 a[240:241], v202 offset:4096\n"
    "ds_read_b64_tr_b16 a[242:243], v202 offset:4608\n"
    "v_mfma_f32_16x16x32_bf16 v[72:75], a[224:227], a[164:167], v[72:75]\n"
    "ds_read_b128 a[176:179], v231 offset:22528\n"
    "ds_read_b64_tr_b16 a[244:245], v203 offset:4096\n"
    "v_mfma_f32_16x16x32_bf16 v[80:83], a[224:227], a[168:171], v[80:83]\n"
    "ds_read_b64_tr_b16 a[246:247], v203 offset:4608\n"
    "v_mfma_f32_16x16x32_bf16 v[88:91], a[224:227], a[172:175], v[88:91]\n"
    "ds_read_b128 a[180:183], v231 offset:22784\n"
    "v_mfma_f32_16x16x32_bf16 v[68:71], a[228:231], a[160:163], v[68:71]\n"
    "ds_read_b64_tr_b16 a[248:249], v202 offset:4352\n"
    "v_mfma_f32_16x16x32_bf16 v[76:79], a[228:231], a[164:167], v[76:79]\n"
    "ds_read_b64_tr_b16 a[250:251], v202 offset:4864\n"
    "s_add_u32 m0, s8, 0\n"
    "s_mov_b32 s73, 128\n"
    "buffer_load_dwordx4 v204, s[40:43], s73 offen offset:0 lds\n"
    "v_mfma_f32_16x16x32_bf16 v[84:87], a[228:231], a[168:171], v[84:87]\n"
    "ds_read_b128 a[184:187], v231 offset:55296\n"
    "v_mfma_f32_16x16x32_bf16 v[92:95], a[228:231], a[172:175], v[92:95]\n"
    "ds_read_b64_tr_b16 a[252:253], v203 offset:4352\n"
    "v_mfma_f32_16x16x32_bf16 v[96:99], a[232:235], a[160:163], v[96:99]\n"
    "ds_read_b64_tr_b16 a[254:255], v203 offset:4864\n"
    "buffer_load_dwordx4 v205, s[40:43], s73 offen offset:1024 lds\n"
    "v_mfma_f32_16x16x32_bf16 v[104:107], a[232:235], a[164:167], v[104:107]\n"
    "ds_read_b128 a[188:191], v231 offset:55552\n"
    "v_mfma_f32_16x16x32_bf16 v[112:115], a[232:235], a[168:171], v[112:115]\n"
    "v_mfma_f32_16x16x32_bf16 v[120:123], a[232:235], a[172:175], v[120:123]\n"
    "buffer_load_dwordx4 v206, s[40:43], s73 offen offset:2048 lds\n"
    "v_mfma_f32_16x16x32_bf16 v[100:103], a[236:239], a[160:163], v[100:103]\n"
    "v_mfma_f32_16x16x32_bf16 v[108:111], a[236:239], a[164:167], v[108:111]\n"
    "buffer_load_dwordx4 v207, s[40:43], s73 offen offset:3072 lds\n"
    "v_mfma_f32_16x16x32_bf16 v[116:119], a[236:239], a[168:171], v[116:119]\n"
    "v_mfma_f32_16x16x32_bf16 v[124:127], a[236:239], a[172:175], v[124:127]\n"
    "s_waitcnt vmcnt(16) lgkmcnt(0)\n"
    "v_mfma_f32_16x16x32_bf16 v[64:67], a[240:243], a[176:179], v[64:67]\n"
    "ds_read_b64_tr_b16 a[224:225], v208 offset:0\n"
    "ds_read_b64_tr_b16 a[226:227], v208 offset:512\n"
    "v_mfma_f32_16x16x32_bf16 v[72:75], a[240:243], a[180:183], v[72:75]\n"
    "ds_read_b128 a[192:195], v231 offset:24576\n"
    "ds_read_b64_tr_b16 a[228:229], v209 offset:0\n"
    "v_mfma_f32_16x16x32_bf16 v[80:83], a[240:243], a[184:187], v[80:83]\n"
    "ds_read_b64_tr_b16 a[230:231], v209 offset:512\n"
    "v_mfma_f32_16x16x32_bf16 v[88:91], a[240:243], a[188:191], v[88:91]\n"
    "ds_read_b128 a[196:199], v231 offset:24832\n"
    "v_mfma_f32_16x16x32_bf16 v[68:71], a[244:247], a[176:179], v[68:71]\n"
    "ds_read_b64_tr_b16 a[232:233], v208 offset:256\n"
    "v_mfma_f32_16x16x32_bf16 v[76:79], a[244:247], a[180:183], v[76:79]\n"
    "ds_read_b64_tr_b16 a[234:235], v208 offset:768\n"
    "s_add_u32 m0, s8, 4096\n"
    "s_mov_b32 s73, 32896\n"
    "buffer_load_dwordx4 v204, s[40:43], s73 offen offset:0 lds\n"
    "v_mfma_f32_16x16x32_bf16 v[84:87], a[244:247], a[184:187], v[84:87]\n"
    "ds_read_b128 a[200:203], v231 offset:57344\n"
    "v_mfma_f32_16x16x32_bf16 v[92:95], a[244:247], a[188:191], v[92:95]\n"
    "ds_read_b64_tr_b16 a[236:237], v209 offset:256\n"
    "v_mfma_f32_16x16x32_bf16 v[96:99], a[248:251], a[176:179], v[96:99]\n"
    "ds_read_b64_tr_b16 a[238:239], v209 offset:768\n"
    "buffer_load_dwordx4 v205, s[40:43], s73 offen offset:1024 lds\n"
    "v_mfma_f32_16x16x32_bf16 v[104:107], a[248:251], a[180:183], v[104:107]\n"
    "ds_read_b128 a[204:207], v231 offset:57600\n"
    "v_mfma_f32_16x16x32_bf16 v[112:115], a[248:251], a[184:187], v[112:115]\n"
    "v_mfma_f32_16x16x32_bf16 v[120:123], a[248:251], a[188:191], v[120:123]\n"
    "buffer_load_dwordx4 v206, s[40:43], s73 offen offset:2048 lds\n"
    "v_mfma_f32_16x16x32_bf16 v[100:103], a[252:255], a[176:179], v[100:103]\n"
    "v_mfma_f32_16x16x32_bf16 v[108:111], a[252:255], a[180:183], v[108:111]\n"
    "buffer_load_dwordx4 v207, s[40:43], s73 offen offset:3072 lds\n"
    "v_mfma_f32_16x16x32_bf16 v[116:119], a[252:255], a[184:187], v[116:119]\n"
    "v_mfma_f32_16x16x32_bf16 v[124:127], a[252:255], a[188:191], v[124:127]\n"
    "s_waitcnt vmcnt(16) lgkmcnt(0)\n"
    "v_mfma_f32_16x16x32_bf16 v[64:67], a[224:227], a[192:195], v[64:67]\n"
    "ds_read_b64_tr_b16 a[240:241], v208 offset:4096\n"
    "ds_read_b64_tr_b16 a[242:243], v208 offset:4608\n"
    "s_lshl_b32 s86, s31, 2\n"
    "v_mfma_f32_16x16x32_bf16 v[72:75], a[224:227], a[196:199], v[72:75]\n"
    "ds_read_b128 a[208:211], v231 offset:26624\n"
    "ds_read_b64_tr_b16 a[244:245], v209 offset:4096\n"
    "s_add_u32 s86, s86, s30\n"
    "v_mfma_f32_16x16x32_bf16 v[80:83], a[224:227], a[200:203], v[80:83]\n"
    "ds_read_b64_tr_b16 a[246:247], v209 offset:4608\n"
    "s_mul_i32 s86, s86, 17408\n"
    "v_mfma_f32_16x16x32_bf16 v[88:91], a[224:227], a[204:207], v[88:91]\n"
    "ds_read_b128 a[212:215], v231 offset:26880\n"
    "s_add_u32 s86, s86, 13107200\n"
    "v_mfma_f32_16x16x32_bf16 v[68:71], a[228:231], a[192:195], v[68:71]\n"
    "ds_read_b64_tr_b16 a[248:249], v208 offset:4352\n"
    "s_add_u32 s7, s86, 4096\n"
    "v_mfma_f32_16x16x32_bf16 v[76:79], a[228:231], a[196:199], v[76:79]\n"
    "ds_read_b64_tr_b16 a[250:251], v208 offset:4864\n"
    "s_add_u32 m0, s72, 0\n"
    "s_mov_b32 s73, 65664\n"
    "buffer_load_dwordx4 v204, s[40:43], s73 offen offset:0 lds\n"
    "s_lshl_b32 s75, s30, 13\n"
    "v_mfma_f32_16x16x32_bf16 v[84:87], a[228:231], a[200:203], v[84:87]\n"
    "ds_read_b128 a[216:219], v231 offset:59392\n"
    "s_lshl_b32 s87, s34, 5\n"
    "v_mfma_f32_16x16x32_bf16 v[92:95], a[228:231], a[204:207], v[92:95]\n"
    "ds_read_b64_tr_b16 a[252:253], v209 offset:4352\n"
    "s_add_u32 s87, s87, s30\n"
    "v_mfma_f32_16x16x32_bf16 v[96:99], a[232:235], a[192:195], v[96:99]\n"
    "ds_read_b64_tr_b16 a[254:255], v209 offset:4864\n"
    "buffer_load_dwordx4 v205, s[40:43], s73 offen offset:1024 lds\n"
    "s_lshl_b32 s88, s34, 2\n"
    "v_mfma_f32_16x16x32_bf16 v[104:107], a[232:235], a[196:199], v[104:107]\n"
    "ds_read_b128 a[220:223], v231 offset:59648\n"
    "s_add_u32 s88, s88, s30\n"
    "v_mfma_f32_16x16x32_bf16 v[112:115], a[232:235], a[200:203], v[112:115]\n"
    "s_lshl_b32 s88, s88, 8\n"
    "v_mfma_f32_16x16x32_bf16 v[120:123], a[232:235], a[204:207], v[120:123]\n"
    "buffer_load_dwordx4 v206, s[40:43], s73 offen offset:2048 lds\n"
    "s_mul_i32 s87, s87, 17408\n"
    "v_mfma_f32_16x16x32_bf16 v[100:103], a[236:239], a[192:195], v[100:103]\n"
    "s_add_u32 s87, s87, 13107200\n"
    "v_mfma_f32_16x16x32_bf16 v[108:111], a[236:239], a[196:199], v[108:111]\n"
    "buffer_load_dwordx4 v207, s[40:43], s73 offen offset:3072 lds\n"
    "s_lshl_b32 s97, s30, 9\n"
    "v_mfma_f32_16x16x32_bf16 v[116:119], a[236:239], a[200:203], v[116:119]\n"
    "s_add_u32 s97, s97, 8192\n"
    "v_mfma_f32_16x16x32_bf16 v[124:127], a[236:239], a[204:207], v[124:127]\n"
    "s_waitcnt vmcnt(16) lgkmcnt(0)\n"
    "v_mfma_f32_16x16x32_bf16 v[64:67], a[240:243], a[208:211], v[64:67]\n"
    "ds_read_b64_tr_b16 a[224:225], v208 offset:8192\n"
    "ds_read_b64_tr_b16 a[226:227], v208 offset:8704\n"
    "s_mov_b32 s96, 4096\n"
    "v_mfma_f32_16x16x32_bf16 v[72:75], a[240:243], a[212:215], v[72:75]\n"
    "ds_read_b128 v[128:131], v231 offset:28672\n"
    "ds_read_b64_tr_b16 a[228:229], v209 offset:8192\n"
    "v_and_b32_e64 v247, 1, v254\n"
    "v_mfma_f32_16x16x32_bf16 v[80:83], a[240:243], a[216:219], v[80:83]\n"
    "ds_read_b64_tr_b16 a[230:231], v209 offset:8704\n"
    "v_lshl_or_b32 v247, v247, 6, v242\n"
    "v_mfma_f32_16x16x32_bf16 v[88:91], a[240:243], a[220:223], v[88:91]\n"
    "ds_read_b128 v[132:135], v231 offset:28928\n"
    "s_lshl_b32 s89, s33, 2\n"
    "v_mfma_f32_16x16x32_bf16 v[68:71], a[244:247], a[208:211], v[68:71]\n"
    "ds_read_b64_tr_b16 a[232:233], v208 offset:8448\n"
    "s_add_u32 s89, s89, s30\n"
    "v_mfma_f32_16x16x32_bf16 v[76:79], a[244:247], a[212:215], v[76:79]\n"
    "ds_read_b64_tr_b16 a[234:235], v208 offset:8960\n"
    "s_add_u32 m0, s72, 4096\n"
    "s_mov_b32 s73, 98432\n"
    "buffer_load_dwordx4 v204, s[40:43], s73 offen offset:0 lds\n"
    "s_lshl_b32 s89, s89, 8\n"
    "v_mfma_f32_16x16x32_bf16 v[84:87], a[244:247], a[216:219], v[84:87]\n"
    "ds_read_b128 v[136:139], v231 offset:61440\n"
    "s_lshl_b32 s69, s3, 3\n"
    "v_mfma_f32_16x16x32_bf16 v[92:95], a[244:247], a[220:223], v[92:95]\n"
    "ds_read_b64_tr_b16 a[236:237], v209 offset:8448\n"
    "s_add_u32 s89, s89, s69\n"
    "v_mfma_f32_16x16x32_bf16 v[96:99], a[248:251], a[208:211], v[96:99]\n"
    "ds_read_b64_tr_b16 a[238:239], v209 offset:8960\n"
    "buffer_load_dwordx4 v205, s[40:43], s73 offen offset:1024 lds\n"
    "s_add_u32 s89, s89, 65536\n"
    "v_mfma_f32_16x16x32_bf16 v[104:107], a[248:251], a[212:215], v[104:107]\n"
    "ds_read_b128 v[140:143], v231 offset:61696\n"
    "s_mov_b64 s[44:45], s[14:15]\n"
    "v_mfma_f32_16x16x32_bf16 v[112:115], a[248:251], a[216:219], v[112:115]\n"
    "s_mov_b32 s46, s9\n"
    "v_mfma_f32_16x16x32_bf16 v[120:123], a[248:251], a[220:223], v[120:123]\n"
    "buffer_load_dwordx4 v206, s[40:43], s73 offen offset:2048 lds\n"
    "s_add_u32 s11, s88, 65536\n"
    "v_mfma_f32_16x16x32_bf16 v[100:103], a[252:255], a[208:211], v[100:103]\n"
    "v_xor_b32 v227, 32, v230\n"
    "v_mfma_f32_16x16x32_bf16 v[108:111], a[252:255], a[212:215], v[108:111]\n"
    "buffer_load_dwordx4 v207, s[40:43], s73 offen offset:3072 lds\n"
    "v_xor_b32_e64 v233, 32, v228\n"
    "v_mfma_f32_16x16x32_bf16 v[116:119], a[252:255], a[216:219], v[116:119]\n"
    "v_mfma_f32_16x16x32_bf16 v[124:127], a[252:255], a[220:223], v[124:127]\n"
    "s_waitcnt vmcnt(16) lgkmcnt(0)\n"
    "v_mfma_f32_16x16x32_bf16 v[64:67], a[224:227], v[128:131], v[64:67]\n"
    "ds_read_b64_tr_b16 a[240:241], v208 offset:12288\n"
    "ds_read_b64_tr_b16 a[242:243], v208 offset:12800\n"
    "v_mfma_f32_16x16x32_bf16 v[72:75], a[224:227], v[132:135], v[72:75]\n"
    "ds_read_b128 v[144:147], v231 offset:30720\n"
    "ds_read_b64_tr_b16 a[244:245], v209 offset:12288\n"
    "v_mfma_f32_16x16x32_bf16 v[80:83], a[224:227], v[136:139], v[80:83]\n"
    "ds_read_b64_tr_b16 a[246:247], v209 offset:12800\n"
    "v_mfma_f32_16x16x32_bf16 v[88:91], a[224:227], v[140:143], v[88:91]\n"
    "ds_read_b128 v[148:151], v231 offset:30976\n"
    "v_mfma_f32_16x16x32_bf16 v[68:71], a[228:231], v[128:131], v[68:71]\n"
    "ds_read_b64_tr_b16 a[248:249], v208 offset:12544\n"
    "v_mfma_f32_16x16x32_bf16 v[76:79], a[228:231], v[132:135], v[76:79]\n"
    "ds_read_b64_tr_b16 a[250:251], v208 offset:13056\n"
    "s_add_u32 m0, s72, 8192\n"
    "s_mov_b32 s73, 131200\n"
    "buffer_load_dwordx4 v204, s[40:43], s73 offen offset:0 lds\n"
    "v_mfma_f32_16x16x32_bf16 v[84:87], a[228:231], v[136:139], v[84:87]\n"
    "ds_read_b128 v[152:155], v231 offset:63488\n"
    "v_mfma_f32_16x16x32_bf16 v[92:95], a[228:231], v[140:143], v[92:95]\n"
    "ds_read_b64_tr_b16 a[252:253], v209 offset:12544\n"
    "v_mfma_f32_16x16x32_bf16 v[96:99], a[232:235], v[128:131], v[96:99]\n"
    "ds_read_b64_tr_b16 a[254:255], v209 offset:13056\n"
    "buffer_load_dwordx4 v205, s[40:43], s73 offen offset:1024 lds\n"
    "v_mfma_f32_16x16x32_bf16 v[104:107], a[232:235], v[132:135], v[104:107]\n"
    "ds_read_b128 v[156:159], v231 offset:63744\n"
    "v_mfma_f32_16x16x32_bf16 v[112:115], a[232:235], v[136:139], v[112:115]\n"
    "v_mfma_f32_16x16x32_bf16 v[120:123], a[232:235], v[140:143], v[120:123]\n"
    "buffer_load_dwordx4 v206, s[40:43], s73 offen offset:2048 lds\n"
    "v_mfma_f32_16x16x32_bf16 v[100:103], a[236:239], v[128:131], v[100:103]\n"
    "v_mfma_f32_16x16x32_bf16 v[108:111], a[236:239], v[132:135], v[108:111]\n"
    "buffer_load_dwordx4 v207, s[40:43], s73 offen offset:3072 lds\n"
    "v_mfma_f32_16x16x32_bf16 v[116:119], a[236:239], v[136:139], v[116:119]\n"
    "v_mfma_f32_16x16x32_bf16 v[124:127], a[236:239], v[140:143], v[124:127]\n"
    "s_waitcnt vmcnt(16) lgkmcnt(0)\n"
    "v_mfma_f32_16x16x32_bf16 v[64:67], a[240:243], v[144:147], v[64:67]\n"
    "ds_read_b64_tr_b16 a[224:225], v202 offset:0\n"
    "ds_read_b64_tr_b16 a[226:227], v202 offset:512\n"
    "v_mfma_f32_16x16x32_bf16 v[72:75], a[240:243], v[148:151], v[72:75]\n"
    "ds_read_b64_tr_b16 a[228:229], v203 offset:0\n"
    "ds_read_b64_tr_b16 a[230:231], v203 offset:512\n"
    "v_mfma_f32_16x16x32_bf16 v[80:83], a[240:243], v[152:155], v[80:83]\n"
    "ds_read_b64_tr_b16 a[232:233], v202 offset:256\n"
    "v_mfma_f32_16x16x32_bf16 v[88:91], a[240:243], v[156:159], v[88:91]\n"
    "ds_read_b64_tr_b16 a[234:235], v202 offset:768\n"
    "v_mfma_f32_16x16x32_bf16 v[68:71], a[244:247], v[144:147], v[68:71]\n"
    "ds_read_b64_tr_b16 a[236:237], v203 offset:256\n"
    "v_mfma_f32_16x16x32_bf16 v[76:79], a[244:247], v[148:151], v[76:79]\n"
    "ds_read_b64_tr_b16 a[238:239], v203 offset:768\n"
    "s_add_u32 m0, s72, 12288\n"
    "s_mov_b32 s73, 163968\n"
    "buffer_load_dwordx4 v204, s[40:43], s73 offen offset:0 lds\n"
    "v_mfma_f32_16x16x32_bf16 v[84:87], a[244:247], v[152:155], v[84:87]\n"
    "v_mfma_f32_16x16x32_bf16 v[92:95], a[244:247], v[156:159], v[92:95]\n"
    "v_mfma_f32_16x16x32_bf16 v[96:99], a[248:251], v[144:147], v[96:99]\n"
    "buffer_load_dwordx4 v205, s[40:43], s73 offen offset:1024 lds\n"
    "v_mfma_f32_16x16x32_bf16 v[104:107], a[248:251], v[148:151], v[104:107]\n"
    "v_mfma_f32_16x16x32_bf16 v[112:115], a[248:251], v[152:155], v[112:115]\n"
    "v_mfma_f32_16x16x32_bf16 v[120:123], a[248:251], v[156:159], v[120:123]\n"
    "buffer_load_dwordx4 v206, s[40:43], s73 offen offset:2048 lds\n"
    "v_mfma_f32_16x16x32_bf16 v[100:103], a[252:255], v[144:147], v[100:103]\n"
    "v_mfma_f32_16x16x32_bf16 v[108:111], a[252:255], v[148:151], v[108:111]\n"
    "buffer_load_dwordx4 v207, s[40:43], s73 offen offset:3072 lds\n"
    "v_mfma_f32_16x16x32_bf16 v[116:119], a[252:255], v[152:155], v[116:119]\n"
    "v_mfma_f32_16x16x32_bf16 v[124:127], a[252:255], v[156:159], v[124:127]\n"
    "s_waitcnt vmcnt(16) lgkmcnt(0)\n"
    "v_mfma_f32_16x16x32_bf16 v[0:3], a[224:227], a[0:3], 0\n"
    "ds_read_b64_tr_b16 a[240:241], v202 offset:4096\n"
    "ds_read_b64_tr_b16 a[242:243], v202 offset:4608\n"
    "v_mfma_f32_16x16x32_bf16 v[8:11], a[224:227], a[4:7], 0\n"
    "ds_read_b64_tr_b16 a[244:245], v203 offset:4096\n"
    "ds_read_b64_tr_b16 a[246:247], v203 offset:4608\n"
    "v_mfma_f32_16x16x32_bf16 v[16:19], a[224:227], a[8:11], 0\n"
    "ds_read_b64_tr_b16 a[248:249], v202 offset:4352\n"
    "v_cvt_pk_bf16_f32 v64, v64, v65\n"
    "v_cvt_pk_bf16_f32 v65, v66, v67\n"
    "v_mfma_f32_16x16x32_bf16 v[24:27], a[224:227], a[12:15], 0\n"
    "ds_read_b64_tr_b16 a[250:251], v202 offset:4864\n"
    "v_cvt_pk_bf16_f32 v66, v68, v69\n"
    "v_cvt_pk_bf16_f32 v67, v70, v71\n"
    "v_mfma_f32_16x16x32_bf16 v[4:7], a[228:231], a[0:3], 0\n"
    "ds_read_b64_tr_b16 a[252:253], v203 offset:4352\n"
    "v_cvt_pk_bf16_f32 v68, v72, v73\n"
    "v_cvt_pk_bf16_f32 v69, v74, v75\n"
    "v_mfma_f32_16x16x32_bf16 v[12:15], a[228:231], a[4:7], 0\n"
    "ds_read_b64_tr_b16 a[254:255], v203 offset:4864\n"
    "s_add_u32 m0, s8, 0\n"
    "s_mov_b32 s73, 196736\n"
    "buffer_load_dwordx4 v204, s[40:43], s73 offen offset:0 lds\n"
    "v_cvt_pk_bf16_f32 v70, v76, v77\n"
    "v_cvt_pk_bf16_f32 v71, v78, v79\n"
    "v_mfma_f32_16x16x32_bf16 v[20:23], a[228:231], a[8:11], 0\n"
    "v_permlane16_swap_b32 v64, v66\n"
    "v_permlane16_swap_b32 v65, v67\n"
    "v_mfma_f32_16x16x32_bf16 v[28:31], a[228:231], a[12:15], 0\n"
    "v_permlane16_swap_b32 v68, v70\n"
    "v_permlane16_swap_b32 v69, v71\n"
    "v_mfma_f32_16x16x32_bf16 v[32:35], a[232:235], a[0:3], 0\n"
    "buffer_load_dwordx4 v205, s[40:43], s73 offen offset:1024 lds\n"
    "v_cvt_pk_bf16_f32 v72, v80, v81\n"
    "v_cvt_pk_bf16_f32 v73, v82, v83\n"
    "v_mfma_f32_16x16x32_bf16 v[40:43], a[232:235], a[4:7], 0\n"
    "v_cvt_pk_bf16_f32 v74, v84, v85\n"
    "v_cvt_pk_bf16_f32 v75, v86, v87\n"
    "v_mfma_f32_16x16x32_bf16 v[48:51], a[232:235], a[8:11], 0\n"
    "v_cvt_pk_bf16_f32 v76, v88, v89\n"
    "v_cvt_pk_bf16_f32 v77, v90, v91\n"
    "v_mfma_f32_16x16x32_bf16 v[56:59], a[232:235], a[12:15], 0\n"
    "buffer_load_dwordx4 v206, s[40:43], s73 offen offset:2048 lds\n"
    "v_cvt_pk_bf16_f32 v78, v92, v93\n"
    "v_cvt_pk_bf16_f32 v79, v94, v95\n"
    "v_mfma_f32_16x16x32_bf16 v[36:39], a[236:239], a[0:3], 0\n"
    "v_permlane16_swap_b32 v72, v74\n"
    "v_permlane16_swap_b32 v73, v75\n"
    "v_mfma_f32_16x16x32_bf16 v[44:47], a[236:239], a[4:7], 0\n"
    "buffer_load_dwordx4 v207, s[40:43], s73 offen offset:3072 lds\n"
    "v_permlane16_swap_b32 v76, v78\n"
    "v_mfma_f32_16x16x32_bf16 v[52:55], a[236:239], a[8:11], 0\n"
    "v_permlane16_swap_b32 v77, v79\n"
    "v_mfma_f32_16x16x32_bf16 v[60:63], a[236:239], a[12:15], 0\n"
    "s_waitcnt vmcnt(16) lgkmcnt(0)\n"
    "v_mfma_f32_16x16x32_bf16 v[0:3], a[240:243], a[16:19], v[0:3]\n"
    "ds_read_b64_tr_b16 a[224:225], v208 offset:0\n"
    "ds_read_b64_tr_b16 a[226:227], v208 offset:512\n"
    "v_cvt_pk_bf16_f32 v80, v96, v97\n"
    "v_cvt_pk_bf16_f32 v81, v98, v99\n"
    "v_mfma_f32_16x16x32_bf16 v[8:11], a[240:243], a[20:23], v[8:11]\n"
    "ds_read_b64_tr_b16 a[228:229], v209 offset:0\n"
    "ds_read_b64_tr_b16 a[230:231], v209 offset:512\n"
    "v_cvt_pk_bf16_f32 v82, v100, v101\n"
    "v_cvt_pk_bf16_f32 v83, v102, v103\n"
    "v_mfma_f32_16x16x32_bf16 v[16:19], a[240:243], a[24:27], v[16:19]\n"
    "ds_read_b64_tr_b16 a[232:233], v208 offset:256\n"
    "v_cvt_pk_bf16_f32 v84, v104, v105\n"
    "v_cvt_pk_bf16_f32 v85, v106, v107\n"
    "v_mfma_f32_16x16x32_bf16 v[24:27], a[240:243], a[28:31], v[24:27]\n"
    "ds_read_b64_tr_b16 a[234:235], v208 offset:768\n"
    "v_cvt_pk_bf16_f32 v86, v108, v109\n"
    "v_cvt_pk_bf16_f32 v87, v110, v111\n"
    "v_mfma_f32_16x16x32_bf16 v[4:7], a[244:247], a[16:19], v[4:7]\n"
    "ds_read_b64_tr_b16 a[236:237], v209 offset:256\n"
    "v_permlane16_swap_b32 v80, v82\n"
    "v_permlane16_swap_b32 v81, v83\n"
    "v_mfma_f32_16x16x32_bf16 v[12:15], a[244:247], a[20:23], v[12:15]\n"
    "ds_read_b64_tr_b16 a[238:239], v209 offset:768\n"
    "s_add_u32 m0, s8, 4096\n"
    "s_mov_b32 s73, 229504\n"
    "buffer_load_dwordx4 v204, s[40:43], s73 offen offset:0 lds\n"
    "v_permlane16_swap_b32 v84, v86\n"
    "v_permlane16_swap_b32 v85, v87\n"
    "v_mfma_f32_16x16x32_bf16 v[20:23], a[244:247], a[24:27], v[20:23]\n"
    "v_cvt_pk_bf16_f32 v88, v112, v113\n"
    "v_cvt_pk_bf16_f32 v89, v114, v115\n"
    "v_mfma_f32_16x16x32_bf16 v[28:31], a[244:247], a[28:31], v[28:31]\n"
    "v_cvt_pk_bf16_f32 v90, v116, v117\n"
    "v_cvt_pk_bf16_f32 v91, v118, v119\n"
    "buffer_store_dwordx4 v[64:67], v224, s[60:63], s86 offen offset:0 sc1\n"
    "v_mfma_f32_16x16x32_bf16 v[32:35], a[248:251], a[16:19], v[32:35]\n"
    "buffer_load_dwordx4 v205, s[40:43], s73 offen offset:1024 lds\n"
    "v_cvt_pk_bf16_f32 v92, v120, v121\n"
    "v_cvt_pk_bf16_f32 v93, v122, v123\n"
    "v_mfma_f32_16x16x32_bf16 v[40:43], a[248:251], a[20:23], v[40:43]\n"
    "v_cvt_pk_bf16_f32 v94, v124, v125\n"
    "v_mfma_f32_16x16x32_bf16 v[48:51], a[248:251], a[24:27], v[48:51]\n"
    "v_cvt_pk_bf16_f32 v95, v126, v127\n"
    "v_mfma_f32_16x16x32_bf16 v[56:59], a[248:251], a[28:31], v[56:59]\n"
    "buffer_load_dwordx4 v206, s[40:43], s73 offen offset:2048 lds\n"
    "v_permlane16_swap_b32 v88, v90\n"
    "v_mfma_f32_16x16x32_bf16 v[36:39], a[252:255], a[16:19], v[36:39]\n"
    "v_permlane16_swap_b32 v89, v91\n"
    "v_mfma_f32_16x16x32_bf16 v[44:47], a[252:255], a[20:23], v[44:47]\n"
    "buffer_load_dwordx4 v207, s[40:43], s73 offen offset:3072 lds\n"
    "v_permlane16_swap_b32 v92, v94\n"
    "v_mfma_f32_16x16x32_bf16 v[52:55], a[252:255], a[24:27], v[52:55]\n"
    "v_permlane16_swap_b32 v93, v95\n"
    "v_mfma_f32_16x16x32_bf16 v[60:63], a[252:255], a[28:31], v[60:63]\n"
    "buffer_store_dwordx4 v[68:71], v224, s[60:63], s86 offen offset:1024 sc1\n"
    "s_waitcnt vmcnt(18) lgkmcnt(0)\n"
    "v_mfma_f32_16x16x32_bf16 v[0:3], a[224:227], a[32:35], v[0:3]\n"
    "ds_read_b64_tr_b16 a[240:241], v208 offset:4096\n"
    "ds_read_b64_tr_b16 a[242:243], v208 offset:4608\n"
    "v_mfma_f32_16x16x32_bf16 v[8:11], a[224:227], a[36:39], v[8:11]\n"
    "ds_read_b64_tr_b16 a[244:245], v209 offset:4096\n"
    "ds_read_b64_tr_b16 a[246:247], v209 offset:4608\n"
    "v_mfma_f32_16x16x32_bf16 v[16:19], a[224:227], a[40:43], v[16:19]\n"
    "ds_read_b64_tr_b16 a[248:249], v208 offset:4352\n"
    "v_mfma_f32_16x16x32_bf16 v[24:27], a[224:227], a[44:47], v[24:27]\n"
    "ds_read_b64_tr_b16 a[250:251], v208 offset:4864\n"
    "v_mfma_f32_16x16x32_bf16 v[4:7], a[228:231], a[32:35], v[4:7]\n"
    "ds_read_b64_tr_b16 a[252:253], v209 offset:4352\n"
    "v_mfma_f32_16x16x32_bf16 v[12:15], a[228:231], a[36:39], v[12:15]\n"
    "ds_read_b64_tr_b16 a[254:255], v209 offset:4864\n"
    "s_add_u32 m0, s72, 0\n"
    "s_mov_b32 s73, 262272\n"
    "buffer_load_dwordx4 v204, s[40:43], s73 offen offset:0 lds\n"
    "v_mfma_f32_16x16x32_bf16 v[20:23], a[228:231], a[40:43], v[20:23]\n"
    "v_mfma_f32_16x16x32_bf16 v[28:31], a[228:231], a[44:47], v[28:31]\n"
    "buffer_store_dwordx4 v[72:75], v224, s[60:63], s86 offen offset:2048 sc1\n"
    "v_mfma_f32_16x16x32_bf16 v[32:35], a[232:235], a[32:35], v[32:35]\n"
    "buffer_load_dwordx4 v205, s[40:43], s73 offen offset:1024 lds\n"
    "v_mfma_f32_16x16x32_bf16 v[40:43], a[232:235], a[36:39], v[40:43]\n"
    "v_mfma_f32_16x16x32_bf16 v[48:51], a[232:235], a[40:43], v[48:51]\n"
    "v_mfma_f32_16x16x32_bf16 v[56:59], a[232:235], a[44:47], v[56:59]\n"
    "buffer_load_dwordx4 v206, s[40:43], s73 offen offset:2048 lds\n"
    "v_mfma_f32_16x16x32_bf16 v[36:39], a[236:239], a[32:35], v[36:39]\n"
    "v_mfma_f32_16x16x32_bf16 v[44:47], a[236:239], a[36:39], v[44:47]\n"
    "buffer_load_dwordx4 v207, s[40:43], s73 offen offset:3072 lds\n"
    "v_mfma_f32_16x16x32_bf16 v[52:55], a[236:239], a[40:43], v[52:55]\n"
    "v_mfma_f32_16x16x32_bf16 v[60:63], a[236:239], a[44:47], v[60:63]\n"
    "buffer_store_dwordx4 v[76:79], v224, s[60:63], s86 offen offset:3072 sc1\n"
    "s_waitcnt vmcnt(20) lgkmcnt(0)\n"
    "v_mfma_f32_16x16x32_bf16 v[0:3], a[240:243], a[48:51], v[0:3]\n"
    "ds_read_b64_tr_b16 a[224:225], v208 offset:8192\n"
    "ds_read_b64_tr_b16 a[226:227], v208 offset:8704\n"
    "v_mfma_f32_16x16x32_bf16 v[8:11], a[240:243], a[52:55], v[8:11]\n"
    "ds_read_b64_tr_b16 a[228:229], v209 offset:8192\n"
    "ds_read_b64_tr_b16 a[230:231], v209 offset:8704\n"
    "v_mfma_f32_16x16x32_bf16 v[16:19], a[240:243], a[56:59], v[16:19]\n"
    "ds_read_b64_tr_b16 a[232:233], v208 offset:8448\n"
    "v_mfma_f32_16x16x32_bf16 v[24:27], a[240:243], a[60:63], v[24:27]\n"
    "ds_read_b64_tr_b16 a[234:235], v208 offset:8960\n"
    "v_mfma_f32_16x16x32_bf16 v[4:7], a[244:247], a[48:51], v[4:7]\n"
    "ds_read_b64_tr_b16 a[236:237], v209 offset:8448\n"
    "v_mfma_f32_16x16x32_bf16 v[12:15], a[244:247], a[52:55], v[12:15]\n"
    "ds_read_b64_tr_b16 a[238:239], v209 offset:8960\n"
    "s_add_u32 m0, s72, 4096\n"
    "s_mov_b32 s73, 295040\n"
    "buffer_load_dwordx4 v204, s[40:43], s73 offen offset:0 lds\n"
    "v_mfma_f32_16x16x32_bf16 v[20:23], a[244:247], a[56:59], v[20:23]\n"
    "v_mfma_f32_16x16x32_bf16 v[28:31], a[244:247], a[60:63], v[28:31]\n"
    "buffer_store_dwordx4 v[80:83], v224, s[60:63], s7 offen offset:0 sc1\n"
    "v_mfma_f32_16x16x32_bf16 v[32:35], a[248:251], a[48:51], v[32:35]\n"
    "buffer_load_dwordx4 v205, s[40:43], s73 offen offset:1024 lds\n"
    "v_mfma_f32_16x16x32_bf16 v[40:43], a[248:251], a[52:55], v[40:43]\n"
    "v_mfma_f32_16x16x32_bf16 v[48:51], a[248:251], a[56:59], v[48:51]\n"
    "v_mfma_f32_16x16x32_bf16 v[56:59], a[248:251], a[60:63], v[56:59]\n"
    "buffer_load_dwordx4 v206, s[40:43], s73 offen offset:2048 lds\n"
    "v_mfma_f32_16x16x32_bf16 v[36:39], a[252:255], a[48:51], v[36:39]\n"
    "v_mfma_f32_16x16x32_bf16 v[44:47], a[252:255], a[52:55], v[44:47]\n"
    "buffer_load_dwordx4 v207, s[40:43], s73 offen offset:3072 lds\n"
    "v_mfma_f32_16x16x32_bf16 v[52:55], a[252:255], a[56:59], v[52:55]\n"
    "v_mfma_f32_16x16x32_bf16 v[60:63], a[252:255], a[60:63], v[60:63]\n"
    "buffer_store_dwordx4 v[84:87], v224, s[60:63], s7 offen offset:1024 sc1\n"
    "s_waitcnt vmcnt(22) lgkmcnt(0)\n"
    "v_mfma_f32_16x16x32_bf16 v[0:3], a[224:227], a[64:67], v[0:3]\n"
    "ds_read_b64_tr_b16 a[240:241], v208 offset:12288\n"
    "ds_read_b64_tr_b16 a[242:243], v208 offset:12800\n"
    "v_mfma_f32_16x16x32_bf16 v[8:11], a[224:227], a[68:71], v[8:11]\n"
    "ds_read_b64_tr_b16 a[244:245], v209 offset:12288\n"
    "ds_read_b64_tr_b16 a[246:247], v209 offset:12800\n"
    "v_mfma_f32_16x16x32_bf16 v[16:19], a[224:227], a[72:75], v[16:19]\n"
    "ds_read_b64_tr_b16 a[248:249], v208 offset:12544\n"
    "v_mfma_f32_16x16x32_bf16 v[24:27], a[224:227], a[76:79], v[24:27]\n"
    "ds_read_b64_tr_b16 a[250:251], v208 offset:13056\n"
    "v_mfma_f32_16x16x32_bf16 v[4:7], a[228:231], a[64:67], v[4:7]\n"
    "ds_read_b64_tr_b16 a[252:253], v209 offset:12544\n"
    "v_mfma_f32_16x16x32_bf16 v[12:15], a[228:231], a[68:71], v[12:15]\n"
    "ds_read_b64_tr_b16 a[254:255], v209 offset:13056\n"
    "s_add_u32 m0, s72, 8192\n"
    "s_mov_b32 s73, 327808\n"
    "buffer_load_dwordx4 v204, s[40:43], s73 offen offset:0 lds\n"
    "v_mfma_f32_16x16x32_bf16 v[20:23], a[228:231], a[72:75], v[20:23]\n"
    "v_mfma_f32_16x16x32_bf16 v[28:31], a[228:231], a[76:79], v[28:31]\n"
    "buffer_store_dwordx4 v[88:91], v224, s[60:63], s7 offen offset:2048 sc1\n"
    "v_mfma_f32_16x16x32_bf16 v[32:35], a[232:235], a[64:67], v[32:35]\n"
    "buffer_load_dwordx4 v205, s[40:43], s73 offen offset:1024 lds\n"
    "v_mfma_f32_16x16x32_bf16 v[40:43], a[232:235], a[68:71], v[40:43]\n"
    "v_mfma_f32_16x16x32_bf16 v[48:51], a[232:235], a[72:75], v[48:51]\n"
    "v_mfma_f32_16x16x32_bf16 v[56:59], a[232:235], a[76:79], v[56:59]\n"
    "buffer_load_dwordx4 v206, s[40:43], s73 offen offset:2048 lds\n"
    "v_mfma_f32_16x16x32_bf16 v[36:39], a[236:239], a[64:67], v[36:39]\n"
    "v_mfma_f32_16x16x32_bf16 v[44:47], a[236:239], a[68:71], v[44:47]\n"
    "buffer_load_dwordx4 v207, s[40:43], s73 offen offset:3072 lds\n"
    "v_mfma_f32_16x16x32_bf16 v[52:55], a[236:239], a[72:75], v[52:55]\n"
    "v_mfma_f32_16x16x32_bf16 v[60:63], a[236:239], a[76:79], v[60:63]\n"
    "buffer_store_dwordx4 v[92:95], v224, s[60:63], s7 offen offset:3072 sc1\n"
    "s_waitcnt vmcnt(24) lgkmcnt(0)\n"
    "v_mfma_f32_16x16x32_bf16 v[0:3], a[240:243], a[80:83], v[0:3]\n"
    "ds_read_b64_tr_b16 a[224:225], v202 offset:0\n"
    "ds_read_b64_tr_b16 a[226:227], v202 offset:512\n"
    "v_mfma_f32_16x16x32_bf16 v[8:11], a[240:243], a[84:87], v[8:11]\n"
    "ds_read_b64_tr_b16 a[228:229], v203 offset:0\n"
    "ds_read_b64_tr_b16 a[230:231], v203 offset:512\n"
    "v_mfma_f32_16x16x32_bf16 v[16:19], a[240:243], a[88:91], v[16:19]\n"
    "ds_read_b64_tr_b16 a[232:233], v202 offset:256\n"
    "v_mfma_f32_16x16x32_bf16 v[24:27], a[240:243], a[92:95], v[24:27]\n"
    "ds_read_b64_tr_b16 a[234:235], v202 offset:768\n"
    "v_mfma_f32_16x16x32_bf16 v[4:7], a[244:247], a[80:83], v[4:7]\n"
    "ds_read_b64_tr_b16 a[236:237], v203 offset:256\n"
    "v_mfma_f32_16x16x32_bf16 v[12:15], a[244:247], a[84:87], v[12:15]\n"
    "ds_read_b64_tr_b16 a[238:239], v203 offset:768\n"
    "s_add_u32 m0, s72, 12288\n"
    "s_mov_b32 s73, 360576\n"
    "buffer_load_dwordx4 v204, s[40:43], s73 offen offset:0 lds\n"
    "v_mfma_f32_16x16x32_bf16 v[20:23], a[244:247], a[88:91], v[20:23]\n"
    "v_mfma_f32_16x16x32_bf16 v[28:31], a[244:247], a[92:95], v[28:31]\n"
    "v_mfma_f32_16x16x32_bf16 v[32:35], a[248:251], a[80:83], v[32:35]\n"
    "buffer_load_dwordx4 v205, s[40:43], s73 offen offset:1024 lds\n"
    "v_mfma_f32_16x16x32_bf16 v[40:43], a[248:251], a[84:87], v[40:43]\n"
    "v_mfma_f32_16x16x32_bf16 v[48:51], a[248:251], a[88:91], v[48:51]\n"
    "v_mfma_f32_16x16x32_bf16 v[56:59], a[248:251], a[92:95], v[56:59]\n"
    "buffer_load_dwordx4 v206, s[40:43], s73 offen offset:2048 lds\n"
    "v_mfma_f32_16x16x32_bf16 v[36:39], a[252:255], a[80:83], v[36:39]\n"
    "v_mfma_f32_16x16x32_bf16 v[44:47], a[252:255], a[84:87], v[44:47]\n"
    "buffer_load_dwordx4 v207, s[40:43], s73 offen offset:3072 lds\n"
    "v_mfma_f32_16x16x32_bf16 v[52:55], a[252:255], a[88:91], v[52:55]\n"
    "v_mfma_f32_16x16x32_bf16 v[60:63], a[252:255], a[92:95], v[60:63]\n"
    "s_waitcnt vmcnt(23) lgkmcnt(0)\n"
    "v_mfma_f32_16x16x32_bf16 v[0:3], a[224:227], a[96:99], v[0:3]\n"
    "ds_read_b64_tr_b16 a[240:241], v202 offset:4096\n"
    "ds_read_b64_tr_b16 a[242:243], v202 offset:4608\n"
    "v_mfma_f32_16x16x32_bf16 v[8:11], a[224:227], a[100:103], v[8:11]\n"
    "ds_read_b64_tr_b16 a[244:245], v203 offset:4096\n"
    "ds_read_b64_tr_b16 a[246:247], v203 offset:4608\n"
    "v_mfma_f32_16x16x32_bf16 v[16:19], a[224:227], a[104:107], v[16:19]\n"
    "ds_read_b64_tr_b16 a[248:249], v202 offset:4352\n"
    "v_mfma_f32_16x16x32_bf16 v[24:27], a[224:227], a[108:111], v[24:27]\n"
    "ds_read_b64_tr_b16 a[250:251], v202 offset:4864\n"
    "v_mfma_f32_16x16x32_bf16 v[4:7], a[228:231], a[96:99], v[4:7]\n"
    "ds_read_b64_tr_b16 a[252:253], v203 offset:4352\n"
    "v_mfma_f32_16x16x32_bf16 v[12:15], a[228:231], a[100:103], v[12:15]\n"
    "ds_read_b64_tr_b16 a[254:255], v203 offset:4864\n"
    "s_add_u32 m0, s8, 0\n"
    "s_mov_b32 s73, 393344\n"
    "buffer_load_dwordx4 v204, s[40:43], s73 offen offset:0 lds\n"
    "v_mfma_f32_16x16x32_bf16 v[20:23], a[228:231], a[104:107], v[20:23]\n"
    "v_mfma_f32_16x16x32_bf16 v[28:31], a[228:231], a[108:111], v[28:31]\n"
    "v_mfma_f32_16x16x32_bf16 v[32:35], a[232:235], a[96:99], v[32:35]\n"
    "buffer_load_dwordx4 v205, s[40:43], s73 offen offset:1024 lds\n"
    "v_mfma_f32_16x16x32_bf16 v[40:43], a[232:235], a[100:103], v[40:43]\n"
    "v_mfma_f32_16x16x32_bf16 v[48:51], a[232:235], a[104:107], v[48:51]\n"
    "v_mfma_f32_16x16x32_bf16 v[56:59], a[232:235], a[108:111], v[56:59]\n"
    "buffer_load_dwordx4 v206, s[40:43], s73 offen offset:2048 lds\n"
    "v_mfma_f32_16x16x32_bf16 v[36:39], a[236:239], a[96:99], v[36:39]\n"
    "v_mfma_f32_16x16x32_bf16 v[44:47], a[236:239], a[100:103], v[44:47]\n"
    "buffer_load_dwordx4 v207, s[40:43], s73 offen offset:3072 lds\n"
    "v_mfma_f32_16x16x32_bf16 v[52:55], a[236:239], a[104:107], v[52:55]\n"
    "v_mfma_f32_16x16x32_bf16 v[60:63], a[236:239], a[108:111], v[60:63]\n"
    "s_waitcnt vmcnt(21) lgkmcnt(0)\n"
    "v_mfma_f32_16x16x32_bf16 v[0:3], a[240:243], a[112:115], v[0:3]\n"
    "ds_read_b64_tr_b16 a[224:225], v208 offset:0\n"
    "ds_read_b64_tr_b16 a[226:227], v208 offset:512\n"
    "v_mfma_f32_16x16x32_bf16 v[8:11], a[240:243], a[116:119], v[8:11]\n"
    "ds_read_b64_tr_b16 a[228:229], v209 offset:0\n"
    "ds_read_b64_tr_b16 a[230:231], v209 offset:512\n"
    "v_mfma_f32_16x16x32_bf16 v[16:19], a[240:243], a[120:123], v[16:19]\n"
    "ds_read_b64_tr_b16 a[232:233], v208 offset:256\n"
    "v_mfma_f32_16x16x32_bf16 v[24:27], a[240:243], a[124:127], v[24:27]\n"
    "ds_read_b64_tr_b16 a[234:235], v208 offset:768\n"
    "v_mfma_f32_16x16x32_bf16 v[4:7], a[244:247], a[112:115], v[4:7]\n"
    "ds_read_b64_tr_b16 a[236:237], v209 offset:256\n"
    "v_mfma_f32_16x16x32_bf16 v[12:15], a[244:247], a[116:119], v[12:15]\n"
    "ds_read_b64_tr_b16 a[238:239], v209 offset:768\n"
    "s_add_u32 m0, s8, 4096\n"
    "s_mov_b32 s73, 426112\n"
    "buffer_load_dwordx4 v204, s[40:43], s73 offen offset:0 lds\n"
    "v_mfma_f32_16x16x32_bf16 v[20:23], a[244:247], a[120:123], v[20:23]\n"
    "v_mfma_f32_16x16x32_bf16 v[28:31], a[244:247], a[124:127], v[28:31]\n"
    "v_mfma_f32_16x16x32_bf16 v[32:35], a[248:251], a[112:115], v[32:35]\n"
    "buffer_load_dwordx4 v205, s[40:43], s73 offen offset:1024 lds\n"
    "v_mfma_f32_16x16x32_bf16 v[40:43], a[248:251], a[116:119], v[40:43]\n"
    "v_mfma_f32_16x16x32_bf16 v[48:51], a[248:251], a[120:123], v[48:51]\n"
    "v_mfma_f32_16x16x32_bf16 v[56:59], a[248:251], a[124:127], v[56:59]\n"
    "buffer_load_dwordx4 v206, s[40:43], s73 offen offset:2048 lds\n"
    "v_mfma_f32_16x16x32_bf16 v[36:39], a[252:255], a[112:115], v[36:39]\n"
    "v_mfma_f32_16x16x32_bf16 v[44:47], a[252:255], a[116:119], v[44:47]\n"
    "buffer_load_dwordx4 v207, s[40:43], s73 offen offset:3072 lds\n"
    "v_mfma_f32_16x16x32_bf16 v[52:55], a[252:255], a[120:123], v[52:55]\n"
    "v_mfma_f32_16x16x32_bf16 v[60:63], a[252:255], a[124:127], v[60:63]\n"
    "s_waitcnt vmcnt(19) lgkmcnt(0)\n"
    "v_mfma_f32_16x16x32_bf16 v[0:3], a[224:227], a[128:131], v[0:3]\n"
    "ds_read_b64_tr_b16 a[240:241], v208 offset:4096\n"
    "ds_read_b64_tr_b16 a[242:243], v208 offset:4608\n"
    "v_mfma_f32_16x16x32_bf16 v[8:11], a[224:227], a[132:135], v[8:11]\n"
    "ds_read_b64_tr_b16 a[244:245], v209 offset:4096\n"
    "ds_read_b64_tr_b16 a[246:247], v209 offset:4608\n"
    "v_mfma_f32_16x16x32_bf16 v[16:19], a[224:227], a[136:139], v[16:19]\n"
    "ds_read_b64_tr_b16 a[248:249], v208 offset:4352\n"
    "v_mfma_f32_16x16x32_bf16 v[24:27], a[224:227], a[140:143], v[24:27]\n"
    "ds_read_b64_tr_b16 a[250:251], v208 offset:4864\n"
    "v_mfma_f32_16x16x32_bf16 v[4:7], a[228:231], a[128:131], v[4:7]\n"
    "ds_read_b64_tr_b16 a[252:253], v209 offset:4352\n"
    "v_mfma_f32_16x16x32_bf16 v[12:15], a[228:231], a[132:135], v[12:15]\n"
    "ds_read_b64_tr_b16 a[254:255], v209 offset:4864\n"
    "s_add_u32 m0, s72, 0\n"
    "s_mov_b32 s73, 458880\n"
    "buffer_load_dwordx4 v204, s[40:43], s73 offen offset:0 lds\n"
    "v_mfma_f32_16x16x32_bf16 v[20:23], a[228:231], a[136:139], v[20:23]\n"
    "v_mfma_f32_16x16x32_bf16 v[28:31], a[228:231], a[140:143], v[28:31]\n"
    "v_mfma_f32_16x16x32_bf16 v[32:35], a[232:235], a[128:131], v[32:35]\n"
    "buffer_load_dwordx4 v205, s[40:43], s73 offen offset:1024 lds\n"
    "v_mfma_f32_16x16x32_bf16 v[40:43], a[232:235], a[132:135], v[40:43]\n"
    "v_mfma_f32_16x16x32_bf16 v[48:51], a[232:235], a[136:139], v[48:51]\n"
    "v_mfma_f32_16x16x32_bf16 v[56:59], a[232:235], a[140:143], v[56:59]\n"
    "buffer_load_dwordx4 v206, s[40:43], s73 offen offset:2048 lds\n"
    "v_mfma_f32_16x16x32_bf16 v[36:39], a[236:239], a[128:131], v[36:39]\n"
    "v_mfma_f32_16x16x32_bf16 v[44:47], a[236:239], a[132:135], v[44:47]\n"
    "buffer_load_dwordx4 v207, s[40:43], s73 offen offset:3072 lds\n"
    "v_mfma_f32_16x16x32_bf16 v[52:55], a[236:239], a[136:139], v[52:55]\n"
    "v_mfma_f32_16x16x32_bf16 v[60:63], a[236:239], a[140:143], v[60:63]\n"
    "s_waitcnt vmcnt(17) lgkmcnt(0)\n"
    "v_mfma_f32_16x16x32_bf16 v[0:3], a[240:243], a[144:147], v[0:3]\n"
    "ds_read_b64_tr_b16 a[224:225], v208 offset:8192\n"
    "ds_read_b64_tr_b16 a[226:227], v208 offset:8704\n"
    "v_mfma_f32_16x16x32_bf16 v[8:11], a[240:243], a[148:151], v[8:11]\n"
    "ds_read_b64_tr_b16 a[228:229], v209 offset:8192\n"
    "ds_read_b64_tr_b16 a[230:231], v209 offset:8704\n"
    "v_mfma_f32_16x16x32_bf16 v[16:19], a[240:243], a[152:155], v[16:19]\n"
    "ds_read_b64_tr_b16 a[232:233], v208 offset:8448\n"
    "v_mfma_f32_16x16x32_bf16 v[24:27], a[240:243], a[156:159], v[24:27]\n"
    "ds_read_b64_tr_b16 a[234:235], v208 offset:8960\n"
    "v_mfma_f32_16x16x32_bf16 v[4:7], a[244:247], a[144:147], v[4:7]\n"
    "ds_read_b64_tr_b16 a[236:237], v209 offset:8448\n"
    "v_mfma_f32_16x16x32_bf16 v[12:15], a[244:247], a[148:151], v[12:15]\n"
    "ds_read_b64_tr_b16 a[238:239], v209 offset:8960\n"
    "s_add_u32 m0, s72, 4096\n"
    "s_mov_b32 s73, 491648\n"
    "buffer_load_dwordx4 v204, s[40:43], s73 offen offset:0 lds\n"
    "v_mfma_f32_16x16x32_bf16 v[20:23], a[244:247], a[152:155], v[20:23]\n"
    "v_mfma_f32_16x16x32_bf16 v[28:31], a[244:247], a[156:159], v[28:31]\n"
    "v_mfma_f32_16x16x32_bf16 v[32:35], a[248:251], a[144:147], v[32:35]\n"
    "buffer_load_dwordx4 v205, s[40:43], s73 offen offset:1024 lds\n"
    "v_mfma_f32_16x16x32_bf16 v[40:43], a[248:251], a[148:151], v[40:43]\n"
    "v_mfma_f32_16x16x32_bf16 v[48:51], a[248:251], a[152:155], v[48:51]\n"
    "v_mfma_f32_16x16x32_bf16 v[56:59], a[248:251], a[156:159], v[56:59]\n"
    "buffer_load_dwordx4 v206, s[40:43], s73 offen offset:2048 lds\n"
    "v_mfma_f32_16x16x32_bf16 v[36:39], a[252:255], a[144:147], v[36:39]\n"
    "v_mfma_f32_16x16x32_bf16 v[44:47], a[252:255], a[148:151], v[44:47]\n"
    "buffer_load_dwordx4 v207, s[40:43], s73 offen offset:3072 lds\n"
    "v_mfma_f32_16x16x32_bf16 v[52:55], a[252:255], a[152:155], v[52:55]\n"
    "v_mfma_f32_16x16x32_bf16 v[60:63], a[252:255], a[156:159], v[60:63]\n"
    "s_waitcnt vmcnt(16) lgkmcnt(0)\n"
    "v_mfma_f32_16x16x32_bf16 v[0:3], a[224:227], a[160:163], v[0:3]\n"
    "ds_read_b64_tr_b16 a[240:241], v208 offset:12288\n"
    "ds_read_b64_tr_b16 a[242:243], v208 offset:12800\n"
    "v_mfma_f32_16x16x32_bf16 v[8:11], a[224:227], a[164:167], v[8:11]\n"
    "ds_read_b64_tr_b16 a[244:245], v209 offset:12288\n"
    "ds_read_b64_tr_b16 a[246:247], v209 offset:12800\n"
    "v_mfma_f32_16x16x32_bf16 v[16:19], a[224:227], a[168:171], v[16:19]\n"
    "ds_read_b64_tr_b16 a[248:249], v208 offset:12544\n"
    "v_mfma_f32_16x16x32_bf16 v[24:27], a[224:227], a[172:175], v[24:27]\n"
    "ds_read_b64_tr_b16 a[250:251], v208 offset:13056\n"
    "v_mfma_f32_16x16x32_bf16 v[4:7], a[228:231], a[160:163], v[4:7]\n"
    "ds_read_b64_tr_b16 a[252:253], v209 offset:12544\n"
    "v_mfma_f32_16x16x32_bf16 v[12:15], a[228:231], a[164:167], v[12:15]\n"
    "ds_read_b64_tr_b16 a[254:255], v209 offset:13056\n"
    "s_add_u32 m0, s72, 8192\n"
    "s_add_u32 s73, s37, 0\n"
    "buffer_load_dwordx4 v230, s[44:47], s73 offen offset:0 lds\n"
    "v_mfma_f32_16x16x32_bf16 v[20:23], a[228:231], a[168:171], v[20:23]\n"
    "v_mfma_f32_16x16x32_bf16 v[28:31], a[228:231], a[172:175], v[28:31]\n"
    "v_mfma_f32_16x16x32_bf16 v[32:35], a[232:235], a[160:163], v[32:35]\n"
    "s_add_u32 s73, s37, 27648\n"
    "buffer_load_dwordx4 v227, s[44:47], s73 offen offset:1024 lds\n"
    "v_mfma_f32_16x16x32_bf16 v[40:43], a[232:235], a[164:167], v[40:43]\n"
    "v_mfma_f32_16x16x32_bf16 v[48:51], a[232:235], a[168:171], v[48:51]\n"
    "v_mfma_f32_16x16x32_bf16 v[56:59], a[232:235], a[172:175], v[56:59]\n"
    "s_add_u32 s73, s37, 55296\n"
    "buffer_load_dwordx4 v230, s[44:47], s73 offen offset:2048 lds\n"
    "v_mfma_f32_16x16x32_bf16 v[36:39], a[236:239], a[160:163], v[36:39]\n"
    "v_mfma_f32_16x16x32_bf16 v[44:47], a[236:239], a[164:167], v[44:47]\n"
    "s_add_u32 s73, s37, 82944\n"
    "buffer_load_dwordx4 v227, s[44:47], s73 offen offset:3072 lds\n"
    "v_mfma_f32_16x16x32_bf16 v[52:55], a[236:239], a[168:171], v[52:55]\n"
    "v_mfma_f32_16x16x32_bf16 v[60:63], a[236:239], a[172:175], v[60:63]\n"
    "v_mov_b32 v220, s94\n"
    "v_mov_b32 v221, s95\n"
    "v_mov_b32 v222, 0\n"
    "s_mov_b64 exec, 1\n"
    "buffer_store_dwordx2 v[220:221], v222, s[60:63], s89 offen sc1\n"
    "s_mov_b64 exec, -1\n"
    "s_waitcnt vmcnt(17) lgkmcnt(0)\n"
    "v_mfma_f32_16x16x32_bf16 v[0:3], a[240:243], a[176:179], v[0:3]\n"
    "ds_read_b64_tr_b16 a[224:225], v202 offset:0\n"
    "ds_read_b64_tr_b16 a[226:227], v202 offset:512\n"
    "v_mfma_f32_16x16x32_bf16 v[8:11], a[240:243], a[180:183], v[8:11]\n"
    "ds_read_b64_tr_b16 a[228:229], v203 offset:0\n"
    "ds_read_b64_tr_b16 a[230:231], v203 offset:512\n"
    "v_mfma_f32_16x16x32_bf16 v[16:19], a[240:243], a[184:187], v[16:19]\n"
    "ds_read_b64_tr_b16 a[232:233], v202 offset:256\n"
    "v_mfma_f32_16x16x32_bf16 v[24:27], a[240:243], a[188:191], v[24:27]\n"
    "ds_read_b64_tr_b16 a[234:235], v202 offset:768\n"
    "v_mfma_f32_16x16x32_bf16 v[4:7], a[244:247], a[176:179], v[4:7]\n"
    "ds_read_b64_tr_b16 a[236:237], v203 offset:256\n"
    "v_mfma_f32_16x16x32_bf16 v[12:15], a[244:247], a[180:183], v[12:15]\n"
    "ds_read_b64_tr_b16 a[238:239], v203 offset:768\n"
    "s_add_u32 m0, s72, 12288\n"
    "s_add_u32 s73, s37, 114688\n"
    "buffer_load_dwordx4 v230, s[44:47], s73 offen offset:0 lds\n"
    "v_mfma_f32_16x16x32_bf16 v[20:23], a[244:247], a[184:187], v[20:23]\n"
    "v_mfma_f32_16x16x32_bf16 v[28:31], a[244:247], a[188:191], v[28:31]\n"
    "v_mfma_f32_16x16x32_bf16 v[32:35], a[248:251], a[176:179], v[32:35]\n"
    "s_add_u32 s73, s37, 142336\n"
    "buffer_load_dwordx4 v227, s[44:47], s73 offen offset:1024 lds\n"
    "v_mfma_f32_16x16x32_bf16 v[40:43], a[248:251], a[180:183], v[40:43]\n"
    "v_mfma_f32_16x16x32_bf16 v[48:51], a[248:251], a[184:187], v[48:51]\n"
    "v_mfma_f32_16x16x32_bf16 v[56:59], a[248:251], a[188:191], v[56:59]\n"
    "s_add_u32 s73, s37, 169984\n"
    "buffer_load_dwordx4 v230, s[44:47], s73 offen offset:2048 lds\n"
    "v_mfma_f32_16x16x32_bf16 v[36:39], a[252:255], a[176:179], v[36:39]\n"
    "v_mfma_f32_16x16x32_bf16 v[44:47], a[252:255], a[180:183], v[44:47]\n"
    "s_add_u32 s73, s37, 197632\n"
    "buffer_load_dwordx4 v227, s[44:47], s73 offen offset:3072 lds\n"
    "v_mfma_f32_16x16x32_bf16 v[52:55], a[252:255], a[184:187], v[52:55]\n"
    "v_mfma_f32_16x16x32_bf16 v[60:63], a[252:255], a[188:191], v[60:63]\n"
    "s_waitcnt vmcnt(17) lgkmcnt(0)\n"
    "v_mfma_f32_16x16x32_bf16 v[0:3], a[224:227], a[192:195], v[0:3]\n"
    "ds_read_b64_tr_b16 a[240:241], v202 offset:4096\n"
    "ds_read_b64_tr_b16 a[242:243], v202 offset:4608\n"
    "v_mfma_f32_16x16x32_bf16 v[8:11], a[224:227], a[196:199], v[8:11]\n"
    "ds_read_b64_tr_b16 a[244:245], v203 offset:4096\n"
    "ds_read_b64_tr_b16 a[246:247], v203 offset:4608\n"
    "v_mfma_f32_16x16x32_bf16 v[16:19], a[224:227], a[200:203], v[16:19]\n"
    "ds_read_b64_tr_b16 a[248:249], v202 offset:4352\n"
    "v_mfma_f32_16x16x32_bf16 v[24:27], a[224:227], a[204:207], v[24:27]\n"
    "ds_read_b64_tr_b16 a[250:251], v202 offset:4864\n"
    "v_mfma_f32_16x16x32_bf16 v[4:7], a[228:231], a[192:195], v[4:7]\n"
    "ds_read_b64_tr_b16 a[252:253], v203 offset:4352\n"
    "v_mfma_f32_16x16x32_bf16 v[12:15], a[228:231], a[196:199], v[12:15]\n"
    "ds_read_b64_tr_b16 a[254:255], v203 offset:4864\n"
    "s_add_u32 m0, s8, 0\n"
    "s_add_u32 s73, s37, 229376\n"
    "buffer_load_dwordx4 v230, s[44:47], s73 offen offset:0 lds\n"
    "v_mfma_f32_16x16x32_bf16 v[20:23], a[228:231], a[200:203], v[20:23]\n"
    "v_mfma_f32_16x16x32_bf16 v[28:31], a[228:231], a[204:207], v[28:31]\n"
    "v_mfma_f32_16x16x32_bf16 v[32:35], a[232:235], a[192:195], v[32:35]\n"
    "s_add_u32 s73, s37, 257024\n"
    "buffer_load_dwordx4 v227, s[44:47], s73 offen offset:1024 lds\n"
    "v_mfma_f32_16x16x32_bf16 v[40:43], a[232:235], a[196:199], v[40:43]\n"
    "v_mfma_f32_16x16x32_bf16 v[48:51], a[232:235], a[200:203], v[48:51]\n"
    "v_mfma_f32_16x16x32_bf16 v[56:59], a[232:235], a[204:207], v[56:59]\n"
    "s_add_u32 s73, s37, 284672\n"
    "buffer_load_dwordx4 v230, s[44:47], s73 offen offset:2048 lds\n"
    "v_mfma_f32_16x16x32_bf16 v[36:39], a[236:239], a[192:195], v[36:39]\n"
    "v_mfma_f32_16x16x32_bf16 v[44:47], a[236:239], a[196:199], v[44:47]\n"
    "s_add_u32 s73, s37, 312320\n"
    "buffer_load_dwordx4 v227, s[44:47], s73 offen offset:3072 lds\n"
    "v_mfma_f32_16x16x32_bf16 v[52:55], a[236:239], a[200:203], v[52:55]\n"
    "v_mfma_f32_16x16x32_bf16 v[60:63], a[236:239], a[204:207], v[60:63]\n"
    "s_waitcnt vmcnt(17) lgkmcnt(0)\n"
    "v_mfma_f32_16x16x32_bf16 v[0:3], a[240:243], a[208:211], v[0:3]\n"
    "ds_read_b64_tr_b16 a[224:225], v208 offset:0\n"
    "ds_read_b64_tr_b16 a[226:227], v208 offset:512\n"
    "v_mfma_f32_16x16x32_bf16 v[8:11], a[240:243], a[212:215], v[8:11]\n"
    "ds_read_b128 v[128:131], v231 offset:28672\n"
    "ds_read_b64_tr_b16 a[228:229], v209 offset:0\n"
    "v_mfma_f32_16x16x32_bf16 v[16:19], a[240:243], a[216:219], v[16:19]\n"
    "ds_read_b64_tr_b16 a[230:231], v209 offset:512\n"
    "v_mfma_f32_16x16x32_bf16 v[24:27], a[240:243], a[220:223], v[24:27]\n"
    "ds_read_b128 v[132:135], v231 offset:28928\n"
    "v_mfma_f32_16x16x32_bf16 v[4:7], a[244:247], a[208:211], v[4:7]\n"
    "ds_read_b64_tr_b16 a[232:233], v208 offset:256\n"
    "v_mfma_f32_16x16x32_bf16 v[12:15], a[244:247], a[212:215], v[12:15]\n"
    "ds_read_b64_tr_b16 a[234:235], v208 offset:768\n"
    "s_add_u32 m0, s8, 4096\n"
    "s_add_u32 s73, s37, 344064\n"
    "buffer_load_dwordx4 v230, s[44:47], s73 offen offset:0 lds\n"
    "v_mfma_f32_16x16x32_bf16 v[20:23], a[244:247], a[216:219], v[20:23]\n"
    "ds_read_b128 v[136:139], v231 offset:61440\n"
    "v_mfma_f32_16x16x32_bf16 v[28:31], a[244:247], a[220:223], v[28:31]\n"
    "ds_read_b64_tr_b16 a[236:237], v209 offset:256\n"
    "v_mfma_f32_16x16x32_bf16 v[32:35], a[248:251], a[208:211], v[32:35]\n"
    "ds_read_b64_tr_b16 a[238:239], v209 offset:768\n"
    "s_add_u32 s73, s37, 371712\n"
    "buffer_load_dwordx4 v227, s[44:47], s73 offen offset:1024 lds\n"
    "v_mfma_f32_16x16x32_bf16 v[40:43], a[248:251], a[212:215], v[40:43]\n"
    "ds_read_b128 v[140:143], v231 offset:61696\n"
    "v_mfma_f32_16x16x32_bf16 v[48:51], a[248:251], a[216:219], v[48:51]\n"
    "v_mfma_f32_16x16x32_bf16 v[56:59], a[248:251], a[220:223], v[56:59]\n"
    "s_add_u32 s73, s37, 399360\n"
    "buffer_load_dwordx4 v230, s[44:47], s73 offen offset:2048 lds\n"
    "v_mfma_f32_16x16x32_bf16 v[36:39], a[252:255], a[208:211], v[36:39]\n"
    "v_mfma_f32_16x16x32_bf16 v[44:47], a[252:255], a[212:215], v[44:47]\n"
    "s_add_u32 s73, s37, 427008\n"
    "buffer_load_dwordx4 v227, s[44:47], s73 offen offset:3072 lds\n"
    "v_mfma_f32_16x16x32_bf16 v[52:55], a[252:255], a[216:219], v[52:55]\n"
    "v_mfma_f32_16x16x32_bf16 v[60:63], a[252:255], a[220:223], v[60:63]\n"
    "s_waitcnt vmcnt(17) lgkmcnt(0)\n"
    "v_mfma_f32_16x16x32_bf16 v[0:3], a[224:227], v[128:131], v[0:3]\n"
    "ds_read_b64_tr_b16 a[240:241], v208 offset:4096\n"
    "ds_read_b64_tr_b16 a[242:243], v208 offset:4608\n"
    "v_mfma_f32_16x16x32_bf16 v[8:11], a[224:227], v[132:135], v[8:11]\n"
    "ds_read_b128 v[144:147], v231 offset:30720\n"
    "ds_read_b64_tr_b16 a[244:245], v209 offset:4096\n"
    "v_mfma_f32_16x16x32_bf16 v[16:19], a[224:227], v[136:139], v[16:19]\n"
    "ds_read_b64_tr_b16 a[246:247], v209 offset:4608\n"
    "v_mfma_f32_16x16x32_bf16 v[24:27], a[224:227], v[140:143], v[24:27]\n"
    "ds_read_b128 v[148:151], v231 offset:30976\n"
    "v_mfma_f32_16x16x32_bf16 v[4:7], a[228:231], v[128:131], v[4:7]\n"
    "ds_read_b64_tr_b16 a[248:249], v208 offset:4352\n"
    "v_mfma_f32_16x16x32_bf16 v[12:15], a[228:231], v[132:135], v[12:15]\n"
    "ds_read_b64_tr_b16 a[250:251], v208 offset:4864\n"
    "s_add_u32 m0, s72, 0\n"
    "s_add_u32 s73, s37, 458752\n"
    "buffer_load_dwordx4 v230, s[44:47], s73 offen offset:0 lds\n"
    "v_mfma_f32_16x16x32_bf16 v[20:23], a[228:231], v[136:139], v[20:23]\n"
    "ds_read_b128 v[152:155], v231 offset:63488\n"
    "v_mfma_f32_16x16x32_bf16 v[28:31], a[228:231], v[140:143], v[28:31]\n"
    "ds_read_b64_tr_b16 a[252:253], v209 offset:4352\n"
    "v_mfma_f32_16x16x32_bf16 v[32:35], a[232:235], v[128:131], v[32:35]\n"
    "ds_read_b64_tr_b16 a[254:255], v209 offset:4864\n"
    "s_add_u32 s73, s37, 486400\n"
    "buffer_load_dwordx4 v227, s[44:47], s73 offen offset:1024 lds\n"
    "v_mfma_f32_16x16x32_bf16 v[40:43], a[232:235], v[132:135], v[40:43]\n"
    "ds_read_b128 v[156:159], v231 offset:63744\n"
    "v_mfma_f32_16x16x32_bf16 v[48:51], a[232:235], v[136:139], v[48:51]\n"
    "v_mfma_f32_16x16x32_bf16 v[56:59], a[232:235], v[140:143], v[56:59]\n"
    "s_add_u32 s73, s37, 514048\n"
    "buffer_load_dwordx4 v230, s[44:47], s73 offen offset:2048 lds\n"
    "v_mfma_f32_16x16x32_bf16 v[36:39], a[236:239], v[128:131], v[36:39]\n"
    "v_mfma_f32_16x16x32_bf16 v[44:47], a[236:239], v[132:135], v[44:47]\n"
    "s_add_u32 s73, s37, 541696\n"
    "buffer_load_dwordx4 v227, s[44:47], s73 offen offset:3072 lds\n"
    "v_mfma_f32_16x16x32_bf16 v[52:55], a[236:239], v[136:139], v[52:55]\n"
    "v_mfma_f32_16x16x32_bf16 v[60:63], a[236:239], v[140:143], v[60:63]\n"
    "buffer_load_dwordx4 v[176:179], v239, s[60:63], s97 offen sc1\n"
    "buffer_load_dwordx4 v[180:183], v240, s[60:63], s96 offen sc1\n"
    "s_waitcnt lgkmcnt(0)\n"
    "v_mfma_f32_16x16x32_bf16 v[0:3], a[240:243], v[144:147], v[0:3]\n"
    "v_mfma_f32_16x16x32_bf16 v[8:11], a[240:243], v[148:151], v[8:11]\n"
    "v_mfma_f32_16x16x32_bf16 v[16:19], a[240:243], v[152:155], v[16:19]\n"
    "v_mfma_f32_16x16x32_bf16 v[24:27], a[240:243], v[156:159], v[24:27]\n"
    "v_mfma_f32_16x16x32_bf16 v[4:7], a[244:247], v[144:147], v[4:7]\n"
    "v_mfma_f32_16x16x32_bf16 v[12:15], a[244:247], v[148:151], v[12:15]\n"
    "s_add_u32 m0, s72, 4096\n"
    "s_add_u32 s73, s37, 573440\n"
    "buffer_load_dwordx4 v230, s[44:47], s73 offen offset:0 lds\n"
    "v_mfma_f32_16x16x32_bf16 v[20:23], a[244:247], v[152:155], v[20:23]\n"
    "v_mfma_f32_16x16x32_bf16 v[28:31], a[244:247], v[156:159], v[28:31]\n"
    "v_mfma_f32_16x16x32_bf16 v[32:35], a[248:251], v[144:147], v[32:35]\n"
    "s_add_u32 s73, s37, 601088\n"
    "buffer_load_dwordx4 v227, s[44:47], s73 offen offset:1024 lds\n"
    "v_mfma_f32_16x16x32_bf16 v[40:43], a[248:251], v[148:151], v[40:43]\n"
    "v_mfma_f32_16x16x32_bf16 v[48:51], a[248:251], v[152:155], v[48:51]\n"
    "v_mfma_f32_16x16x32_bf16 v[56:59], a[248:251], v[156:159], v[56:59]\n"
    "s_add_u32 s73, s37, 628736\n"
    "buffer_load_dwordx4 v230, s[44:47], s73 offen offset:2048 lds\n"
    "v_mfma_f32_16x16x32_bf16 v[36:39], a[252:255], v[144:147], v[36:39]\n"
    "v_mfma_f32_16x16x32_bf16 v[44:47], a[252:255], v[148:151], v[44:47]\n"
    "s_add_u32 s73, s37, 656384\n"
    "buffer_load_dwordx4 v227, s[44:47], s73 offen offset:3072 lds\n"
    "v_mfma_f32_16x16x32_bf16 v[52:55], a[252:255], v[152:155], v[52:55]\n"
    "v_mfma_f32_16x16x32_bf16 v[60:63], a[252:255], v[156:159], v[60:63]\n"
    "buffer_load_dwordx2 v[210:211], v247, s[60:63], s11 offen sc1\n"
    "s_add_u32 s86, s86, 8192\n"
    "s_add_u32 s7, s7, 8192\n"
    "s_mov_b64 s[52:53], s[26:27]\n"
    "s_nop 2\n"
    "v_cvt_pk_bf16_f32 v96, v0, v1\n"
    "v_cvt_pk_bf16_f32 v97, v2, v3\n"
    "v_cvt_pk_bf16_f32 v98, v4, v5\n"
    "v_cvt_pk_bf16_f32 v99, v6, v7\n"
    "v_cvt_pk_bf16_f32 v100, v8, v9\n"
    "v_cvt_pk_bf16_f32 v101, v10, v11\n"
    "v_cvt_pk_bf16_f32 v102, v12, v13\n"
    "v_cvt_pk_bf16_f32 v103, v14, v15\n"
    "v_permlane16_swap_b32 v96, v98\n"
    "v_permlane16_swap_b32 v97, v99\n"
    "v_permlane16_swap_b32 v100, v102\n"
    "v_permlane16_swap_b32 v101, v103\n"
    "buffer_store_dwordx4 v[96:99], v224, s[60:63], s86 offen offset:0 sc1\n"
    "buffer_store_dwordx4 v[100:103], v224, s[60:63], s86 offen offset:1024 sc1\n"
    "v_cvt_pk_bf16_f32 v104, v16, v17\n"
    "v_cvt_pk_bf16_f32 v105, v18, v19\n"
    "v_cvt_pk_bf16_f32 v106, v20, v21\n"
    "v_cvt_pk_bf16_f32 v107, v22, v23\n"
    "v_cvt_pk_bf16_f32 v108, v24, v25\n"
    "v_cvt_pk_bf16_f32 v109, v26, v27\n"
    "v_cvt_pk_bf16_f32 v110, v28, v29\n"
    "v_cvt_pk_bf16_f32 v111, v30, v31\n"
    "v_permlane16_swap_b32 v104, v106\n"
    "v_permlane16_swap_b32 v105, v107\n"
    "v_permlane16_swap_b32 v108, v110\n"
    "v_permlane16_swap_b32 v109, v111\n"
    "buffer_store_dwordx4 v[104:107], v224, s[60:63], s86 offen offset:2048 sc1\n"
    "buffer_store_dwordx4 v[108:111], v224, s[60:63], s86 offen offset:3072 sc1\n"
    "v_cvt_pk_bf16_f32 v112, v32, v33\n"
    "v_cvt_pk_bf16_f32 v113, v34, v35\n"
    "v_cvt_pk_bf16_f32 v114, v36, v37\n"
    "v_cvt_pk_bf16_f32 v115, v38, v39\n"
    "v_cvt_pk_bf16_f32 v116, v40, v41\n"
    "v_cvt_pk_bf16_f32 v117, v42, v43\n"
    "v_cvt_pk_bf16_f32 v118, v44, v45\n"
    "v_cvt_pk_bf16_f32 v119, v46, v47\n"
    "v_permlane16_swap_b32 v112, v114\n"
    "v_permlane16_swap_b32 v113, v115\n"
    "v_permlane16_swap_b32 v116, v118\n"
    "v_permlane16_swap_b32 v117, v119\n"
    "buffer_store_dwordx4 v[112:115], v224, s[60:63], s7 offen offset:0 sc1\n"
    "buffer_store_dwordx4 v[116:119], v224, s[60:63], s7 offen offset:1024 sc1\n"
    "v_cvt_pk_bf16_f32 v120, v48, v49\n"
    "v_cvt_pk_bf16_f32 v121, v50, v51\n"
    "v_cvt_pk_bf16_f32 v122, v52, v53\n"
    "v_cvt_pk_bf16_f32 v123, v54, v55\n"
    "v_cvt_pk_bf16_f32 v124, v56, v57\n"
    "v_cvt_pk_bf16_f32 v125, v58, v59\n"
    "v_cvt_pk_bf16_f32 v126, v60, v61\n"
    "v_cvt_pk_bf16_f32 v127, v62, v63\n"
    "v_permlane16_swap_b32 v120, v122\n"
    "v_permlane16_swap_b32 v121, v123\n"
    "v_permlane16_swap_b32 v124, v126\n"
    "v_permlane16_swap_b32 v125, v127\n"
    "buffer_store_dwordx4 v[120:123], v224, s[60:63], s7 offen offset:2048 sc1\n"
    "buffer_store_dwordx4 v[124:127], v224, s[60:63], s7 offen offset:3072 sc1\n"
    "v_mov_b32 v216, v208\n"
    "v_mov_b32 v217, v209\n"
    "v_mov_b32_e64 v218, v202\n"
    "v_mov_b32 v219, v203\n"
    "s_waitcnt vmcnt(9) lgkmcnt(0)\n"
    "ds_read_b64_tr_b16 a[0:1], v216 offset:8192\n"
    "ds_read_b64_tr_b16 a[2:3], v216 offset:8704\n"
    "ds_read_b64_tr_b16 a[4:5], v217 offset:8192\n"
    "ds_read_b64_tr_b16 a[6:7], v217 offset:8704\n"
    "ds_read_b64_tr_b16 a[8:9], v216 offset:8448\n"
    "ds_read_b64_tr_b16 a[10:11], v216 offset:8960\n"
    "ds_read_b64_tr_b16 a[12:13], v217 offset:8448\n"
    "ds_read_b64_tr_b16 a[14:15], v217 offset:8960\n"
    "ds_read_b64_tr_b16 a[16:17], v216 offset:12288\n"
    "ds_read_b64_tr_b16 a[18:19], v216 offset:12800\n"
    "ds_read_b64_tr_b16 a[20:21], v217 offset:12288\n"
    "ds_read_b64_tr_b16 a[22:23], v217 offset:12800\n"
    "ds_read_b64_tr_b16 a[24:25], v216 offset:12544\n"
    "ds_read_b64_tr_b16 a[26:27], v216 offset:13056\n"
    "ds_read_b64_tr_b16 a[28:29], v217 offset:12544\n"
    "ds_read_b64_tr_b16 a[30:31], v217 offset:13056\n"
    "ds_read_b64_tr_b16 a[32:33], v218 offset:0\n"
    "ds_read_b64_tr_b16 a[34:35], v218 offset:512\n"
    "ds_read_b64_tr_b16 a[36:37], v219 offset:0\n"
    "ds_read_b64_tr_b16 a[38:39], v219 offset:512\n"
    "ds_read_b64_tr_b16 a[40:41], v218 offset:256\n"
    "ds_read_b64_tr_b16 a[42:43], v218 offset:768\n"
    "ds_read_b64_tr_b16 a[44:45], v219 offset:256\n"
    "ds_read_b64_tr_b16 a[46:47], v219 offset:768\n"
    "ds_read_b64_tr_b16 a[48:49], v218 offset:4096\n"
    "ds_read_b64_tr_b16 a[50:51], v218 offset:4608\n"
    "ds_read_b64_tr_b16 a[52:53], v219 offset:4096\n"
    "ds_read_b64_tr_b16 a[54:55], v219 offset:4608\n"
    "ds_read_b64_tr_b16 a[56:57], v218 offset:4352\n"
    "ds_read_b64_tr_b16 a[58:59], v218 offset:4864\n"
    "ds_read_b64_tr_b16 a[60:61], v219 offset:4352\n"
    "ds_read_b64_tr_b16 a[62:63], v219 offset:4864\n"
    "ds_read_b64_tr_b16 a[64:65], v216 offset:0\n"
    "ds_read_b64_tr_b16 a[66:67], v216 offset:512\n"
    "ds_read_b64_tr_b16 a[68:69], v217 offset:0\n"
    "ds_read_b64_tr_b16 a[70:71], v217 offset:512\n"
    "ds_read_b64_tr_b16 a[72:73], v216 offset:256\n"
    "ds_read_b64_tr_b16 a[74:75], v216 offset:768\n"
    "ds_read_b64_tr_b16 a[76:77], v217 offset:256\n"
    "ds_read_b64_tr_b16 a[78:79], v217 offset:768\n"
    "ds_read_b64_tr_b16 a[80:81], v216 offset:4096\n"
    "ds_read_b64_tr_b16 a[82:83], v216 offset:4608\n"
    "ds_read_b64_tr_b16 a[84:85], v217 offset:4096\n"
    "ds_read_b64_tr_b16 a[86:87], v217 offset:4608\n"
    "ds_read_b64_tr_b16 a[88:89], v216 offset:4352\n"
    "ds_read_b64_tr_b16 a[90:91], v216 offset:4864\n"
    "ds_read_b64_tr_b16 a[92:93], v217 offset:4352\n"
    "ds_read_b64_tr_b16 a[94:95], v217 offset:4864\n"
    "s_waitcnt vmcnt(8)\n"
    "v_cmp_eq_u64 vcc, s[94:95], v[210:211]\n"
    "s_mov_b32 s91, 0xffff\n"
    "s_and_b32 vcc_lo, vcc_lo, 0xff\n"
    "s_cmp_lg_u32 vcc_lo, 0xff\n"
    "s_cbranch_scc1 x1_noearly\n"
    "s_mov_b32 s91, 0xff00\n"
    "s_add_u32 s73, s87, 0\n"
    "buffer_load_dwordx4 v[0:3], v241, s[60:63], s73 offen sc1\n"
    "s_add_u32 s73, s87, 69632\n"
    "buffer_load_dwordx4 v[4:7], v241, s[60:63], s73 offen sc1\n"
    "s_add_u32 s73, s87, 139264\n"
    "buffer_load_dwordx4 v[8:11], v241, s[60:63], s73 offen sc1\n"
    "s_add_u32 s73, s87, 208896\n"
    "buffer_load_dwordx4 v[12:15], v241, s[60:63], s73 offen sc1\n"
    "s_add_u32 s73, s87, 278528\n"
    "buffer_load_dwordx4 v[16:19], v241, s[60:63], s73 offen sc1\n"
    "s_add_u32 s73, s87, 348160\n"
    "buffer_load_dwordx4 v[20:23], v241, s[60:63], s73 offen sc1\n"
    "s_add_u32 s73, s87, 417792\n"
    "buffer_load_dwordx4 v[24:27], v241, s[60:63], s73 offen sc1\n"
    "s_add_u32 s73, s87, 487424\n"
    "buffer_load_dwordx4 v[28:31], v241, s[60:63], s73 offen sc1\n"
    "x1_noearly:\n"
    "s_barrier\n"
    "v_xor_b32 v176, s94, v176\n"
    "v_xor_b32 v178, s95, v178\n"
    "v_xor_b32 v180, s94, v180\n"
    "v_xor_b32 v182, s95, v182\n"
    "v_or3_b32 v220, v176, v178, v180\n"
    "v_or_b32 v220, v220, v182\n"
    "v_cmp_ne_u32 vcc, 0, v220\n"
    "s_cbranch_vccz gp2_ok\n"
    "s_mov_b32 s90, 32768\n"
    "gp2:\n"
    "buffer_load_dwordx4 v[176:179], v239, s[60:63], s97 offen sc1\n"
    "buffer_load_dwordx4 v[180:183], v240, s[60:63], s96 offen sc1\n"
    "gp2_chk:\n"
    "s_waitcnt vmcnt(0)\n"
    "v_xor_b32 v176, s94, v176\n"
    "v_xor_b32 v178, s95, v178\n"
    "v_or_b32 v220, v176, v178\n"
    "v_xor_b32 v180, s94, v180\n"
    "v_xor_b32 v182, s95, v182\n"
    "v_or3_b32 v220, v220, v180, v182\n"
    "v_cmp_ne_u32 vcc, 0, v220\n"
    "s_cbranch_vccz gp2_ok\n"
    "s_sub_u32 s90, s90, 1\n"
    "s_cbranch_scc1 gp2_ok\n"
    "s_sleep 2\n"
    "s_branch gp2\n"
    "gp2_ok:\n"
    "v_lshrrev_b32 v222, 1, v224\n"
    "v_add_u32 v222, s75, v222\n"
    "v_mov_b32 v178, v177\n"
    "v_mov_b32 v182, v181\n"
    "ds_write_b64 v222, v[178:179]\n"
    "ds_write_b64 v222, v[182:183] offset:512\n"
    "v_lshlrev_b32 v222, 4, v254\n"
    "v_add_u32 v222, s75, v222\n"
    "ds_read_b128 v[192:195], v222 offset:0\n"
    "ds_read_b128 v[196:199], v222 offset:128\n"
    "ds_read_b128 v[200:203], v222 offset:256\n"
    "ds_read_b128 v[204:207], v222 offset:384\n"
    "ds_read_b128 v[208:211], v222 offset:512\n"
    "ds_read_b128 v[212:215], v222 offset:640\n"
    "v_add_u32 v222, s75, v229\n"
    "ds_read_b128 v[234:237], v222 offset:768\n"
    "v_lshlrev_b32 v64, 16, v160\n"
    "v_and_b32 v65, s74, v160\n"
    "v_lshlrev_b32 v66, 16, v161\n"
    "v_and_b32 v67, s74, v161\n"
    "v_lshlrev_b32 v68, 16, v162\n"
    "v_and_b32 v69, s74, v162\n"
    "v_lshlrev_b32 v70, 16, v163\n"
    "v_and_b32_e64 v71, s74, v163\n"
    "v_pk_mov_b32 v[72:73], v[64:65], v[64:65] op_sel:[0,1]\n"
    "v_pk_mov_b32 v[74:75], v[66:67], v[66:67] op_sel:[0,1]\n"
    "v_pk_mov_b32 v[76:77], v[68:69], v[68:69] op_sel:[0,1]\n"
    "v_pk_mov_b32 v[78:79], v[70:71], v[70:71] op_sel:[0,1]\n"
    "v_pk_mov_b32 v[80:81], v[64:65], v[64:65] op_sel:[0,1]\n"
    "v_pk_mov_b32 v[82:83], v[66:67], v[66:67] op_sel:[0,1]\n"
    "v_pk_mov_b32 v[84:85], v[68:69], v[68:69] op_sel:[0,1]\n"
    "v_pk_mov_b32 v[86:87], v[70:71], v[70:71] op_sel:[0,1]\n"
    "v_pk_mov_b32 v[88:89], v[64:65], v[64:65] op_sel:[0,1]\n"
    "v_pk_mov_b32 v[90:91], v[66:67], v[66:67] op_sel:[0,1]\n"
    "v_pk_mov_b32 v[92:93], v[68:69], v[68:69] op_sel:[0,1]\n"
    "v_pk_mov_b32 v[94:95], v[70:71], v[70:71] op_sel:[0,1]\n"
    "v_lshlrev_b32 v96, 16, v164\n"
    "v_and_b32 v97, s74, v164\n"
    "v_lshlrev_b32 v98, 16, v165\n"
    "v_and_b32 v99, s74, v165\n"
    "v_lshlrev_b32 v100, 16, v166\n"
    "v_and_b32 v101, s74, v166\n"
    "v_lshlrev_b32 v102, 16, v167\n"
    "v_and_b32 v103, s74, v167\n"
    "v_pk_mov_b32 v[104:105], v[96:97], v[96:97] op_sel:[0,1]\n"
    "v_pk_mov_b32 v[106:107], v[98:99], v[98:99] op_sel:[0,1]\n"
    "v_pk_mov_b32 v[108:109], v[100:101], v[100:101] op_sel:[0,1]\n"
    "v_pk_mov_b32 v[110:111], v[102:103], v[102:103] op_sel:[0,1]\n"
    "v_pk_mov_b32 v[112:113], v[96:97], v[96:97] op_sel:[0,1]\n"
    "v_pk_mov_b32 v[114:115], v[98:99], v[98:99] op_sel:[0,1]\n"
    "v_pk_mov_b32 v[116:117], v[100:101], v[100:101] op_sel:[0,1]\n"
    "v_pk_mov_b32 v[118:119], v[102:103], v[102:103] op_sel:[0,1]\n"
    "v_pk_mov_b32 v[120:121], v[96:97], v[96:97] op_sel:[0,1]\n"
    "v_pk_mov_b32 v[122:123], v[98:99], v[98:99] op_sel:[0,1]\n"
    "v_pk_mov_b32 v[124:125], v[100:101], v[100:101] op_sel:[0,1]\n"
    "v_pk_mov_b32 v[126:127], v[102:103], v[102:103] op_sel:[0,1]\n"
    "s_waitcnt lgkmcnt(0)\n"
    "v_lshlrev_b32 v128, 16, v192\n"
    "v_and_b32 v129, s74, v192\n"
    "v_lshlrev_b32 v144, 16, v208\n"
    "v_and_b32 v145, s74, v208\n"
    "v_lshlrev_b32 v130, 16, v193\n"
    "v_and_b32 v131, s74, v193\n"
    "v_lshlrev_b32 v146, 16, v209\n"
    "v_and_b32 v147, s74, v209\n"
    "v_lshlrev_b32 v132, 16, v194\n"
    "v_and_b32 v133, s74, v194\n"
    "v_lshlrev_b32 v148, 16, v210\n"
    "v_and_b32 v149, s74, v210\n"
    "v_lshlrev_b32 v134, 16, v195\n"
    "v_and_b32 v135, s74, v195\n"
    "v_lshlrev_b32 v150, 16, v211\n"
    "v_and_b32 v151, s74, v211\n"
    "v_lshlrev_b32 v136, 16, v196\n"
    "v_and_b32 v137, s74, v196\n"
    "v_lshlrev_b32 v152, 16, v212\n"
    "v_and_b32 v153, s74, v212\n"
    "v_lshlrev_b32 v138, 16, v197\n"
    "v_and_b32 v139, s74, v197\n"
    "v_lshlrev_b32 v154, 16, v213\n"
    "v_and_b32 v155, s74, v213\n"
    "v_lshlrev_b32 v140, 16, v198\n"
    "v_and_b32 v141, s74, v198\n"
    "v_lshlrev_b32 v156, 16, v214\n"
    "v_and_b32 v157, s74, v214\n"
    "v_lshlrev_b32 v142, 16, v199\n"
    "v_and_b32 v143, s74, v199\n"
    "v_lshlrev_b32 v158, 16, v215\n"
    "v_and_b32 v159, s74, v215\n"
    "v_lshlrev_b32 v160, 16, v200\n"
    "v_and_b32 v161, s74, v200\n"
    "v_lshlrev_b32 v162, 16, v201\n"
    "v_and_b32 v163, s74, v201\n"
    "v_lshlrev_b32 v164, 16, v202\n"
    "v_and_b32 v165, s74, v202\n"
    "v_lshlrev_b32 v166, 16, v203\n"
    "v_and_b32 v167, s74, v203\n"
    "v_lshlrev_b32 v168, 16, v204\n"
    "v_and_b32 v169, s74, v204\n"
    "v_lshlrev_b32 v170, 16, v205\n"
    "v_and_b32 v171, s74, v205\n"
    "v_lshlrev_b32 v172, 16, v206\n"
    "v_and_b32 v173, s74, v206\n"
    "v_lshlrev_b32 v174, 16, v207\n"
    "v_and_b32 v175, s74, v207\n"
    "v_mov_b32 v200, v184\n"
    "v_mov_b32 v201, v185\n"
    "v_mov_b32 v202, v186\n"
    "v_mov_b32 v203, v187\n"
    "v_mov_b32 v204, v188\n"
    "v_mov_b32 v205, v189\n"
    "v_mov_b32 v206, v190\n"
    "v_mov_b32 v207, v191\n"
    "v_lshlrev_b32 v176, 16, v200\n"
    "v_and_b32 v177, s74, v200\n"
    "v_lshlrev_b32 v178, 16, v201\n"
    "v_and_b32 v179, s74, v201\n"
    "v_lshlrev_b32 v180, 16, v202\n"
    "v_and_b32 v181, s74, v202\n"
    "v_lshlrev_b32 v182, 16, v203\n"
    "v_and_b32 v183, s74, v203\n"
    "v_lshlrev_b32 v184, 16, v204\n"
    "v_and_b32 v185, s74, v204\n"
    "v_lshlrev_b32 v186, 16, v205\n"
    "v_and_b32 v187, s74, v205\n"
    "v_lshlrev_b32 v188, 16, v206\n"
    "v_and_b32 v189, s74, v206\n"
    "v_lshlrev_b32 v190, 16, v207\n"
    "v_and_b32 v191, s74, v207\n"
    "s_add_u32 s89, s89, 64\n"
    "s_add_u32 s97, s88, 65536\n"
    "s_mov_b32 s90, 32768\n"
    "s_mov_b32 s6, 0\n"
    "s_cmp_lg_u32 s91, 0xffff\n"
    "s_cbranch_scc1 x1_w8\n"
    "s_waitcnt vmcnt(0)\n"
    "x1_w8:\n"
    "s_waitcnt vmcnt(8)\n"
    "v_mov_b32 v220, s94\n"
    "v_mov_b32 v221, s95\n"
    "v_mov_b32 v222, 0\n"
    "s_mov_b64 exec, 1\n"
    "buffer_store_dwordx2 v[220:221], v222, s[60:63], s89 offen sc1\n"
    "s_mov_b64 exec, -1\n"
    "x1_loop:\n"
    "buffer_load_dwordx2 v[220:221], v247, s[60:63], s97 offen sc1\n"
    "s_waitcnt vmcnt(0)\n"
    "v_cmp_eq_u64 vcc, s[94:95], v[220:221]\n"
    "s_sub_u32 s90, s90, 1\n"
    "s_cselect_b32 s73, -1, 0\n"
    "s_or_b32 s73, vcc_lo, s73\n"
    "s_and_b32 s96, s73, s91\n"
    "s_cbranch_scc0 x1_none\n"
    "s_andn2_b32 s91, s91, s96\n"
    "s_bitcmp1_b32 s96, 0\n"
    "s_cselect_b64 exec, -1, 0\n"
    "s_add_u32 s73, s87, 0\n"
    "buffer_load_dwordx4 v[0:3], v241, s[60:63], s73 offen sc1\n"
    "s_bitcmp1_b32 s96, 1\n"
    "s_cselect_b64 exec, -1, 0\n"
    "s_add_u32 s73, s87, 69632\n"
    "buffer_load_dwordx4 v[4:7], v241, s[60:63], s73 offen sc1\n"
    "s_bitcmp1_b32 s96, 2\n"
    "s_cselect_b64 exec, -1, 0\n"
    "s_add_u32 s73, s87, 139264\n"
    "buffer_load_dwordx4 v[8:11], v241, s[60:63], s73 offen sc1\n"
    "s_bitcmp1_b32 s96, 3\n"
    "s_cselect_b64 exec, -1, 0\n"
    "s_add_u32 s73, s87, 208896\n"
    "buffer_load_dwordx4 v[12:15], v241, s[60:63], s73 offen sc1\n"
    "s_bitcmp1_b32 s96, 4\n"
    "s_cselect_b64 exec, -1, 0\n"
    "s_add_u32 s73, s87, 278528\n"
    "buffer_load_dwordx4 v[16:19], v241, s[60:63], s73 offen sc1\n"
    "s_bitcmp1_b32 s96, 5\n"
    "s_cselect_b64 exec, -1, 0\n"
    "s_add_u32 s73, s87, 348160\n"
    "buffer_load_dwordx4 v[20:23], v241, s[60:63], s73 offen sc1\n"
    "s_bitcmp1_b32 s96, 6\n"
    "s_cselect_b64 exec, -1, 0\n"
    "s_add_u32 s73, s87, 417792\n"
    "buffer_load_dwordx4 v[24:27], v241, s[60:63], s73 offen sc1\n"
    "s_bitcmp1_b32 s96, 7\n"
    "s_cselect_b64 exec, -1, 0\n"
    "s_add_u32 s73, s87, 487424\n"
    "buffer_load_dwordx4 v[28:31], v241, s[60:63], s73 offen sc1\n"
    "s_bitcmp1_b32 s96, 8\n"
    "s_cselect_b64 exec, -1, 0\n"
    "s_add_u32 s73, s87, 8192\n"
    "buffer_load_dwordx4 v[32:35], v241, s[60:63], s73 offen sc1\n"
    "s_bitcmp1_b32 s96, 9\n"
    "s_cselect_b64 exec, -1, 0\n"
    "s_add_u32 s73, s87, 77824\n"
    "buffer_load_dwordx4 v[36:39], v241, s[60:63], s73 offen sc1\n"
    "s_bitcmp1_b32 s96, 10\n"
    "s_cselect_b64 exec, -1, 0\n"
    "s_add_u32 s73, s87, 147456\n"
    "buffer_load_dwordx4 v[40:43], v241, s[60:63], s73 offen sc1\n"
    "s_bitcmp1_b32 s96, 11\n"
    "s_cselect_b64 exec, -1, 0\n"
    "s_add_u32 s73, s87, 217088\n"
    "buffer_load_dwordx4 v[44:47], v241, s[60:63], s73 offen sc1\n"
    "s_bitcmp1_b32 s96, 12\n"
    "s_cselect_b64 exec, -1, 0\n"
    "s_add_u32 s73, s87, 286720\n"
    "buffer_load_dwordx4 v[48:51], v241, s[60:63], s73 offen sc1\n"
    "s_bitcmp1_b32 s96, 13\n"
    "s_cselect_b64 exec, -1, 0\n"
    "s_add_u32 s73, s87, 356352\n"
    "buffer_load_dwordx4 v[52:55], v241, s[60:63], s73 offen sc1\n"
    "s_bitcmp1_b32 s96, 14\n"
    "s_cselect_b64 exec, -1, 0\n"
    "s_add_u32 s73, s87, 425984\n"
    "buffer_load_dwordx4 v[56:59], v241, s[60:63], s73 offen sc1\n"
    "s_bitcmp1_b32 s96, 15\n"
    "s_cselect_b64 exec, -1, 0\n"
    "s_add_u32 s73, s87, 495616\n"
    "buffer_load_dwordx4 v[60:63], v241, s[60:63], s73 offen sc1\n"
    "s_mov_b64 exec, -1\n"
    "x1_none:\n"
    "s_cmp_lg_u32 s6, 0\n"
    "s_cbranch_scc1 x1_next\n"
    "s_mov_b32 s6, 1\n"
    "s_add_u32 m0, s72, 0\n"
    "s_add_u32 s73, s37, 688128\n"
    "buffer_load_dwordx4 v230, s[44:47], s73 offen offset:0 lds\n"
    "s_add_u32 s73, s37, 715776\n"
    "buffer_load_dwordx4 v227, s[44:47], s73 offen offset:1024 lds\n"
    "s_add_u32 s73, s37, 743424\n"
    "buffer_load_dwordx4 v230, s[44:47], s73 offen offset:2048 lds\n"
    "s_add_u32 s73, s37, 771072\n"
    "buffer_load_dwordx4 v227, s[44:47], s73 offen offset:3072 lds\n"
    "s_add_u32 m0, s72, 4096\n"
    "s_add_u32 s73, s37, 802816\n"
    "buffer_load_dwordx4 v230, s[44:47], s73 offen offset:0 lds\n"
    "s_add_u32 s73, s37, 830464\n"
    "buffer_load_dwordx4 v227, s[44:47], s73 offen offset:1024 lds\n"
    "s_add_u32 s73, s37, 858112\n"
    "buffer_load_dwordx4 v230, s[44:47], s73 offen offset:2048 lds\n"
    "s_add_u32 s73, s37, 885760\n"
    "buffer_load_dwordx4 v227, s[44:47], s73 offen offset:3072 lds\n"
    "s_add_u32 m0, s72, 8192\n"
    "s_add_u32 s73, s37, 917504\n"
    "buffer_load_dwordx4 v230, s[44:47], s73 offen offset:0 lds\n"
    "s_add_u32 s73, s37, 945152\n"
    "buffer_load_dwordx4 v227, s[44:47], s73 offen offset:1024 lds\n"
    "s_add_u32 s73, s37, 972800\n"
    "buffer_load_dwordx4 v230, s[44:47], s73 offen offset:2048 lds\n"
    "s_add_u32 s73, s37, 1000448\n"
    "buffer_load_dwordx4 v227, s[44:47], s73 offen offset:3072 lds\n"
    "s_add_u32 m0, s72, 12288\n"
    "s_add_u32 s73, s37, 1032192\n"
    "buffer_load_dwordx4 v230, s[44:47], s73 offen offset:0 lds\n"
    "s_add_u32 s73, s37, 1059840\n"
    "buffer_load_dwordx4 v227, s[44:47], s73 offen offset:1024 lds\n"
    "s_add_u32 s73, s37, 1087488\n"
    "buffer_load_dwordx4 v230, s[44:47], s73 offen offset:2048 lds\n"
    "s_add_u32 s73, s37, 1115136\n"
    "buffer_load_dwordx4 v227, s[44:47], s73 offen offset:3072 lds\n"
    "s_add_u32 m0, s8, 0\n"
    "s_add_u32 s73, s37, 1146880\n"
    "buffer_load_dwordx4 v230, s[44:47], s73 offen offset:0 lds\n"
    "s_add_u32 s73, s37, 1174528\n"
    "buffer_load_dwordx4 v227, s[44:47], s73 offen offset:1024 lds\n"
    "s_add_u32 s73, s37, 1202176\n"
    "buffer_load_dwordx4 v230, s[44:47], s73 offen offset:2048 lds\n"
    "s_add_u32 s73, s37, 1229824\n"
    "buffer_load_dwordx4 v227, s[44:47], s73 offen offset:3072 lds\n"
    "s_add_u32 m0, s8, 4096\n"
    "s_add_u32 s73, s37, 1261568\n"
    "buffer_load_dwordx4 v230, s[44:47], s73 offen offset:0 lds\n"
    "s_add_u32 s73, s37, 1289216\n"
    "buffer_load_dwordx4 v227, s[44:47], s73 offen offset:1024 lds\n"
    "s_add_u32 s73, s37, 1316864\n"
    "buffer_load_dwordx4 v230, s[44:47], s73 offen offset:2048 lds\n"
    "s_add_u32 s73, s37, 1344512\n"
    "buffer_load_dwordx4 v227, s[44:47], s73 offen offset:3072 lds\n"
    "s_cmp_lg_u32 s91, 0\n"
    "s_cbranch_scc1 x1_loop\n"
    "s_waitcnt vmcnt(24)\n"
    "s_branch x1_sum\n"
    "x1_next:\n"
    "s_sleep 2\n"
    "s_cmp_lg_u32 s91, 0\n"
    "s_cbranch_scc1 x1_loop\n"
    "s_waitcnt vmcnt(0)\n"
    "x1_sum:\n"
    "v_dot2_f32_bf16 v192, v0, s98, 0\n"
    "v_dot2_f32_bf16 v193, v0, s99, 0\n"
    "v_dot2_f32_bf16 v194, v1, s98, 0\n"
    "v_dot2_f32_bf16 v195, v1, s99, 0\n"
    "v_dot2_f32_bf16 v196, v2, s98, 0\n"
    "v_dot2_f32_bf16 v197, v2, s99, 0\n"
    "v_dot2_f32_bf16 v198, v3, s98, 0\n"
    "v_dot2_f32_bf16 v199, v3, s99, 0\n"
    "v_dot2_f32_bf16 v200, v32, s98, 0\n"
    "v_dot2_f32_bf16 v201, v32, s99, 0\n"
    "v_dot2_f32_bf16 v202, v33, s98, 0\n"
    "v_dot2_f32_bf16 v203, v33, s99, 0\n"
    "v_dot2_f32_bf16 v204, v34, s98, 0\n"
    "v_dot2_f32_bf16 v205, v34, s99, 0\n"
    "v_dot2_f32_bf16 v206, v35, s98, 0\n"
    "v_dot2_f32_bf16 v207, v35, s99, 0\n"
    "v_dot2_f32_bf16 v192, v4, s98, v192\n"
    "v_dot2_f32_bf16 v193, v4, s99, v193\n"
    "v_dot2_f32_bf16 v194, v5, s98, v194\n"
    "v_dot2_f32_bf16 v195, v5, s99, v195\n"
    "v_dot2_f32_bf16 v196, v6, s98, v196\n"
    "v_dot2_f32_bf16 v197, v6, s99, v197\n"
    "v_dot2_f32_bf16 v198, v7, s98, v198\n"
    "v_dot2_f32_bf16 v199, v7, s99, v199\n"
    "v_dot2_f32_bf16 v200, v36, s98, v200\n"
    "v_dot2_f32_bf16 v201, v36, s99, v201\n"
    "v_dot2_f32_bf16 v202, v37, s98, v202\n"
    "v_dot2_f32_bf16 v203, v37, s99, v203\n"
    "v_dot2_f32_bf16 v204, v38, s98, v204\n"
    "v_dot2_f32_bf16 v205, v38, s99, v205\n"
    "v_dot2_f32_bf16 v206, v39, s98, v206\n"
    "v_dot2_f32_bf16 v207, v39, s99, v207\n"
    "v_dot2_f32_bf16 v192, v8, s98, v192\n"
    "v_dot2_f32_bf16 v193, v8, s99, v193\n"
    "v_dot2_f32_bf16 v194, v9, s98, v194\n"
    "v_dot2_f32_bf16 v195, v9, s99, v195\n"
    "v_dot2_f32_bf16 v196, v10, s98, v196\n"
    "v_dot2_f32_bf16 v197, v10, s99, v197\n"
    "v_dot2_f32_bf16 v198, v11, s98, v198\n"
    "v_dot2_f32_bf16 v199, v11, s99, v199\n"
    "v_dot2_f32_bf16 v200, v40, s98, v200\n"
    "v_dot2_f32_bf16 v201, v40, s99, v201\n"
    "v_dot2_f32_bf16 v202, v41, s98, v202\n"
    "v_dot2_f32_bf16 v203, v41, s99, v203\n"
    "v_dot2_f32_bf16 v204, v42, s98, v204\n"
    "v_dot2_f32_bf16 v205, v42, s99, v205\n"
    "v_dot2_f32_bf16 v206, v43, s98, v206\n"
    "v_dot2_f32_bf16 v207, v43, s99, v207\n"
    "v_dot2_f32_bf16 v192, v12, s98, v192\n"
    "v_dot2_f32_bf16 v193, v12, s99, v193\n"
    "v_dot2_f32_bf16 v194, v13, s98, v194\n"
    "v_dot2_f32_bf16 v195, v13, s99, v195\n"
    "v_dot2_f32_bf16 v196, v14, s98, v196\n"
    "v_dot2_f32_bf16 v197, v14, s99, v197\n"
    "v_dot2_f32_bf16 v198, v15, s98, v198\n"
    "v_dot2_f32_bf16 v199, v15, s99, v199\n"
    "v_dot2_f32_bf16 v200, v44, s98, v200\n"
    "v_dot2_f32_bf16 v201, v44, s99, v201\n"
    "v_dot2_f32_bf16 v202, v45, s98, v202\n"
    "v_dot2_f32_bf16 v203, v45, s99, v203\n"
    "v_dot2_f32_bf16 v204, v46, s98, v204\n"
    "v_dot2_f32_bf16 v205, v46, s99, v205\n"
    "v_dot2_f32_bf16 v206, v47, s98, v206\n"
    "v_dot2_f32_bf16 v207, v47, s99, v207\n"
    "v_dot2_f32_bf16 v192, v16, s98, v192\n"
    "v_dot2_f32_bf16 v193, v16, s99, v193\n"
    "v_dot2_f32_bf16 v194, v17, s98, v194\n"
    "v_dot2_f32_bf16 v195, v17, s99, v195\n"
    "v_dot2_f32_bf16 v196, v18, s98, v196\n"
    "v_dot2_f32_bf16 v197, v18, s99, v197\n"
    "v_dot2_f32_bf16 v198, v19, s98, v198\n"
    "v_dot2_f32_bf16 v199, v19, s99, v199\n"
    "v_dot2_f32_bf16 v200, v48, s98, v200\n"
    "v_dot2_f32_bf16 v201, v48, s99, v201\n"
    "v_dot2_f32_bf16 v202, v49, s98, v202\n"
    "v_dot2_f32_bf16 v203, v49, s99, v203\n"
    "v_dot2_f32_bf16 v204, v50, s98, v204\n"
    "v_dot2_f32_bf16 v205, v50, s99, v205\n"
    "v_dot2_f32_bf16 v206, v51, s98, v206\n"
    "v_dot2_f32_bf16 v207, v51, s99, v207\n"
    "v_dot2_f32_bf16 v192, v20, s98, v192\n"
    "v_dot2_f32_bf16 v193, v20, s99, v193\n"
    "v_dot2_f32_bf16 v194, v21, s98, v194\n"
    "v_dot2_f32_bf16 v195, v21, s99, v195\n"
    "v_dot2_f32_bf16 v196, v22, s98, v196\n"
    "v_dot2_f32_bf16 v197, v22, s99, v197\n"
    "v_dot2_f32_bf16 v198, v23, s98, v198\n"
    "v_dot2_f32_bf16 v199, v23, s99, v199\n"
    "v_dot2_f32_bf16 v200, v52, s98, v200\n"
    "v_dot2_f32_bf16 v201, v52, s99, v201\n"
    "v_dot2_f32_bf16 v202, v53, s98, v202\n"
    "v_dot2_f32_bf16 v203, v53, s99, v203\n"
    "v_dot2_f32_bf16 v204, v54, s98, v204\n"
    "v_dot2_f32_bf16 v205, v54, s99, v205\n"
    "v_dot2_f32_bf16 v206, v55, s98, v206\n"
    "v_dot2_f32_bf16 v207, v55, s99, v207\n"
    "v_dot2_f32_bf16 v192, v24, s98, v192\n"
    "v_dot2_f32_bf16 v193, v24, s99, v193\n"
    "v_dot2_f32_bf16 v194, v25, s98, v194\n"
    "v_dot2_f32_bf16 v195, v25, s99, v195\n"
    "v_dot2_f32_bf16 v196, v26, s98, v196\n"
    "v_dot2_f32_bf16 v197, v26, s99, v197\n"
    "v_dot2_f32_bf16 v198, v27, s98, v198\n"
    "v_dot2_f32_bf16 v199, v27, s99, v199\n"
    "v_dot2_f32_bf16 v200, v56, s98, v200\n"
    "v_dot2_f32_bf16 v201, v56, s99, v201\n"
    "v_dot2_f32_bf16 v202, v57, s98, v202\n"
    "v_dot2_f32_bf16 v203, v57, s99, v203\n"
    "v_dot2_f32_bf16 v204, v58, s98, v204\n"
    "v_dot2_f32_bf16 v205, v58, s99, v205\n"
    "v_dot2_f32_bf16 v206, v59, s98, v206\n"
    "v_dot2_f32_bf16 v207, v59, s99, v207\n"
    "v_dot2_f32_bf16 v192, v28, s98, v192\n"
    "v_dot2_f32_bf16 v193, v28, s99, v193\n"
    "v_dot2_f32_bf16 v194, v29, s98, v194\n"
    "v_dot2_f32_bf16 v195, v29, s99, v195\n"
    "v_dot2_f32_bf16 v196, v30, s98, v196\n"
    "v_dot2_f32_bf16 v197, v30, s99, v197\n"
    "v_dot2_f32_bf16 v198, v31, s98, v198\n"
    "v_dot2_f32_bf16 v199, v31, s99, v199\n"
    "v_dot2_f32_bf16 v200, v60, s98, v200\n"
    "v_dot2_f32_bf16 v201, v60, s99, v201\n"
    "v_dot2_f32_bf16 v202, v61, s98, v202\n"
    "v_dot2_f32_bf16 v203, v61, s99, v203\n"
    "v_dot2_f32_bf16 v204, v62, s98, v204\n"
    "v_dot2_f32_bf16 v205, v62, s99, v205\n"
    "v_dot2_f32_bf16 v206, v63, s98, v206\n"
    "v_dot2_f32_bf16 v207, v63, s99, v207\n"
    "s_lshl_b32 s89, s34, 16\n"
    "s_add_u32 s89, s89, s75\n"
    "s_add_u32 s97, s89, s38\n"
    "s_add_u32 s96, s97, 2621440\n"
    "s_nop 0\n"
    "v_pk_fma_f32 v[192:193], v[128:129], v[192:193], v[176:177]\n"
    "v_pk_fma_f32 v[194:195], v[130:131], v[194:195], v[178:179]\n"
    "v_pk_fma_f32 v[196:197], v[132:133], v[196:197], v[180:181]\n"
    "v_pk_fma_f32 v[198:199], v[134:135], v[198:199], v[182:183]\n"
    "v_pk_fma_f32 v[200:201], v[136:137], v[200:201], v[184:185]\n"
    "v_pk_fma_f32 v[202:203], v[138:139], v[202:203], v[186:187]\n"
    "v_pk_fma_f32 v[204:205], v[140:141], v[204:205], v[188:189]\n"
    "v_pk_fma_f32 v[206:207], v[142:143], v[206:207], v[190:191]\n"
    "v_cvt_pk_bf16_f32 v208, v192, v193\n"
    "v_cvt_pk_bf16_f32 v209, v194, v195\n"
    "v_cvt_pk_bf16_f32 v210, v196, v197\n"
    "v_cvt_pk_bf16_f32 v211, v198, v199\n"
    "v_cvt_pk_bf16_f32 v212, v200, v201\n"
    "v_cvt_pk_bf16_f32 v213, v202, v203\n"
    "v_cvt_pk_bf16_f32 v214, v204, v205\n"
    "v_cvt_pk_bf16_f32 v215, v206, v207\n"
    "s_mov_b32 s73, 0x3f803f80\n"
    "v_dot2_f32_bf16 v0, v208, s73, 0\n"
    "v_dot2_f32_bf16 v1, v208, v208, 0\n"
    "v_dot2_f32_bf16 v0, v209, s73, v0\n"
    "v_dot2_f32_bf16 v1, v209, v209, v1\n"
    "v_dot2_f32_bf16 v0, v210, s73, v0\n"
    "v_dot2_f32_bf16 v1, v210, v210, v1\n"
    "v_dot2_f32_bf16 v0, v211, s73, v0\n"
    "v_dot2_f32_bf16 v1, v211, v211, v1\n"
    "v_dot2_f32_bf16 v0, v212, s73, v0\n"
    "v_dot2_f32_bf16 v1, v212, v212, v1\n"
    "v_dot2_f32_bf16 v0, v213, s73, v0\n"
    "v_dot2_f32_bf16 v1, v213, v213, v1\n"
    "v_dot2_f32_bf16 v0, v214, s73, v0\n"
    "v_dot2_f32_bf16 v1, v214, v214, v1\n"
    "v_dot2_f32_bf16 v0, v215, s73, v0\n"
    "v_dot2_f32_bf16 v1, v215, v215, v1\n"
    "v_lshlrev_b32 v2, 5, v253\n"
    "v_add_u32 v2, s75, v2\n"
    "s_nop 0\n"
    "v_permlane32_swap_b32 v0, v1\n"
    "v_add_f32 v0, v0, v1\n"
    "v_mov_b32 v1, v0\n"
    "s_nop 1\n"
    "v_permlane16_swap_b32 v0, v1\n"
    "v_add_f32 v0, v0, v1\n"
    "s_nop 1\n"
    "v_add_f32_dpp v0, v0, v0 row_ror:8 row_mask:0xf bank_mask:0xf\n"
    "ds_write_b32 v243, v0 offset:2048\n"
    "ds_write_b32 v243, v0 offset:10240\n"
    "ds_write_b32 v243, v0 offset:18432\n"
    "ds_write_b32 v243, v0 offset:26624\n"
    "buffer_store_dwordx4 v[208:211], v226, s[60:63], s96 offen sc1\n"
    "s_add_u32 s96, s96, 4096\n"
    "buffer_store_dwordx4 v[212:215], v226, s[60:63], s96 offen sc1\n"
    "s_waitcnt lgkmcnt(0)\n"
    "s_barrier\n"
    "ds_read_b128 v[4:7], v2 offset:2048\n"
    "ds_read_b128 v[8:11], v2 offset:2064\n"
    "v_lshlrev_b32 v192, 16, v208\n"
    "v_and_b32 v193, s74, v208\n"
    "v_lshlrev_b32 v194, 16, v209\n"
    "v_and_b32 v195, s74, v209\n"
    "v_lshlrev_b32 v196, 16, v210\n"
    "v_and_b32 v197, s74, v210\n"
    "v_lshlrev_b32 v198, 16, v211\n"
    "v_and_b32 v199, s74, v211\n"
    "v_lshlrev_b32 v200, 16, v212\n"
    "v_and_b32 v201, s74, v212\n"
    "v_lshlrev_b32 v202, 16, v213\n"
    "v_and_b32 v203, s74, v213\n"
    "v_lshlrev_b32 v204, 16, v214\n"
    "v_and_b32 v205, s74, v214\n"
    "v_lshlrev_b32_e64 v206, 16, v215\n"
    "v_and_b32 v207, s74, v215\n"
    "s_waitcnt lgkmcnt(0)\n"
    "v_pk_add_f32 v[4:5], v[4:5], v[6:7]\n"
    "v_pk_add_f32 v[8:9], v[8:9], v[10:11]\n"
    "v_add_f32 v4, v4, v5\n"
    "v_add_f32 v8, v8, v9\n"
    "v_mul_f32 v4, 0xbb000000, v4\n"
    "v_mul_f32 v8, 0x3b000000, v8\n"
    "v_fma_f32 v8, -v4, v4, v8\n"
    "v_max_f32_e64 v8, 0, v8\n"
    "v_cvt_pk_bf16_f32 v8, 0, v8\n"
    "v_add_f32 v8, 0x3727c5ac, v8\n"
    "v_cvt_pk_bf16_f32 v8, 0, v8\n"
    "v_sqrt_f32 v8, v8\n"
    "v_cvt_pk_bf16_f32 v4, 0, v4\n"
    "v_cvt_pk_bf16_f32 v8, 0, v8\n"
    "v_rcp_f32 v8, v8\n"
    "s_add_u32 s96, s97, 524288\n"
    "v_mul_f32_e64 v4, v4, v8\n"
    "v_pk_fma_f32 v[192:193], v[192:193], v[8:9], v[4:5] op_sel_hi:[1,0,0]\n"
    "v_pk_fma_f32 v[194:195], v[194:195], v[8:9], v[4:5] op_sel_hi:[1,0,0]\n"
    "v_pk_fma_f32 v[196:197], v[196:197], v[8:9], v[4:5] op_sel_hi:[1,0,0]\n"
    "v_pk_fma_f32 v[198:199], v[198:199], v[8:9], v[4:5] op_sel_hi:[1,0,0]\n"
    "v_pk_fma_f32 v[200:201], v[200:201], v[8:9], v[4:5] op_sel_hi:[1,0,0]\n"
    "v_pk_fma_f32 v[202:203], v[202:203], v[8:9], v[4:5] op_sel_hi:[1,0,0]\n"
    "v_pk_fma_f32 v[204:205], v[204:205], v[8:9], v[4:5] op_sel_hi:[1,0,0]\n"
    "v_pk_fma_f32 v[206:207], v[206:207], v[8:9], v[4:5] op_sel_hi:[1,0,0]\n"
    "v_pk_fma_f32 v[192:193], v[192:193], v[144:145], v[160:161]\n"
    "v_pk_fma_f32 v[194:195], v[194:195], v[146:147], v[162:163]\n"
    "v_pk_fma_f32 v[196:197], v[196:197], v[148:149], v[164:165]\n"
    "v_pk_fma_f32 v[198:199], v[198:199], v[150:151], v[166:167]\n"
    "v_pk_fma_f32 v[200:201], v[200:201], v[152:153], v[168:169]\n"
    "v_pk_fma_f32 v[202:203], v[202:203], v[154:155], v[170:171]\n"
    "v_pk_fma_f32 v[204:205], v[204:205], v[156:157], v[172:173]\n"
    "v_pk_fma_f32 v[206:207], v[206:207], v[158:159], v[174:175]\n"
    "v_cvt_pk_bf16_f32 v176, v192, v193\n"
    "v_cvt_pk_bf16_f32 v177, v194, v195\n"
    "v_cvt_pk_bf16_f32 v178, v196, v197\n"
    "v_cvt_pk_bf16_f32 v179, v198, v199\n"
    "v_cvt_pk_bf16_f32 v180, v200, v201\n"
    "v_cvt_pk_bf16_f32 v181, v202, v203\n"
    "v_cvt_pk_bf16_f32 v182, v204, v205\n"
    "v_cvt_pk_bf16_f32 v183, v206, v207\n"
    "buffer_store_dwordx4 v[176:179], v226, s[60:63], s96 offen sc1\n"
    "s_add_u32 s96, s96, 4096\n"
    "buffer_store_dwordx4 v[180:183], v226, s[60:63], s96 offen sc1\n"
    "s_waitcnt vmcnt(4)\n"
    "ds_read_b64_tr_b16 a[96:97], v216 offset:0\n"
    "ds_read_b64_tr_b16 a[98:99], v216 offset:512\n"
    "ds_read_b64_tr_b16 a[100:101], v217 offset:0\n"
    "ds_read_b64_tr_b16 a[102:103], v217 offset:512\n"
    "ds_read_b64_tr_b16 a[104:105], v216 offset:256\n"
    "ds_read_b64_tr_b16 a[106:107], v216 offset:768\n"
    "ds_read_b64_tr_b16 a[108:109], v217 offset:256\n"
    "ds_read_b64_tr_b16 a[110:111], v217 offset:768\n"
    "ds_read_b64_tr_b16 a[112:113], v216 offset:4096\n"
    "ds_read_b64_tr_b16 a[114:115], v216 offset:4608\n"
    "ds_read_b64_tr_b16 a[116:117], v217 offset:4096\n"
    "ds_read_b64_tr_b16 a[118:119], v217 offset:4608\n"
    "ds_read_b64_tr_b16 a[120:121], v216 offset:4352\n"
    "ds_read_b64_tr_b16 a[122:123], v216 offset:4864\n"
    "ds_read_b64_tr_b16 a[124:125], v217 offset:4352\n"
    "ds_read_b64_tr_b16 a[126:127], v217 offset:4864\n"
    "ds_read_b64_tr_b16 a[128:129], v216 offset:8192\n"
    "ds_read_b64_tr_b16 a[130:131], v216 offset:8704\n"
    "ds_read_b64_tr_b16 a[132:133], v217 offset:8192\n"
    "ds_read_b64_tr_b16 a[134:135], v217 offset:8704\n"
    "ds_read_b64_tr_b16 a[136:137], v216 offset:8448\n"
    "ds_read_b64_tr_b16 a[138:139], v216 offset:8960\n"
    "ds_read_b64_tr_b16 a[140:141], v217 offset:8448\n"
    "ds_read_b64_tr_b16 a[142:143], v217 offset:8960\n"
    "ds_read_b64_tr_b16 a[144:145], v216 offset:12288\n"
    "ds_read_b64_tr_b16 a[146:147], v216 offset:12800\n"
    "ds_read_b64_tr_b16 a[148:149], v217 offset:12288\n"
    "ds_read_b64_tr_b16 a[150:151], v217 offset:12800\n"
    "ds_read_b64_tr_b16 a[152:153], v216 offset:12544\n"
    "ds_read_b64_tr_b16 a[154:155], v216 offset:13056\n"
    "ds_read_b64_tr_b16 a[156:157], v217 offset:12544\n"
    "ds_read_b64_tr_b16 a[158:159], v217 offset:13056\n"
    "ds_read_b64_tr_b16 a[160:161], v218 offset:0\n"
    "ds_read_b64_tr_b16 a[162:163], v218 offset:512\n"
    "ds_read_b64_tr_b16 a[164:165], v219 offset:0\n"
    "ds_read_b64_tr_b16 a[166:167], v219 offset:512\n"
    "ds_read_b64_tr_b16 a[168:169], v218 offset:256\n"
    "ds_read_b64_tr_b16 a[170:171], v218 offset:768\n"
    "ds_read_b64_tr_b16 a[172:173], v219 offset:256\n"
    "ds_read_b64_tr_b16 a[174:175], v219 offset:768\n"
    "ds_read_b64_tr_b16 a[176:177], v218 offset:4096\n"
    "ds_read_b64_tr_b16 a[178:179], v218 offset:4608\n"
    "ds_read_b64_tr_b16 a[180:181], v219 offset:4096\n"
    "ds_read_b64_tr_b16 a[182:183], v219 offset:4608\n"
    "ds_read_b64_tr_b16 a[184:185], v218 offset:4352\n"
    "ds_read_b64_tr_b16 a[186:187], v218 offset:4864\n"
    "ds_read_b64_tr_b16 a[188:189], v219 offset:4352\n"
    "ds_read_b64_tr_b16 a[190:191], v219 offset:4864\n"
    "s_lshl_b32 s96, s35, 3\n"
    "s_add_u32 s96, s96, s88\n"
    "s_add_u32 s96, s96, 131072\n"
    "s_add_u32 s97, s88, 131072\n"
    "s_waitcnt vmcnt(0) lgkmcnt(0)\n"
    "v_mov_b32 v220, s94\n"
    "v_mov_b32 v221, s95\n"
    "v_mov_b32 v222, 0\n"
    "s_mov_b64 exec, 1\n"
    "buffer_store_dwordx2 v[220:221], v222, s[60:63], s96 offen sc1\n"
    "s_mov_b64 exec, -1\n"
    "s_lshr_b32 s73, s56, 7\n"
    "s_sub_u32 s73, 3, s73\n"
    "s_and_b32 s73, s73, 7\n"
    "s_lshl_b32 s73, s73, 7\n"
    "s_add_u32 s73, s73, s10\n"
    "v_min_u32 v12, 208, v224\n"
    "v_lshlrev_b32 v12, 6, v12\n"
    "buffer_load_dword v13, v12, s[56:59], s73 offen\n"
    "s_mov_b64 s[56:57], s[18:19]\n"
    "s_mov_b32 s58, 458752\n"
    "s_mov_b32 s59, 0x20000\n"
    "s_add_u32 m0, s72, 0\n"
    "s_add_u32 s73, s37, 1376256\n"
    "buffer_load_dwordx4 v230, s[44:47], s73 offen offset:0 lds\n"
    "s_add_u32 s73, s37, 1403904\n"
    "buffer_load_dwordx4 v227, s[44:47], s73 offen offset:1024 lds\n"
    "s_add_u32 s73, s37, 1431552\n"
    "buffer_load_dwordx4 v230, s[44:47], s73 offen offset:2048 lds\n"
    "s_add_u32 s73, s37, 1459200\n"
    "buffer_load_dwordx4 v227, s[44:47], s73 offen offset:3072 lds\n"
    "s_add_u32 m0, s72, 4096\n"
    "s_add_u32 s73, s37, 1490944\n"
    "buffer_load_dwordx4 v230, s[44:47], s73 offen offset:0 lds\n"
    "s_add_u32 s73, s37, 1518592\n"
    "buffer_load_dwordx4 v227, s[44:47], s73 offen offset:1024 lds\n"
    "s_add_u32 s73, s37, 1546240\n"
    "buffer_load_dwordx4 v230, s[44:47], s73 offen offset:2048 lds\n"
    "s_add_u32 s73, s37, 1573888\n"
    "buffer_load_dwordx4 v227, s[44:47], s73 offen offset:3072 lds\n"
    "s_add_u32 m0, s72, 8192\n"
    "s_add_u32 s73, s37, 1605632\n"
    "buffer_load_dwordx4 v230, s[44:47], s73 offen offset:0 lds\n"
    "s_add_u32 s73, s37, 1633280\n"
    "buffer_load_dwordx4 v227, s[44:47], s73 offen offset:1024 lds\n"
    "s_add_u32 s73, s37, 1660928\n"
    "buffer_load_dwordx4 v230, s[44:47], s73 offen offset:2048 lds\n"
    "s_add_u32 s73, s37, 1688576\n"
    "buffer_load_dwordx4 v227, s[44:47], s73 offen offset:3072 lds\n"
    "s_add_u32 m0, s72, 12288\n"
    "s_add_u32 s73, s37, 1720320\n"
    "buffer_load_dwordx4 v230, s[44:47], s73 offen offset:0 lds\n"
    "s_add_u32 s73, s37, 1747968\n"
    "buffer_load_dwordx4 v227, s[44:47], s73 offen offset:1024 lds\n"
    "s_add_u32 s73, s37, 1775616\n"
    "buffer_load_dwordx4 v230, s[44:47], s73 offen offset:2048 lds\n"
    "s_add_u32 s73, s37, 1803264\n"
    "buffer_load_dwordx4 v227, s[44:47], s73 offen offset:3072 lds\n"
    "v_add_u32 v223, s75, v224\n"
    "s_mul_i32 s87, s34, 229376\n"
    "s_add_u32 s87, s87, 4718592\n"
    "s_lshl_b32 s86, s37, 6\n"
    "s_add_u32 s86, s86, s87\n"
    "s_add_u32 s64, s86, 4096\n"
    "s_mul_i32 s73, s30, 57344\n"
    "s_add_u32 s87, s87, s73\n"
    "s_cmp_eq_u32 s9, 0\n"
    "s_cselect_b32 s54, 0, 30932992\n"
    "s_lshl_b32 s96, s34, 10\n"
    "s_lshr_b32 s73, s35, 1\n"
    "s_lshl_b32 s73, s73, 8\n"
    "s_add_u32 s96, s96, s73\n"
    "s_and_b32 s73, s35, 1\n"
    "s_lshl_b32 s73, s73, 5\n"
    "s_add_u32 s96, s96, s73\n"
    "s_lshl_b32 s73, s30, 3\n"
    "s_add_u32 s96, s96, s73\n"
    "s_add_u32 s96, s96, 196608\n"
    "s_mov_b64 s[44:45], s[20:21]\n"
    "s_mov_b32 s46, 1024\n"
    "s_mov_b32 s47, 0x20000\n"
    "s_mov_b64 s[48:49], s[28:29]\n"
    "s_mov_b32 s50, 65536\n"
    "s_mov_b32 s51, 0x20000\n"
    "s_mov_b32 s90, 32768\n"
    "pl2:\n"
    "buffer_load_dwordx2 v[220:221], v242, s[60:63], s97 offen sc1\n"
    "s_waitcnt vmcnt(0)\n"
    "v_cmp_ne_u64 vcc, s[94:95], v[220:221]\n"
    "s_cbranch_vccz pl2_ok\n"
    "s_sub_u32 s90, s90, 1\n"
    "s_cbranch_scc1 pl2_ok\n"
    "s_sleep 2\n"
    "s_branch pl2\n"
    "pl2_ok:\n"
    "s_add_u32 s73, s89, 524288\n"
    "buffer_load_dwordx4 v[0:3], v224, s[60:63], s73 offen offset:0 sc1\n"
    "buffer_load_dwordx4 v[4:7], v224, s[60:63], s73 offen offset:1024 sc1\n"
    "buffer_load_dwordx4 v[8:11], v224, s[60:63], s73 offen offset:2048 sc1\n"
    "buffer_load_dwordx4 v[12:15], v224, s[60:63], s73 offen offset:3072 sc1\n"
    "s_add_u32 s73, s89, 528384\n"
    "buffer_load_dwordx4 v[16:19], v224, s[60:63], s73 offen offset:0 sc1\n"
    "buffer_load_dwordx4 v[20:23], v224, s[60:63], s73 offen offset:1024 sc1\n"
    "buffer_load_dwordx4 v[24:27], v224, s[60:63], s73 offen offset:2048 sc1\n"
    "buffer_load_dwordx4 v[28:31], v224, s[60:63], s73 offen offset:3072 sc1\n"
    "s_add_u32 s73, s89, 557056\n"
    "buffer_load_dwordx4 v[32:35], v224, s[60:63], s73 offen offset:0 sc1\n"
    "buffer_load_dwordx4 v[36:39], v224, s[60:63], s73 offen offset:1024 sc1\n"
    "buffer_load_dwordx4 v[40:43], v224, s[60:63], s73 offen offset:2048 sc1\n"
    "buffer_load_dwordx4 v[44:47], v224, s[60:63], s73 offen offset:3072 sc1\n"
    "s_add_u32 s73, s89, 561152\n"
    "buffer_load_dwordx4 v[48:51], v224, s[60:63], s73 offen offset:0 sc1\n"
    "buffer_load_dwordx4 v[52:55], v224, s[60:63], s73 offen offset:1024 sc1\n"
    "buffer_load_dwordx4 v[56:59], v224, s[60:63], s73 offen offset:2048 sc1\n"
    "buffer_load_dwordx4 v[60:63], v224, s[60:63], s73 offen offset:3072 sc1\n"
    "ds_read_b64_tr_b16 a[192:193], v216 offset:0\n"
    "ds_read_b64_tr_b16 a[194:195], v216 offset:512\n"
    "ds_read_b64_tr_b16 a[196:197], v217 offset:0\n"
    "ds_read_b64_tr_b16 a[198:199], v217 offset:512\n"
    "ds_read_b64_tr_b16 a[200:201], v216 offset:256\n"
    "ds_read_b64_tr_b16 a[202:203], v216 offset:768\n"
    "ds_read_b64_tr_b16 a[204:205], v217 offset:256\n"
    "ds_read_b64_tr_b16 a[206:207], v217 offset:768\n"
    "ds_read_b64_tr_b16 a[208:209], v216 offset:4096\n"
    "ds_read_b64_tr_b16 a[210:211], v216 offset:4608\n"
    "ds_read_b64_tr_b16 a[212:213], v217 offset:4096\n"
    "ds_read_b64_tr_b16 a[214:215], v217 offset:4608\n"
    "ds_read_b64_tr_b16 a[216:217], v216 offset:4352\n"
    "ds_read_b64_tr_b16 a[218:219], v216 offset:4864\n"
    "ds_read_b64_tr_b16 a[220:221], v217 offset:4352\n"
    "ds_read_b64_tr_b16 a[222:223], v217 offset:4864\n"
    "ds_read_b64_tr_b16 a[224:225], v216 offset:8192\n"
    "ds_read_b64_tr_b16 a[226:227], v216 offset:8704\n"
    "ds_read_b64_tr_b16 a[228:229], v217 offset:8192\n"
    "ds_read_b64_tr_b16 a[230:231], v217 offset:8704\n"
    "ds_read_b64_tr_b16 a[232:233], v216 offset:8448\n"
    "ds_read_b64_tr_b16 a[234:235], v216 offset:8960\n"
    "ds_read_b64_tr_b16 a[236:237], v217 offset:8448\n"
    "ds_read_b64_tr_b16 a[238:239], v217 offset:8960\n"
    "ds_read_b64_tr_b16 a[240:241], v216 offset:12288\n"
    "ds_read_b64_tr_b16 a[242:243], v216 offset:12800\n"
    "ds_read_b64_tr_b16 a[244:245], v217 offset:12288\n"
    "ds_read_b64_tr_b16 a[246:247], v217 offset:12800\n"
    "ds_read_b64_tr_b16 a[248:249], v216 offset:12544\n"
    "ds_read_b64_tr_b16 a[250:251], v216 offset:13056\n"
    "ds_read_b64_tr_b16 a[252:253], v217 offset:12544\n"
    "ds_read_b64_tr_b16 a[254:255], v217 offset:13056\n"
    "s_add_u32 m0, s8, 0\n"
    "s_mov_b32 s73, 0\n"
    "buffer_load_dwordx4 v228, s[56:59], s73 offen offset:0 lds\n"
    "s_mov_b32 s73, 3072\n"
    "buffer_load_dwordx4 v233, s[56:59], s73 offen offset:1024 lds\n"
    "s_mov_b32 s73, 6144\n"
    "buffer_load_dwordx4 v228, s[56:59], s73 offen offset:2048 lds\n"
    "s_mov_b32 s73, 9216\n"
    "buffer_load_dwordx4 v233, s[56:59], s73 offen offset:3072 lds\n"
    "s_add_u32 m0, s8, 4096\n"
    "s_mov_b32 s73, 32768\n"
    "buffer_load_dwordx4 v228, s[56:59], s73 offen offset:0 lds\n"
    "s_mov_b32 s73, 35840\n"
    "buffer_load_dwordx4 v233, s[56:59], s73 offen offset:1024 lds\n"
    "s_mov_b32 s73, 38912\n"
    "buffer_load_dwordx4 v228, s[56:59], s73 offen offset:2048 lds\n"
    "s_mov_b32 s73, 41984\n"
    "buffer_load_dwordx4 v233, s[56:59], s73 offen offset:3072 lds\n"
    "s_waitcnt vmcnt(23)\n"
    "ds_write_b128 v223, v[0:3] offset:0\n"
    "s_waitcnt vmcnt(22)\n"
    "ds_write_b128 v223, v[4:7] offset:1024\n"
    "s_waitcnt vmcnt(21)\n"
    "ds_write_b128 v223, v[8:11] offset:2048\n"
    "s_waitcnt vmcnt(20)\n"
    "ds_write_b128 v223, v[12:15] offset:3072\n"
    "s_waitcnt vmcnt(19)\n"
    "ds_write_b128 v223, v[16:19] offset:4096\n"
    "s_waitcnt vmcnt(18)\n"
    "ds_write_b128 v223, v[20:23] offset:5120\n"
    "s_waitcnt vmcnt(17)\n"
    "ds_write_b128 v223, v[24:27] offset:6144\n"
    "s_waitcnt vmcnt(16)\n"
    "ds_write_b128 v223, v[28:31] offset:7168\n"
    "s_waitcnt lgkmcnt(0)\n"
    "s_barrier\n"
    "ds_read_b128 v[128:131], v231 offset:0\n"
    "ds_read_b128 v[132:135], v231 offset:256\n"
    "ds_read_b128 v[136:139], v231 offset:2048\n"
    "s_waitcnt lgkmcnt(2)\n"
    "v_mfma_f32_16x16x32_bf16 v[64:67], a[0:3], v[128:131], v[64:67]\n"
    "ds_read_b128 v[140:143], v231 offset:2304\n"
    "s_waitcnt vmcnt(15)\n"
    "v_mfma_f32_16x16x32_bf16 v[68:71], a[4:7], v[128:131], v[68:71]\n"
    "ds_write_b128 v223, v[32:35] offset:32768\n"
    "v_mfma_f32_16x16x32_bf16 v[96:99], a[8:11], v[128:131], v[96:99]\n"
    "s_lshl_b32 s73, s34, 16\n"
    "s_add_u32 s73, s73, 2621440\n"
    "buffer_load_dwordx4 v[208:211], v244, s[60:63], s73 offen sc1\n"
    "v_mfma_f32_16x16x32_bf16 v[100:103], a[12:15], v[128:131], v[100:103]\n"
    "s_waitcnt lgkmcnt(3)\n"
    "v_mfma_f32_16x16x32_bf16 v[72:75], a[0:3], v[132:135], v[72:75]\n"
    "ds_read_b128 v[128:131], v231 offset:4096\n"
    "s_waitcnt vmcnt(15)\n"
    "v_mfma_f32_16x16x32_bf16 v[76:79], a[4:7], v[132:135], v[76:79]\n"
    "ds_write_b128 v223, v[36:39] offset:33792\n"
    "v_mfma_f32_16x16x32_bf16 v[104:107], a[8:11], v[132:135], v[104:107]\n"
    "s_add_u32 s73, s73, 32768\n"
    "buffer_load_dwordx4 v[212:215], v244, s[60:63], s73 offen sc1\n"
    "v_mfma_f32_16x16x32_bf16 v[108:111], a[12:15], v[132:135], v[108:111]\n"
    "s_waitcnt lgkmcnt(4)\n"
    "v_mfma_f32_16x16x32_bf16 v[64:67], a[16:19], v[136:139], v[64:67]\n"
    "ds_read_b128 v[132:135], v231 offset:4352\n"
    "s_waitcnt vmcnt(15)\n"
    "v_mfma_f32_16x16x32_bf16 v[68:71], a[20:23], v[136:139], v[68:71]\n"
    "ds_write_b128 v223, v[40:43] offset:34816\n"
    "v_mfma_f32_16x16x32_bf16 v[96:99], a[24:27], v[136:139], v[96:99]\n"
    "buffer_load_dwordx4 v[248:251], v245, s[44:47], 0 offen\n"
    "v_mfma_f32_16x16x32_bf16 v[100:103], a[28:31], v[136:139], v[100:103]\n"
    "s_waitcnt lgkmcnt(5)\n"
    "v_mfma_f32_16x16x32_bf16 v[72:75], a[16:19], v[140:143], v[72:75]\n"
    "ds_read_b128 v[136:139], v231 offset:6144\n"
    "s_waitcnt vmcnt(15)\n"
    "v_mfma_f32_16x16x32_bf16 v[76:79], a[20:23], v[140:143], v[76:79]\n"
    "ds_write_b128 v223, v[44:47] offset:35840\n"
    "v_mfma_f32_16x16x32_bf16 v[104:107], a[24:27], v[140:143], v[104:107]\n"
    "s_add_u32 m0, s72, 0\n"
    "s_mov_b32 s73, 65536\n"
    "buffer_load_dwordx4 v228, s[56:59], s73 offen offset:0 lds\n"
    "v_mfma_f32_16x16x32_bf16 v[108:111], a[28:31], v[140:143], v[108:111]\n"
    "s_waitcnt lgkmcnt(5)\n"
    "v_mfma_f32_16x16x32_bf16 v[64:67], a[32:35], v[128:131], v[64:67]\n"
    "ds_read_b128 v[140:143], v231 offset:6400\n"
    "s_waitcnt vmcnt(15)\n"
    "v_mfma_f32_16x16x32_bf16 v[68:71], a[36:39], v[128:131], v[68:71]\n"
    "ds_write_b128 v223, v[48:51] offset:36864\n"
    "s_mov_b32 s73, 68608\n"
    "buffer_load_dwordx4 v233, s[56:59], s73 offen offset:1024 lds\n"
    "v_mfma_f32_16x16x32_bf16 v[96:99], a[40:43], v[128:131], v[96:99]\n"
    "s_mov_b32 s73, 71680\n"
    "buffer_load_dwordx4 v228, s[56:59], s73 offen offset:2048 lds\n"
    "v_mfma_f32_16x16x32_bf16 v[100:103], a[44:47], v[128:131], v[100:103]\n"
    "s_waitcnt lgkmcnt(5)\n"
    "v_mfma_f32_16x16x32_bf16 v[72:75], a[32:35], v[132:135], v[72:75]\n"
    "ds_read_b128 v[128:131], v231 offset:8192\n"
    "s_waitcnt vmcnt(16)\n"
    "v_mfma_f32_16x16x32_bf16 v[76:79], a[36:39], v[132:135], v[76:79]\n"
    "ds_write_b128 v223, v[52:55] offset:37888\n"
    "s_mov_b32 s73, 74752\n"
    "buffer_load_dwordx4 v233, s[56:59], s73 offen offset:3072 lds\n"
    "v_mfma_f32_16x16x32_bf16 v[104:107], a[40:43], v[132:135], v[104:107]\n"
    "s_add_u32 m0, s72, 4096\n"
    "s_mov_b32 s73, 98304\n"
    "buffer_load_dwordx4 v228, s[56:59], s73 offen offset:0 lds\n"
    "v_mfma_f32_16x16x32_bf16 v[108:111], a[44:47], v[132:135], v[108:111]\n"
    "s_waitcnt lgkmcnt(5)\n"
    "v_mfma_f32_16x16x32_bf16 v[64:67], a[48:51], v[136:139], v[64:67]\n"
    "ds_read_b128 v[132:135], v231 offset:8448\n"
    "s_waitcnt vmcnt(17)\n"
    "v_mfma_f32_16x16x32_bf16 v[68:71], a[52:55], v[136:139], v[68:71]\n"
    "ds_write_b128 v223, v[56:59] offset:38912\n"
    "s_mov_b32 s73, 101376\n"
    "buffer_load_dwordx4 v233, s[56:59], s73 offen offset:1024 lds\n"
    "v_mfma_f32_16x16x32_bf16 v[96:99], a[56:59], v[136:139], v[96:99]\n"
    "s_mov_b32 s73, 104448\n"
    "buffer_load_dwordx4 v228, s[56:59], s73 offen offset:2048 lds\n"
    "v_mfma_f32_16x16x32_bf16 v[100:103], a[60:63], v[136:139], v[100:103]\n"
    "s_waitcnt lgkmcnt(5)\n"
    "v_mfma_f32_16x16x32_bf16 v[72:75], a[48:51], v[140:143], v[72:75]\n"
    "ds_read_b128 v[136:139], v231 offset:10240\n"
    "s_waitcnt vmcnt(18)\n"
    "v_mfma_f32_16x16x32_bf16 v[76:79], a[52:55], v[140:143], v[76:79]\n"
    "ds_write_b128 v223, v[60:63] offset:39936\n"
    "s_mov_b32 s73, 107520\n"
    "buffer_load_dwordx4 v233, s[56:59], s73 offen offset:3072 lds\n"
    "v_mfma_f32_16x16x32_bf16 v[104:107], a[56:59], v[140:143], v[104:107]\n"
    "s_add_u32 m0, s72, 8192\n"
    "s_mov_b32 s73, 131072\n"
    "buffer_load_dwordx4 v228, s[56:59], s73 offen offset:0 lds\n"
    "v_mfma_f32_16x16x32_bf16 v[108:111], a[60:63], v[140:143], v[108:111]\n"
    "s_waitcnt lgkmcnt(5)\n"
    "v_mfma_f32_16x16x32_bf16 v[64:67], a[64:67], v[128:131], v[64:67]\n"
    "ds_read_b128 v[140:143], v231 offset:10496\n"
    "s_mov_b32 s73, 134144\n"
    "buffer_load_dwordx4 v233, s[56:59], s73 offen offset:1024 lds\n"
    "s_waitcnt vmcnt(17)\n"
    "v_mfma_f32_16x16x32_bf16 v[68:71], a[68:71], v[128:131], v[68:71]\n"
    "ds_read_b64_tr_b16 v[144:145], v218 offset:0\n"
    "ds_read_b64_tr_b16 v[146:147], v218 offset:512\n"
    "v_mfma_f32_16x16x32_bf16 v[96:99], a[72:75], v[128:131], v[96:99]\n"
    "s_mov_b32 s73, 137216\n"
    "buffer_load_dwordx4 v228, s[56:59], s73 offen offset:2048 lds\n"
    "ds_read_b64_tr_b16 v[148:149], v219 offset:0\n"
    "v_mfma_f32_16x16x32_bf16 v[100:103], a[76:79], v[128:131], v[100:103]\n"
    "ds_read_b64_tr_b16 v[150:151], v219 offset:512\n"
    "s_waitcnt lgkmcnt(8)\n"
    "v_mfma_f32_16x16x32_bf16 v[72:75], a[64:67], v[132:135], v[72:75]\n"
    "ds_read_b128 v[128:131], v231 offset:12288\n"
    "s_mov_b32 s73, 140288\n"
    "buffer_load_dwordx4 v233, s[56:59], s73 offen offset:3072 lds\n"
    "v_mfma_f32_16x16x32_bf16 v[76:79], a[68:71], v[132:135], v[76:79]\n"
    "ds_read_b64_tr_b16 v[152:153], v218 offset:256\n"
    "ds_read_b64_tr_b16 v[154:155], v218 offset:768\n"
    "v_mfma_f32_16x16x32_bf16 v[104:107], a[72:75], v[132:135], v[104:107]\n"
    "s_add_u32 m0, s72, 12288\n"
    "s_mov_b32 s73, 163840\n"
    "buffer_load_dwordx4 v228, s[56:59], s73 offen offset:0 lds\n"
    "ds_read_b64_tr_b16 v[156:157], v219 offset:256\n"
    "v_mfma_f32_16x16x32_bf16 v[108:111], a[76:79], v[132:135], v[108:111]\n"
    "ds_read_b64_tr_b16 v[158:159], v219 offset:768\n"
    "s_waitcnt lgkmcnt(11)\n"
    "v_mfma_f32_16x16x32_bf16 v[64:67], a[80:83], v[136:139], v[64:67]\n"
    "ds_read_b128 v[132:135], v231 offset:12544\n"
    "s_mov_b32 s73, 166912\n"
    "buffer_load_dwordx4 v233, s[56:59], s73 offen offset:1024 lds\n"
    "s_waitcnt vmcnt(17)\n"
    "v_mfma_f32_16x16x32_bf16 v[68:71], a[84:87], v[136:139], v[68:71]\n"
    "ds_read_b64_tr_b16 v[160:161], v218 offset:4096\n"
    "ds_read_b64_tr_b16 v[162:163], v218 offset:4608\n"
    "v_mfma_f32_16x16x32_bf16 v[96:99], a[88:91], v[136:139], v[96:99]\n"
    "s_mov_b32 s73, 169984\n"
    "buffer_load_dwordx4 v228, s[56:59], s73 offen offset:2048 lds\n"
    "ds_read_b64_tr_b16 v[164:165], v219 offset:4096\n"
    "v_mfma_f32_16x16x32_bf16 v[100:103], a[92:95], v[136:139], v[100:103]\n"
    "ds_read_b64_tr_b16 v[166:167], v219 offset:4608\n"
    "s_waitcnt lgkmcnt(14)\n"
    "v_mfma_f32_16x16x32_bf16 v[72:75], a[80:83], v[140:143], v[72:75]\n"
    "ds_read_b128 v[136:139], v231 offset:14336\n"
    "s_mov_b32 s73, 173056\n"
    "buffer_load_dwordx4 v233, s[56:59], s73 offen offset:3072 lds\n"
    "v_mfma_f32_16x16x32_bf16 v[76:79], a[84:87], v[140:143], v[76:79]\n"
    "ds_read_b64_tr_b16 v[168:169], v218 offset:4352\n"
    "ds_read_b64_tr_b16 v[170:171], v218 offset:4864\n"
    "v_mfma_f32_16x16x32_bf16 v[104:107], a[88:91], v[140:143], v[104:107]\n"
    "ds_read_b64_tr_b16 v[172:173], v219 offset:4352\n"
    "v_mfma_f32_16x16x32_bf16 v[108:111], a[92:95], v[140:143], v[108:111]\n"
    "ds_read_b64_tr_b16 v[174:175], v219 offset:4864\n"
    "s_waitcnt lgkmcnt(14)\n"
    "v_mfma_f32_16x16x32_bf16 v[64:67], a[96:99], v[128:131], v[64:67]\n"
    "ds_read_b128 v[140:143], v231 offset:14592\n"
    "v_mfma_f32_16x16x32_bf16 v[68:71], a[100:103], v[128:131], v[68:71]\n"
    "v_mfma_f32_16x16x32_bf16 v[96:99], a[104:107], v[128:131], v[96:99]\n"
    "v_mfma_f32_16x16x32_bf16 v[100:103], a[108:111], v[128:131], v[100:103]\n"
    "s_waitcnt lgkmcnt(10)\n"
    "v_mfma_f32_16x16x32_bf16 v[72:75], a[96:99], v[132:135], v[72:75]\n"
    "ds_read_b128 v[128:131], v231 offset:16384\n"
    "v_mfma_f32_16x16x32_bf16 v[76:79], a[100:103], v[132:135], v[76:79]\n"
    "v_mfma_f32_16x16x32_bf16 v[104:107], a[104:107], v[132:135], v[104:107]\n"
    "v_mfma_f32_16x16x32_bf16 v[108:111], a[108:111], v[132:135], v[108:111]\n"
    "s_waitcnt lgkmcnt(6)\n"
    "v_mfma_f32_16x16x32_bf16 v[64:67], a[112:115], v[136:139], v[64:67]\n"
    "ds_read_b128 v[132:135], v231 offset:16640\n"
    "s_waitcnt lgkmcnt(3)\n"
    "v_mfma_f32_16x16x32_bf16 v[68:71], a[116:119], v[136:139], v[68:71]\n"
    "s_barrier\n"
    "v_mfma_f32_16x16x32_bf16 v[96:99], a[120:123], v[136:139], v[96:99]\n"
    "s_add_u32 m0, s8, 0\n"
    "s_mov_b32 s73, 196608\n"
    "buffer_load_dwordx4 v228, s[56:59], s73 offen offset:0 lds\n"
    "v_mfma_f32_16x16x32_bf16 v[100:103], a[124:127], v[136:139], v[100:103]\n"
    "s_waitcnt lgkmcnt(2)\n"
    "v_mfma_f32_16x16x32_bf16 v[72:75], a[112:115], v[140:143], v[72:75]\n"
    "ds_read_b128 v[136:139], v231 offset:18432\n"
    "v_mfma_f32_16x16x32_bf16 v[76:79], a[116:119], v[140:143], v[76:79]\n"
    "s_mov_b32 s73, 199680\n"
    "buffer_load_dwordx4 v233, s[56:59], s73 offen offset:1024 lds\n"
    "v_mfma_f32_16x16x32_bf16 v[104:107], a[120:123], v[140:143], v[104:107]\n"
    "s_mov_b32 s73, 202752\n"
    "buffer_load_dwordx4 v228, s[56:59], s73 offen offset:2048 lds\n"
    "v_mfma_f32_16x16x32_bf16 v[108:111], a[124:127], v[140:143], v[108:111]\n"
    "s_waitcnt lgkmcnt(2)\n"
    "v_mfma_f32_16x16x32_bf16 v[64:67], a[128:131], v[128:131], v[64:67]\n"
    "ds_read_b128 v[140:143], v231 offset:18688\n"
    "v_mfma_f32_16x16x32_bf16 v[68:71], a[132:135], v[128:131], v[68:71]\n"
    "s_mov_b32 s73, 205824\n"
    "buffer_load_dwordx4 v233, s[56:59], s73 offen offset:3072 lds\n"
    "v_mfma_f32_16x16x32_bf16 v[96:99], a[136:139], v[128:131], v[96:99]\n"
    "s_add_u32 m0, s8, 4096\n"
    "s_mov_b32 s73, 229376\n"
    "buffer_load_dwordx4 v228, s[56:59], s73 offen offset:0 lds\n"
    "v_mfma_f32_16x16x32_bf16 v[100:103], a[140:143], v[128:131], v[100:103]\n"
    "s_waitcnt lgkmcnt(2)\n"
    "v_mfma_f32_16x16x32_bf16 v[72:75], a[128:131], v[132:135], v[72:75]\n"
    "ds_read_b128 v[128:131], v231 offset:20480\n"
    "v_mfma_f32_16x16x32_bf16 v[76:79], a[132:135], v[132:135], v[76:79]\n"
    "s_mov_b32 s73, 232448\n"
    "buffer_load_dwordx4 v233, s[56:59], s73 offen offset:1024 lds\n"
    "v_mfma_f32_16x16x32_bf16 v[104:107], a[136:139], v[132:135], v[104:107]\n"
    "s_mov_b32 s73, 235520\n"
    "buffer_load_dwordx4 v228, s[56:59], s73 offen offset:2048 lds\n"
    "v_mfma_f32_16x16x32_bf16 v[108:111], a[140:143], v[132:135], v[108:111]\n"
    "s_waitcnt lgkmcnt(2)\n"
    "v_mfma_f32_16x16x32_bf16 v[64:67], a[144:147], v[136:139], v[64:67]\n"
    "ds_read_b128 v[132:135], v231 offset:20736\n"
    "v_mfma_f32_16x16x32_bf16 v[68:71], a[148:151], v[136:139], v[68:71]\n"
    "s_mov_b32 s73, 238592\n"
    "buffer_load_dwordx4 v233, s[56:59], s73 offen offset:3072 lds\n"
    "v_mfma_f32_16x16x32_bf16 v[96:99], a[152:155], v[136:139], v[96:99]\n"
    "s_waitcnt vmcnt(20)\n"
    "ds_read_b64_tr_b16 v[176:177], v216 offset:0\n"
    "v_mfma_f32_16x16x32_bf16 v[100:103], a[156:159], v[136:139], v[100:103]\n"
    "ds_read_b64_tr_b16 v[178:179], v216 offset:512\n"
    "s_waitcnt lgkmcnt(4)\n"
    "v_mfma_f32_16x16x32_bf16 v[72:75], a[144:147], v[140:143], v[72:75]\n"
    "ds_read_b128 v[136:139], v231 offset:22528\n"
    "ds_read_b64_tr_b16 v[180:181], v217 offset:0\n"
    "v_mfma_f32_16x16x32_bf16 v[76:79], a[148:151], v[140:143], v[76:79]\n"
    "ds_read_b64_tr_b16 v[182:183], v217 offset:512\n"
    "v_mfma_f32_16x16x32_bf16 v[104:107], a[152:155], v[140:143], v[104:107]\n"
    "ds_read_b64_tr_b16 v[184:185], v216 offset:256\n"
    "v_mfma_f32_16x16x32_bf16 v[108:111], a[156:159], v[140:143], v[108:111]\n"
    "ds_read_b64_tr_b16 v[186:187], v216 offset:768\n"
    "s_waitcnt lgkmcnt(8)\n"
    "v_mfma_f32_16x16x32_bf16 v[64:67], a[160:163], v[128:131], v[64:67]\n"
    "ds_read_b128 v[140:143], v231 offset:22784\n"
    "ds_read_b64_tr_b16 v[188:189], v217 offset:256\n"
    "v_mfma_f32_16x16x32_bf16 v[68:71], a[164:167], v[128:131], v[68:71]\n"
    "ds_read_b64_tr_b16 v[190:191], v217 offset:768\n"
    "v_mfma_f32_16x16x32_bf16 v[96:99], a[168:171], v[128:131], v[96:99]\n"
    "s_waitcnt vmcnt(16)\n"
    "ds_read_b64_tr_b16 v[192:193], v216 offset:4096\n"
    "v_mfma_f32_16x16x32_bf16 v[100:103], a[172:175], v[128:131], v[100:103]\n"
    "ds_read_b64_tr_b16 v[194:195], v216 offset:4608\n"
    "s_waitcnt lgkmcnt(12)\n"
    "v_mfma_f32_16x16x32_bf16 v[72:75], a[160:163], v[132:135], v[72:75]\n"
    "ds_read_b128 v[128:131], v231 offset:24576\n"
    "ds_read_b64_tr_b16 v[196:197], v217 offset:4096\n"
    "v_mfma_f32_16x16x32_bf16 v[76:79], a[164:167], v[132:135], v[76:79]\n"
    "ds_read_b64_tr_b16 v[198:199], v217 offset:4608\n"
    "v_mfma_f32_16x16x32_bf16 v[104:107], a[168:171], v[132:135], v[104:107]\n"
    "ds_read_b64_tr_b16 v[200:201], v216 offset:4352\n"
    "v_mfma_f32_16x16x32_bf16 v[108:111], a[172:175], v[132:135], v[108:111]\n"
    "ds_read_b64_tr_b16 v[202:203], v216 offset:4864\n"
    "s_waitcnt lgkmcnt(14)\n"
    "v_mfma_f32_16x16x32_bf16 v[64:67], a[176:179], v[136:139], v[64:67]\n"
    "ds_read_b128 v[132:135], v231 offset:24832\n"
    "ds_read_b64_tr_b16 v[204:205], v217 offset:4352\n"
    "v_mfma_f32_16x16x32_bf16 v[68:71], a[180:183], v[136:139], v[68:71]\n"
    "ds_read_b64_tr_b16 v[206:207], v217 offset:4864\n"
    "v_mfma_f32_16x16x32_bf16 v[96:99], a[184:187], v[136:139], v[96:99]\n"
    "v_mfma_f32_16x16x32_bf16 v[100:103], a[188:191], v[136:139], v[100:103]\n"
    "s_waitcnt lgkmcnt(12)\n"
    "v_mfma_f32_16x16x32_bf16 v[72:75], a[176:179], v[140:143], v[72:75]\n"
    "ds_read_b128 v[136:139], v231 offset:26624\n"
    "v_mfma_f32_16x16x32_bf16 v[76:79], a[180:183], v[140:143], v[76:79]\n"
    "v_mfma_f32_16x16x32_bf16 v[104:107], a[184:187], v[140:143], v[104:107]\n"
    "v_mfma_f32_16x16x32_bf16 v[108:111], a[188:191], v[140:143], v[108:111]\n"
    "s_waitcnt lgkmcnt(8)\n"
    "v_mfma_f32_16x16x32_bf16 v[64:67], a[192:195], v[128:131], v[64:67]\n"
    "ds_read_b128 v[140:143], v231 offset:26880\n"
    "v_mfma_f32_16x16x32_bf16 v[68:71], a[196:199], v[128:131], v[68:71]\n"
    "s_waitcnt lgkmcnt(2)\n"
    "v_mfma_f32_16x16x32_bf16 v[96:99], a[200:203], v[128:131], v[96:99]\n"
    "s_add_u32 m0, s72, 0\n"
    "s_mov_b32 s73, 262144\n"
    "buffer_load_dwordx4 v228, s[56:59], s73 offen offset:0 lds\n"
    "v_mfma_f32_16x16x32_bf16 v[100:103], a[204:207], v[128:131], v[100:103]\n"
    "v_mfma_f32_16x16x32_bf16 v[72:75], a[192:195], v[132:135], v[72:75]\n"
    "ds_read_b128 v[128:131], v231 offset:28672\n"
    "v_mfma_f32_16x16x32_bf16 v[76:79], a[196:199], v[132:135], v[76:79]\n"
    "s_mov_b32 s73, 265216\n"
    "buffer_load_dwordx4 v233, s[56:59], s73 offen offset:1024 lds\n"
    "v_mfma_f32_16x16x32_bf16 v[104:107], a[200:203], v[132:135], v[104:107]\n"
    "s_mov_b32 s73, 268288\n"
    "buffer_load_dwordx4 v228, s[56:59], s73 offen offset:2048 lds\n"
    "v_mfma_f32_16x16x32_bf16 v[108:111], a[204:207], v[132:135], v[108:111]\n"
    "s_waitcnt lgkmcnt(2)\n"
    "v_mfma_f32_16x16x32_bf16 v[64:67], a[208:211], v[136:139], v[64:67]\n"
    "ds_read_b128 v[132:135], v231 offset:28928\n"
    "v_mfma_f32_16x16x32_bf16 v[68:71], a[212:215], v[136:139], v[68:71]\n"
    "s_mov_b32 s73, 271360\n"
    "buffer_load_dwordx4 v233, s[56:59], s73 offen offset:3072 lds\n"
    "v_mfma_f32_16x16x32_bf16 v[96:99], a[216:219], v[136:139], v[96:99]\n"
    "s_add_u32 m0, s72, 4096\n"
    "s_mov_b32 s73, 294912\n"
    "buffer_load_dwordx4 v228, s[56:59], s73 offen offset:0 lds\n"
    "v_mfma_f32_16x16x32_bf16 v[100:103], a[220:223], v[136:139], v[100:103]\n"
    "s_waitcnt lgkmcnt(2)\n"
    "v_mfma_f32_16x16x32_bf16 v[72:75], a[208:211], v[140:143], v[72:75]\n"
    "ds_read_b128 v[136:139], v231 offset:30720\n"
    "v_mfma_f32_16x16x32_bf16 v[76:79], a[212:215], v[140:143], v[76:79]\n"
    "s_mov_b32 s73, 297984\n"
    "buffer_load_dwordx4 v233, s[56:59], s73 offen offset:1024 lds\n"
    "v_mfma_f32_16x16x32_bf16 v[104:107], a[216:219], v[140:143], v[104:107]\n"
    "s_mov_b32 s73, 301056\n"
    "buffer_load_dwordx4 v228, s[56:59], s73 offen offset:2048 lds\n"
    "v_mfma_f32_16x16x32_bf16 v[108:111], a[220:223], v[140:143], v[108:111]\n"
    "s_waitcnt lgkmcnt(2)\n"
    "v_mfma_f32_16x16x32_bf16 v[64:67], a[224:227], v[128:131], v[64:67]\n"
    "ds_read_b128 v[140:143], v231 offset:30976\n"
    "v_mfma_f32_16x16x32_bf16 v[68:71], a[228:231], v[128:131], v[68:71]\n"
    "s_mov_b32 s73, 304128\n"
    "buffer_load_dwordx4 v233, s[56:59], s73 offen offset:3072 lds\n"
    "v_mfma_f32_16x16x32_bf16 v[96:99], a[232:235], v[128:131], v[96:99]\n"
    "v_mfma_f32_16x16x32_bf16 v[100:103], a[236:239], v[128:131], v[100:103]\n"
    "s_waitcnt lgkmcnt(2)\n"
    "v_mfma_f32_16x16x32_bf16 v[72:75], a[224:227], v[132:135], v[72:75]\n"
    "ds_read_b128 v[128:131], v231 offset:32768\n"
    "v_mfma_f32_16x16x32_bf16 v[76:79], a[228:231], v[132:135], v[76:79]\n"
    "v_mfma_f32_16x16x32_bf16 v[104:107], a[232:235], v[132:135], v[104:107]\n"
    "v_mfma_f32_16x16x32_bf16 v[108:111], a[236:239], v[132:135], v[108:111]\n"
    "s_waitcnt lgkmcnt(2)\n"
    "v_mfma_f32_16x16x32_bf16 v[64:67], a[240:243], v[136:139], v[64:67]\n"
    "ds_read_b128 v[132:135], v231 offset:33024\n"
    "v_mfma_f32_16x16x32_bf16 v[68:71], a[244:247], v[136:139], v[68:71]\n"
    "v_mfma_f32_16x16x32_bf16 v[96:99], a[248:251], v[136:139], v[96:99]\n"
    "v_mfma_f32_16x16x32_bf16 v[100:103], a[252:255], v[136:139], v[100:103]\n"
    "s_waitcnt lgkmcnt(2)\n"
    "v_mfma_f32_16x16x32_bf16 v[72:75], a[240:243], v[140:143], v[72:75]\n"
    "ds_read_b128 v[136:139], v231 offset:34816\n"
    "v_mfma_f32_16x16x32_bf16 v[76:79], a[244:247], v[140:143], v[76:79]\n"
    "v_mfma_f32_16x16x32_bf16 v[104:107], a[248:251], v[140:143], v[104:107]\n"
    "v_mfma_f32_16x16x32_bf16 v[108:111], a[252:255], v[140:143], v[108:111]\n"
    "s_waitcnt lgkmcnt(2)\n"
    "v_mfma_f32_16x16x32_bf16 v[80:83], a[0:3], v[128:131], v[80:83]\n"
    "ds_read_b128 v[140:143], v231 offset:35072\n"
    "v_mfma_f32_16x16x32_bf16 v[84:87], a[4:7], v[128:131], v[84:87]\n"
    "v_mfma_f32_16x16x32_bf16 v[112:115], a[8:11], v[128:131], v[112:115]\n"
    "v_mfma_f32_16x16x32_bf16 v[116:119], a[12:15], v[128:131], v[116:119]\n"
    "s_waitcnt lgkmcnt(2)\n"
    "v_mfma_f32_16x16x32_bf16 v[88:91], a[0:3], v[132:135], v[88:91]\n"
    "ds_read_b128 v[128:131], v231 offset:36864\n"
    "v_mfma_f32_16x16x32_bf16 v[92:95], a[4:7], v[132:135], v[92:95]\n"
    "v_mfma_f32_16x16x32_bf16 v[120:123], a[8:11], v[132:135], v[120:123]\n"
    "v_mfma_f32_16x16x32_bf16 v[124:127], a[12:15], v[132:135], v[124:127]\n"
    "s_waitcnt lgkmcnt(2)\n"
    "v_mfma_f32_16x16x32_bf16 v[80:83], a[16:19], v[136:139], v[80:83]\n"
    "ds_read_b128 v[132:135], v231 offset:37120\n"
    "v_mfma_f32_16x16x32_bf16 v[84:87], a[20:23], v[136:139], v[84:87]\n"
    "v_mfma_f32_16x16x32_bf16 v[112:115], a[24:27], v[136:139], v[112:115]\n"
    "v_mfma_f32_16x16x32_bf16 v[116:119], a[28:31], v[136:139], v[116:119]\n"
    "s_waitcnt lgkmcnt(2)\n"
    "v_mfma_f32_16x16x32_bf16 v[88:91], a[16:19], v[140:143], v[88:91]\n"
    "ds_read_b128 v[136:139], v231 offset:38912\n"
    "v_max_f32 v64, 0, v64\n"
    "v_max_f32 v65, 0, v65\n"
    "v_mfma_f32_16x16x32_bf16 v[92:95], a[20:23], v[140:143], v[92:95]\n"
    "v_max_f32 v66, 0, v66\n"
    "v_max_f32 v67, 0, v67\n"
    "v_max_f32 v68, 0, v68\n"
    "v_mfma_f32_16x16x32_bf16 v[120:123], a[24:27], v[140:143], v[120:123]\n"
    "v_max_f32 v69, 0, v69\n"
    "v_max_f32 v70, 0, v70\n"
    "v_max_f32 v71, 0, v71\n"
    "v_mfma_f32_16x16x32_bf16 v[124:127], a[28:31], v[140:143], v[124:127]\n"
    "v_max_f32_e64 v72, 0, v72\n"
    "v_max_f32 v73, 0, v73\n"
    "s_waitcnt lgkmcnt(2)\n"
    "v_mfma_f32_16x16x32_bf16 v[80:83], a[32:35], v[128:131], v[80:83]\n"
    "ds_read_b128 v[140:143], v231 offset:39168\n"
    "v_max_f32 v74, 0, v74\n"
    "v_max_f32 v75, 0, v75\n"
    "v_mfma_f32_16x16x32_bf16 v[84:87], a[36:39], v[128:131], v[84:87]\n"
    "v_max_f32 v76, 0, v76\n"
    "v_max_f32 v77, 0, v77\n"
    "v_max_f32 v78, 0, v78\n"
    "v_mfma_f32_16x16x32_bf16 v[112:115], a[40:43], v[128:131], v[112:115]\n"
    "v_max_f32 v79, 0, v79\n"
    "v_mul_f32 v64, v64, v64\n"
    "v_mul_f32 v65, v65, v65\n"
    "v_mfma_f32_16x16x32_bf16 v[116:119], a[44:47], v[128:131], v[116:119]\n"
    "v_mul_f32_e64 v66, v66, v66\n"
    "v_mul_f32 v67, v67, v67\n"
    "s_waitcnt lgkmcnt(2)\n"
    "v_mfma_f32_16x16x32_bf16 v[88:91], a[32:35], v[132:135], v[88:91]\n"
    "ds_read_b128 v[128:131], v231 offset:40960\n"
    "v_mul_f32 v68, v68, v68\n"
    "v_mul_f32 v69, v69, v69\n"
    "v_mfma_f32_16x16x32_bf16 v[92:95], a[36:39], v[132:135], v[92:95]\n"
    "v_mul_f32 v70, v70, v70\n"
    "v_mul_f32 v71, v71, v71\n"
    "v_mul_f32 v72, v72, v72\n"
    "v_mfma_f32_16x16x32_bf16 v[120:123], a[40:43], v[132:135], v[120:123]\n"
    "v_mul_f32 v73, v73, v73\n"
    "v_mul_f32 v74, v74, v74\n"
    "v_mul_f32 v75, v75, v75\n"
    "v_mfma_f32_16x16x32_bf16 v[124:127], a[44:47], v[132:135], v[124:127]\n"
    "v_mul_f32_e64 v76, v76, v76\n"
    "v_mul_f32 v77, v77, v77\n"
    "s_waitcnt lgkmcnt(2)\n"
    "v_mfma_f32_16x16x32_bf16 v[80:83], a[48:51], v[136:139], v[80:83]\n"
    "ds_read_b128 v[132:135], v231 offset:41216\n"
    "v_mul_f32 v78, v78, v78\n"
    "v_mul_f32 v79, v79, v79\n"
    "v_mfma_f32_16x16x32_bf16 v[84:87], a[52:55], v[136:139], v[84:87]\n"
    "v_cvt_pk_bf16_f32 v64, v64, v65\n"
    "v_cvt_pk_bf16_f32 v65, v66, v67\n"
    "v_cvt_pk_bf16_f32 v66, v68, v69\n"
    "v_mfma_f32_16x16x32_bf16 v[112:115], a[56:59], v[136:139], v[112:115]\n"
    "v_cvt_pk_bf16_f32 v67, v70, v71\n"
    "v_cvt_pk_bf16_f32 v68, v72, v73\n"
    "v_cvt_pk_bf16_f32 v69, v74, v75\n"
    "v_mfma_f32_16x16x32_bf16 v[116:119], a[60:63], v[136:139], v[116:119]\n"
    "v_cvt_pk_bf16_f32 v70, v76, v77\n"
    "v_cvt_pk_bf16_f32 v71, v78, v79\n"
    "s_waitcnt lgkmcnt(2)\n"
    "v_mfma_f32_16x16x32_bf16 v[88:91], a[48:51], v[140:143], v[88:91]\n"
    "ds_read_b128 v[136:139], v231 offset:43008\n"
    "buffer_store_dwordx4 v[64:67], v224, s[52:55], s86 offen offset:0 sc1\n"
    "buffer_store_dwordx4 v[68:71], v224, s[52:55], s86 offen offset:1024 sc1\n"
    "v_mfma_f32_16x16x32_bf16 v[92:95], a[52:55], v[140:143], v[92:95]\n"
    "v_max_f32 v96, 0, v96\n"
    "v_max_f32 v97, 0, v97\n"
    "v_max_f32 v98, 0, v98\n"
    "v_mfma_f32_16x16x32_bf16 v[120:123], a[56:59], v[140:143], v[120:123]\n"
    "v_max_f32 v99, 0, v99\n"
    "v_max_f32 v100, 0, v100\n"
    "v_max_f32 v101, 0, v101\n"
    "v_mfma_f32_16x16x32_bf16 v[124:127], a[60:63], v[140:143], v[124:127]\n"
    "v_max_f32 v102, 0, v102\n"
    "v_max_f32 v103, 0, v103\n"
    "s_waitcnt lgkmcnt(2)\n"
    "v_mfma_f32_16x16x32_bf16 v[80:83], a[64:67], v[128:131], v[80:83]\n"
    "ds_read_b128 v[140:143], v231 offset:43264\n"
    "v_max_f32 v104, 0, v104\n"
    "v_max_f32 v105, 0, v105\n"
    "v_mfma_f32_16x16x32_bf16 v[84:87], a[68:71], v[128:131], v[84:87]\n"
    "v_max_f32 v106, 0, v106\n"
    "v_max_f32 v107, 0, v107\n"
    "v_max_f32 v108, 0, v108\n"
    "v_mfma_f32_16x16x32_bf16 v[112:115], a[72:75], v[128:131], v[112:115]\n"
    "v_max_f32 v109, 0, v109\n"
    "v_max_f32 v110, 0, v110\n"
    "v_max_f32 v111, 0, v111\n"
    "v_mfma_f32_16x16x32_bf16 v[116:119], a[76:79], v[128:131], v[116:119]\n"
    "v_mul_f32_e64 v96, v96, v96\n"
    "v_mul_f32 v97, v97, v97\n"
    "s_waitcnt lgkmcnt(2)\n"
    "v_mfma_f32_16x16x32_bf16 v[88:91], a[64:67], v[132:135], v[88:91]\n"
    "ds_read_b128 v[128:131], v231 offset:45056\n"
    "v_mul_f32 v98, v98, v98\n"
    "v_mul_f32 v99, v99, v99\n"
    "v_mfma_f32_16x16x32_bf16 v[92:95], a[68:71], v[132:135], v[92:95]\n"
    "v_mul_f32 v100, v100, v100\n"
    "v_mul_f32 v101, v101, v101\n"
    "v_mul_f32 v102, v102, v102\n"
    "v_mfma_f32_16x16x32_bf16 v[120:123], a[72:75], v[132:135], v[120:123]\n"
    "v_mul_f32 v103, v103, v103\n"
    "v_mul_f32 v104, v104, v104\n"
    "v_mul_f32 v105, v105, v105\n"
    "v_mfma_f32_16x16x32_bf16 v[124:127], a[76:79], v[132:135], v[124:127]\n"
    "v_mul_f32_e64 v106, v106, v106\n"
    "v_mul_f32 v107, v107, v107\n"
    "s_waitcnt lgkmcnt(2)\n"
    "v_mfma_f32_16x16x32_bf16 v[80:83], a[80:83], v[136:139], v[80:83]\n"
    "ds_read_b128 v[132:135], v231 offset:45312\n"
    "v_mul_f32 v108, v108, v108\n"
    "v_mul_f32 v109, v109, v109\n"
    "v_mfma_f32_16x16x32_bf16 v[84:87], a[84:87], v[136:139], v[84:87]\n"
    "v_mul_f32 v110, v110, v110\n"
    "v_mul_f32 v111, v111, v111\n"
    "v_cvt_pk_bf16_f32 v96, v96, v97\n"
    "v_mfma_f32_16x16x32_bf16 v[112:115], a[88:91], v[136:139], v[112:115]\n"
    "v_cvt_pk_bf16_f32 v97, v98, v99\n"
    "v_cvt_pk_bf16_f32 v98, v100, v101\n"
    "v_cvt_pk_bf16_f32 v99, v102, v103\n"
    "v_mfma_f32_16x16x32_bf16 v[116:119], a[92:95], v[136:139], v[116:119]\n"
    "v_cvt_pk_bf16_f32 v100, v104, v105\n"
    "v_cvt_pk_bf16_f32 v101, v106, v107\n"
    "s_waitcnt lgkmcnt(2)\n"
    "v_mfma_f32_16x16x32_bf16 v[88:91], a[80:83], v[140:143], v[88:91]\n"
    "ds_read_b128 v[136:139], v231 offset:47104\n"
    "v_cvt_pk_bf16_f32 v102, v108, v109\n"
    "v_cvt_pk_bf16_f32 v103, v110, v111\n"
    "v_mfma_f32_16x16x32_bf16 v[92:95], a[84:87], v[140:143], v[92:95]\n"
    "buffer_store_dwordx4 v[96:99], v224, s[52:55], s64 offen offset:0 sc1\n"
    "buffer_store_dwordx4 v[100:103], v224, s[52:55], s64 offen offset:1024 sc1\n"
    "v_mfma_f32_16x16x32_bf16 v[120:123], a[88:91], v[140:143], v[120:123]\n"
    "s_waitcnt vmcnt(24)\n"
    "ds_read_b64_tr_b16 a[0:1], v216 offset:8192\n"
    "v_mfma_f32_16x16x32_bf16 v[124:127], a[92:95], v[140:143], v[124:127]\n"
    "ds_read_b64_tr_b16 a[2:3], v216 offset:8704\n"
    "s_waitcnt lgkmcnt(4)\n"
    "v_mfma_f32_16x16x32_bf16 v[80:83], a[96:99], v[128:131], v[80:83]\n"
    "ds_read_b128 v[140:143], v231 offset:47360\n"
    "ds_read_b64_tr_b16 a[4:5], v217 offset:8192\n"
    "v_mfma_f32_16x16x32_bf16 v[84:87], a[100:103], v[128:131], v[84:87]\n"
    "ds_read_b64_tr_b16 a[6:7], v217 offset:8704\n"
    "v_mfma_f32_16x16x32_bf16 v[112:115], a[104:107], v[128:131], v[112:115]\n"
    "ds_read_b64_tr_b16 a[8:9], v216 offset:8448\n"
    "v_mfma_f32_16x16x32_bf16 v[116:119], a[108:111], v[128:131], v[116:119]\n"
    "ds_read_b64_tr_b16 a[10:11], v216 offset:8960\n"
    "s_waitcnt lgkmcnt(8)\n"
    "v_mfma_f32_16x16x32_bf16 v[88:91], a[96:99], v[132:135], v[88:91]\n"
    "ds_read_b128 v[128:131], v231 offset:49152\n"
    "ds_read_b64_tr_b16 a[12:13], v217 offset:8448\n"
    "v_mfma_f32_16x16x32_bf16 v[92:95], a[100:103], v[132:135], v[92:95]\n"
    "ds_read_b64_tr_b16 a[14:15], v217 offset:8960\n"
    "v_mfma_f32_16x16x32_bf16 v[120:123], a[104:107], v[132:135], v[120:123]\n"
    "s_waitcnt vmcnt(20)\n"
    "ds_read_b64_tr_b16 a[16:17], v216 offset:12288\n"
    "v_mfma_f32_16x16x32_bf16 v[124:127], a[108:111], v[132:135], v[124:127]\n"
    "ds_read_b64_tr_b16 a[18:19], v216 offset:12800\n"
    "s_waitcnt lgkmcnt(12)\n"
    "v_mfma_f32_16x16x32_bf16 v[80:83], a[112:115], v[136:139], v[80:83]\n"
    "ds_read_b128 v[132:135], v231 offset:49408\n"
    "ds_read_b64_tr_b16 a[20:21], v217 offset:12288\n"
    "v_mfma_f32_16x16x32_bf16 v[84:87], a[116:119], v[136:139], v[84:87]\n"
    "ds_read_b64_tr_b16 a[22:23], v217 offset:12800\n"
    "v_mfma_f32_16x16x32_bf16 v[112:115], a[120:123], v[136:139], v[112:115]\n"
    "ds_read_b64_tr_b16 a[24:25], v216 offset:12544\n"
    "v_mfma_f32_16x16x32_bf16 v[116:119], a[124:127], v[136:139], v[116:119]\n"
    "ds_read_b64_tr_b16 a[26:27], v216 offset:13056\n"
    "s_waitcnt lgkmcnt(14)\n"
    "v_mfma_f32_16x16x32_bf16 v[88:91], a[112:115], v[140:143], v[88:91]\n"
    "ds_read_b128 v[136:139], v231 offset:51200\n"
    "ds_read_b64_tr_b16 a[28:29], v217 offset:12544\n"
    "v_mfma_f32_16x16x32_bf16 v[92:95], a[116:119], v[140:143], v[92:95]\n"
    "ds_read_b64_tr_b16 a[30:31], v217 offset:13056\n"
    "v_mfma_f32_16x16x32_bf16 v[120:123], a[120:123], v[140:143], v[120:123]\n"
    "s_waitcnt vmcnt(16)\n"
    "ds_read_b64_tr_b16 a[32:33], v218 offset:0\n"
    "v_mfma_f32_16x16x32_bf16 v[124:127], a[124:127], v[140:143], v[124:127]\n"
    "ds_read_b64_tr_b16 a[34:35], v218 offset:512\n"
    "s_waitcnt lgkmcnt(14)\n"
    "v_mfma_f32_16x16x32_bf16 v[80:83], a[128:131], v[128:131], v[80:83]\n"
    "ds_read_b128 v[140:143], v231 offset:51456\n"
    "ds_read_b64_tr_b16 a[36:37], v219 offset:0\n"
    "v_mfma_f32_16x16x32_bf16 v[84:87], a[132:135], v[128:131], v[84:87]\n"
    "ds_read_b64_tr_b16 a[38:39], v219 offset:512\n"
    "v_mfma_f32_16x16x32_bf16 v[112:115], a[136:139], v[128:131], v[112:115]\n"
    "ds_read_b64_tr_b16 a[40:41], v218 offset:256\n"
    "v_mfma_f32_16x16x32_bf16 v[116:119], a[140:143], v[128:131], v[116:119]\n"
    "ds_read_b64_tr_b16 a[42:43], v218 offset:768\n"
    "s_waitcnt lgkmcnt(14)\n"
    "v_mfma_f32_16x16x32_bf16 v[88:91], a[128:131], v[132:135], v[88:91]\n"
    "ds_read_b128 v[128:131], v231 offset:53248\n"
    "ds_read_b64_tr_b16 a[44:45], v219 offset:256\n"
    "v_mfma_f32_16x16x32_bf16 v[92:95], a[132:135], v[132:135], v[92:95]\n"
    "ds_read_b64_tr_b16 a[46:47], v219 offset:768\n"
    "v_mfma_f32_16x16x32_bf16 v[120:123], a[136:139], v[132:135], v[120:123]\n"
    "s_waitcnt vmcnt(12)\n"
    "ds_read_b64_tr_b16 a[48:49], v218 offset:4096\n"
    "v_mfma_f32_16x16x32_bf16 v[124:127], a[140:143], v[132:135], v[124:127]\n"
    "ds_read_b64_tr_b16 a[50:51], v218 offset:4608\n"
    "s_waitcnt lgkmcnt(14)\n"
    "v_mfma_f32_16x16x32_bf16 v[80:83], a[144:147], v[136:139], v[80:83]\n"
    "ds_read_b128 v[132:135], v231 offset:53504\n"
    "ds_read_b64_tr_b16 a[52:53], v219 offset:4096\n"
    "v_mfma_f32_16x16x32_bf16 v[84:87], a[148:151], v[136:139], v[84:87]\n"
    "ds_read_b64_tr_b16 a[54:55], v219 offset:4608\n"
    "v_mfma_f32_16x16x32_bf16 v[112:115], a[152:155], v[136:139], v[112:115]\n"
    "ds_read_b64_tr_b16 a[56:57], v218 offset:4352\n"
    "v_mfma_f32_16x16x32_bf16 v[116:119], a[156:159], v[136:139], v[116:119]\n"
    "ds_read_b64_tr_b16 a[58:59], v218 offset:4864\n"
    "s_waitcnt lgkmcnt(14)\n"
    "v_mfma_f32_16x16x32_bf16 v[88:91], a[144:147], v[140:143], v[88:91]\n"
    "ds_read_b128 v[136:139], v231 offset:55296\n"
    "ds_read_b64_tr_b16 a[60:61], v219 offset:4352\n"
    "v_mfma_f32_16x16x32_bf16 v[92:95], a[148:151], v[140:143], v[92:95]\n"
    "ds_read_b64_tr_b16 a[62:63], v219 offset:4864\n"
    "v_mfma_f32_16x16x32_bf16 v[120:123], a[152:155], v[140:143], v[120:123]\n"
    "s_waitcnt vmcnt(8)\n"
    "ds_read_b64_tr_b16 a[64:65], v216 offset:0\n"
    "v_mfma_f32_16x16x32_bf16 v[124:127], a[156:159], v[140:143], v[124:127]\n"
    "ds_read_b64_tr_b16 a[66:67], v216 offset:512\n"
    "s_waitcnt lgkmcnt(14)\n"
    "v_mfma_f32_16x16x32_bf16 v[80:83], a[160:163], v[128:131], v[80:83]\n"
    "ds_read_b128 v[140:143], v231 offset:55552\n"
    "ds_read_b64_tr_b16 a[68:69], v217 offset:0\n"
    "v_mfma_f32_16x16x32_bf16 v[84:87], a[164:167], v[128:131], v[84:87]\n"
    "ds_read_b64_tr_b16 a[70:71], v217 offset:512\n"
    "v_mfma_f32_16x16x32_bf16 v[112:115], a[168:171], v[128:131], v[112:115]\n"
    "ds_read_b64_tr_b16 a[72:73], v216 offset:256\n"
    "ds_read_b64_tr_b16 a[74:75], v216 offset:768\n"
    "v_mfma_f32_16x16x32_bf16 v[116:119], a[172:175], v[128:131], v[116:119]\n"
    "s_waitcnt lgkmcnt(7)\n"
    "v_mfma_f32_16x16x32_bf16 v[88:91], a[160:163], v[132:135], v[88:91]\n"
    "ds_read_b128 v[128:131], v231 offset:57344\n"
    "ds_read_b64_tr_b16 a[76:77], v217 offset:256\n"
    "v_mfma_f32_16x16x32_bf16 v[92:95], a[164:167], v[132:135], v[92:95]\n"
    "ds_read_b64_tr_b16 a[78:79], v217 offset:768\n"
    "s_add_u32 m0, s72, 8192\n"
    "s_mov_b32 s73, 327680\n"
    "buffer_load_dwordx4 v228, s[56:59], s73 offen offset:0 lds\n"
    "v_mfma_f32_16x16x32_bf16 v[120:123], a[168:171], v[132:135], v[120:123]\n"
    "s_waitcnt vmcnt(5)\n"
    "ds_read_b64_tr_b16 a[80:81], v216 offset:4096\n"
    "v_mfma_f32_16x16x32_bf16 v[124:127], a[172:175], v[132:135], v[124:127]\n"
    "ds_read_b64_tr_b16 a[82:83], v216 offset:4608\n"
    "s_mov_b32 s73, 330752\n"
    "buffer_load_dwordx4 v233, s[56:59], s73 offen offset:1024 lds\n"
    "v_mfma_f32_16x16x32_bf16 v[80:83], a[176:179], v[136:139], v[80:83]\n"
    "ds_read_b128 v[132:135], v231 offset:57600\n"
    "ds_read_b64_tr_b16 a[84:85], v217 offset:4096\n"
    "v_mfma_f32_16x16x32_bf16 v[84:87], a[180:183], v[136:139], v[84:87]\n"
    "ds_read_b64_tr_b16 a[86:87], v217 offset:4608\n"
    "s_mov_b32 s73, 333824\n"
    "buffer_load_dwordx4 v228, s[56:59], s73 offen offset:2048 lds\n"
    "v_mfma_f32_16x16x32_bf16 v[112:115], a[184:187], v[136:139], v[112:115]\n"
    "ds_read_b64_tr_b16 a[88:89], v216 offset:4352\n"
    "ds_read_b64_tr_b16 a[90:91], v216 offset:4864\n"
    "v_mfma_f32_16x16x32_bf16 v[116:119], a[188:191], v[136:139], v[116:119]\n"
    "s_mov_b32 s73, 336896\n"
    "buffer_load_dwordx4 v233, s[56:59], s73 offen offset:3072 lds\n"
    "s_waitcnt lgkmcnt(14)\n"
    "v_mfma_f32_16x16x32_bf16 v[88:91], a[176:179], v[140:143], v[88:91]\n"
    "ds_read_b128 v[136:139], v231 offset:59392\n"
    "ds_read_b64_tr_b16 a[92:93], v217 offset:4352\n"
    "v_mfma_f32_16x16x32_bf16 v[92:95], a[180:183], v[140:143], v[92:95]\n"
    "ds_read_b64_tr_b16 a[94:95], v217 offset:4864\n"
    "s_add_u32 m0, s72, 12288\n"
    "s_mov_b32 s73, 360448\n"
    "buffer_load_dwordx4 v228, s[56:59], s73 offen offset:0 lds\n"
    "v_mfma_f32_16x16x32_bf16 v[120:123], a[184:187], v[140:143], v[120:123]\n"
    "s_mov_b32 s73, 363520\n"
    "buffer_load_dwordx4 v233, s[56:59], s73 offen offset:1024 lds\n"
    "v_mfma_f32_16x16x32_bf16 v[124:127], a[188:191], v[140:143], v[124:127]\n"
    "s_waitcnt lgkmcnt(12)\n"
    "v_mfma_f32_16x16x32_bf16 v[80:83], a[192:195], v[128:131], v[80:83]\n"
    "ds_read_b128 v[140:143], v231 offset:59648\n"
    "v_mfma_f32_16x16x32_bf16 v[84:87], a[196:199], v[128:131], v[84:87]\n"
    "s_mov_b32 s73, 366592\n"
    "buffer_load_dwordx4 v228, s[56:59], s73 offen offset:2048 lds\n"
    "v_mfma_f32_16x16x32_bf16 v[112:115], a[200:203], v[128:131], v[112:115]\n"
    "s_mov_b32 s73, 369664\n"
    "buffer_load_dwordx4 v233, s[56:59], s73 offen offset:3072 lds\n"
    "v_mfma_f32_16x16x32_bf16 v[116:119], a[204:207], v[128:131], v[116:119]\n"
    "s_waitcnt lgkmcnt(8)\n"
    "v_mfma_f32_16x16x32_bf16 v[88:91], a[192:195], v[132:135], v[88:91]\n"
    "ds_read_b128 v[128:131], v231 offset:61440\n"
    "s_add_u32 m0, s8, 0\n"
    "s_mov_b32 s73, 393216\n"
    "buffer_load_dwordx4 v228, s[56:59], s73 offen offset:0 lds\n"
    "v_mov_b32 v220, s94\n"
    "v_mfma_f32_16x16x32_bf16 v[92:95], a[196:199], v[132:135], v[92:95]\n"
    "v_mov_b32 v221, s95\n"
    "v_mov_b32 v222, 0\n"
    "v_mfma_f32_16x16x32_bf16 v[120:123], a[200:203], v[132:135], v[120:123]\n"
    "s_mov_b32 s73, 396288\n"
    "buffer_load_dwordx4 v233, s[56:59], s73 offen offset:1024 lds\n"
    "s_waitcnt vmcnt(10)\n"
    "v_mfma_f32_16x16x32_bf16 v[124:127], a[204:207], v[132:135], v[124:127]\n"
    "s_mov_b64 exec, 1\n"
    "buffer_store_dwordx2 v[220:221], v222, s[60:63], s96 offen sc1\n"
    "s_mov_b64 exec, -1\n"
    "s_waitcnt lgkmcnt(4)\n"
    "v_mfma_f32_16x16x32_bf16 v[80:83], a[208:211], v[136:139], v[80:83]\n"
    "ds_read_b128 v[132:135], v231 offset:61696\n"
    "v_mfma_f32_16x16x32_bf16 v[84:87], a[212:215], v[136:139], v[84:87]\n"
    "s_mov_b32 s73, 399360\n"
    "buffer_load_dwordx4 v228, s[56:59], s73 offen offset:2048 lds\n"
    "v_mfma_f32_16x16x32_bf16 v[112:115], a[216:219], v[136:139], v[112:115]\n"
    "s_mov_b32 s73, 402432\n"
    "buffer_load_dwordx4 v233, s[56:59], s73 offen offset:3072 lds\n"
    "v_mfma_f32_16x16x32_bf16 v[116:119], a[220:223], v[136:139], v[116:119]\n"
    "s_waitcnt lgkmcnt(2)\n"
    "v_mfma_f32_16x16x32_bf16 v[88:91], a[208:211], v[140:143], v[88:91]\n"
    "ds_read_b128 v[136:139], v231 offset:63488\n"
    "v_mfma_f32_16x16x32_bf16 v[92:95], a[212:215], v[140:143], v[92:95]\n"
    "s_add_u32 m0, s8, 4096\n"
    "s_mov_b32 s73, 425984\n"
    "buffer_load_dwordx4 v228, s[56:59], s73 offen offset:0 lds\n"
    "v_mfma_f32_16x16x32_bf16 v[120:123], a[216:219], v[140:143], v[120:123]\n"
    "s_mov_b32 s73, 429056\n"
    "buffer_load_dwordx4 v233, s[56:59], s73 offen offset:1024 lds\n"
    "v_mfma_f32_16x16x32_bf16 v[124:127], a[220:223], v[140:143], v[124:127]\n"
    "s_waitcnt lgkmcnt(2)\n"
    "v_mfma_f32_16x16x32_bf16 v[80:83], a[224:227], v[128:131], v[80:83]\n"
    "ds_read_b128 v[140:143], v231 offset:63744\n"
    "v_mfma_f32_16x16x32_bf16 v[84:87], a[228:231], v[128:131], v[84:87]\n"
    "s_mov_b32 s73, 432128\n"
    "buffer_load_dwordx4 v228, s[56:59], s73 offen offset:2048 lds\n"
    "v_mfma_f32_16x16x32_bf16 v[112:115], a[232:235], v[128:131], v[112:115]\n"
    "s_mov_b32 s73, 435200\n"
    "buffer_load_dwordx4 v233, s[56:59], s73 offen offset:3072 lds\n"
    "v_mfma_f32_16x16x32_bf16 v[116:119], a[236:239], v[128:131], v[116:119]\n"
    "s_waitcnt lgkmcnt(2)\n"
    "v_mfma_f32_16x16x32_bf16 v[88:91], a[224:227], v[132:135], v[88:91]\n"
    "v_mfma_f32_16x16x32_bf16 v[92:95], a[228:231], v[132:135], v[92:95]\n"
    "v_mfma_f32_16x16x32_bf16 v[120:123], a[232:235], v[132:135], v[120:123]\n"
    "v_mfma_f32_16x16x32_bf16 v[124:127], a[236:239], v[132:135], v[124:127]\n"
    "s_waitcnt lgkmcnt(1)\n"
    "v_mfma_f32_16x16x32_bf16 v[80:83], a[240:243], v[136:139], v[80:83]\n"
    "v_mfma_f32_16x16x32_bf16 v[84:87], a[244:247], v[136:139], v[84:87]\n"
    "v_mfma_f32_16x16x32_bf16 v[112:115], a[248:251], v[136:139], v[112:115]\n"
    "v_mfma_f32_16x16x32_bf16 v[116:119], a[252:255], v[136:139], v[116:119]\n"
    "s_waitcnt lgkmcnt(0)\n"
    "v_mfma_f32_16x16x32_bf16 v[88:91], a[240:243], v[140:143], v[88:91]\n"
    "v_mfma_f32_16x16x32_bf16 v[92:95], a[244:247], v[140:143], v[92:95]\n"
    "v_mfma_f32_16x16x32_bf16 v[120:123], a[248:251], v[140:143], v[120:123]\n"
    "v_mfma_f32_16x16x32_bf16 v[124:127], a[252:255], v[140:143], v[124:127]\n"
    "s_add_u32 s97, s88, 196608\n"
    "v_mov_b32 v225, v242\n"
    "v_lshlrev_b32 v226, 16, v234\n"
    "v_and_b32 v227, s74, v234\n"
    "v_lshlrev_b32 v228, 16, v235\n"
    "v_and_b32 v229, s74, v235\n"
    "v_lshlrev_b32 v230, 16, v236\n"
    "v_and_b32 v231, s74, v236\n"
    "v_lshlrev_b32 v232, 16, v237\n"
    "v_and_b32 v233, s74, v237\n"
    "s_nop 1\n"
    "v_max_f32 v80, 0, v80\n"
    "v_max_f32 v81, 0, v81\n"
    "v_max_f32 v82, 0, v82\n"
    "v_max_f32 v83, 0, v83\n"
    "v_max_f32 v84, 0, v84\n"
    "v_max_f32 v85, 0, v85\n"
    "v_max_f32 v86, 0, v86\n"
    "v_max_f32 v87, 0, v87\n"
    "v_max_f32 v88, 0, v88\n"
    "v_max_f32 v89, 0, v89\n"
    "v_max_f32 v90, 0, v90\n"
    "v_max_f32 v91, 0, v91\n"
    "v_max_f32 v92, 0, v92\n"
    "v_max_f32 v93, 0, v93\n"
    "v_max_f32 v94, 0, v94\n"
    "v_max_f32_e64 v95, 0, v95\n"
    "v_pk_mul_f32 v[80:81], v[80:81], v[80:81]\n"
    "v_pk_mul_f32 v[82:83], v[82:83], v[82:83]\n"
    "v_pk_mul_f32 v[84:85], v[84:85], v[84:85]\n"
    "v_pk_mul_f32 v[86:87], v[86:87], v[86:87]\n"
    "v_pk_mul_f32 v[88:89], v[88:89], v[88:89]\n"
    "v_pk_mul_f32 v[90:91], v[90:91], v[90:91]\n"
    "v_pk_mul_f32 v[92:93], v[92:93], v[92:93]\n"
    "v_pk_mul_f32 v[94:95], v[94:95], v[94:95]\n"
    "v_cvt_pk_bf16_f32 v80, v80, v81\n"
    "v_cvt_pk_bf16_f32 v81, v82, v83\n"
    "v_cvt_pk_bf16_f32 v82, v84, v85\n"
    "v_cvt_pk_bf16_f32 v83, v86, v87\n"
    "v_cvt_pk_bf16_f32 v84, v88, v89\n"
    "v_cvt_pk_bf16_f32 v85, v90, v91\n"
    "v_cvt_pk_bf16_f32 v86, v92, v93\n"
    "v_cvt_pk_bf16_f32 v87, v94, v95\n"
    "buffer_load_dwordx2 v[252:253], v225, s[60:63], s97 offen sc1\n"
    "buffer_store_dwordx4 v[80:83], v224, s[52:55], s86 offen offset:2048 sc1\n"
    "buffer_store_dwordx4 v[84:87], v224, s[52:55], s86 offen offset:3072 sc1\n"
    "v_max_f32 v112, 0, v112\n"
    "v_max_f32 v113, 0, v113\n"
    "v_max_f32 v114, 0, v114\n"
    "v_max_f32 v115, 0, v115\n"
    "v_max_f32 v116, 0, v116\n"
    "v_max_f32 v117, 0, v117\n"
    "v_max_f32 v118, 0, v118\n"
    "v_max_f32 v119, 0, v119\n"
    "v_max_f32 v120, 0, v120\n"
    "v_max_f32 v121, 0, v121\n"
    "v_max_f32 v122, 0, v122\n"
    "v_max_f32 v123, 0, v123\n"
    "v_max_f32 v124, 0, v124\n"
    "v_max_f32 v125, 0, v125\n"
    "v_max_f32 v126, 0, v126\n"
    "v_max_f32 v127, 0, v127\n"
    "v_pk_mul_f32 v[112:113], v[112:113], v[112:113]\n"
    "v_pk_mul_f32 v[114:115], v[114:115], v[114:115]\n"
    "v_pk_mul_f32 v[116:117], v[116:117], v[116:117]\n"
    "v_pk_mul_f32 v[118:119], v[118:119], v[118:119]\n"
    "v_pk_mul_f32 v[120:121], v[120:121], v[120:121]\n"
    "v_pk_mul_f32 v[122:123], v[122:123], v[122:123]\n"
    "v_pk_mul_f32 v[124:125], v[124:125], v[124:125]\n"
    "v_pk_mul_f32 v[126:127], v[126:127], v[126:127]\n"
    "v_cvt_pk_bf16_f32 v112, v112, v113\n"
    "v_cvt_pk_bf16_f32 v113, v114, v115\n"
    "v_cvt_pk_bf16_f32 v114, v116, v117\n"
    "v_cvt_pk_bf16_f32 v115, v118, v119\n"
    "v_cvt_pk_bf16_f32 v116, v120, v121\n"
    "v_cvt_pk_bf16_f32 v117, v122, v123\n"
    "v_cvt_pk_bf16_f32 v118, v124, v125\n"
    "v_cvt_pk_bf16_f32 v119, v126, v127\n"
    "buffer_store_dwordx4 v[112:115], v224, s[52:55], s64 offen offset:2048 sc1\n"
    "buffer_store_dwordx4 v[116:119], v224, s[52:55], s64 offen offset:3072 sc1\n"
    "s_barrier\n"
    "v_lshlrev_b32 v0, 16, v248\n"
    "v_and_b32 v1, s74, v248\n"
    "v_lshlrev_b32 v238, 16, v208\n"
    "v_and_b32 v239, s74, v208\n"
    "v_lshlrev_b32 v8, 16, v212\n"
    "v_and_b32 v9, s74, v212\n"
    "v_lshlrev_b32 v2, 16, v249\n"
    "v_and_b32 v3, s74, v249\n"
    "v_lshlrev_b32 v240, 16, v209\n"
    "v_and_b32 v241, s74, v209\n"
    "v_lshlrev_b32 v10, 16, v213\n"
    "v_and_b32 v11, s74, v213\n"
    "v_lshlrev_b32 v4, 16, v250\n"
    "v_and_b32 v5, s74, v250\n"
    "v_lshlrev_b32 v242, 16, v210\n"
    "v_and_b32 v243, s74, v210\n"
    "v_lshlrev_b32 v12, 16, v214\n"
    "v_and_b32 v13, s74, v214\n"
    "v_lshlrev_b32 v6, 16, v251\n"
    "v_and_b32 v7, s74, v251\n"
    "v_lshlrev_b32 v244, 16, v211\n"
    "v_and_b32 v245, s74, v211\n"
    "v_lshlrev_b32 v14, 16, v215\n"
    "v_and_b32_e64 v15, s74, v215\n"
    "v_pk_fma_f32 v[238:239], v[226:227], v[0:1], v[238:239]\n"
    "v_pk_fma_f32 v[208:209], v[226:227], v[0:1], v[8:9]\n"
    "v_pk_fma_f32 v[240:241], v[228:229], v[2:3], v[240:241]\n"
    "v_pk_fma_f32 v[210:211], v[228:229], v[2:3], v[10:11]\n"
    "v_pk_fma_f32 v[242:243], v[230:231], v[4:5], v[242:243]\n"
    "v_pk_fma_f32 v[212:213], v[230:231], v[4:5], v[12:13]\n"
    "v_pk_fma_f32 v[244:245], v[232:233], v[6:7], v[244:245]\n"
    "v_pk_fma_f32 v[214:215], v[232:233], v[6:7], v[14:15]\n"
    "s_waitcnt vmcnt(4)\n"
    "ds_read_b64_tr_b16 a[96:97], v216 offset:8192\n"
    "ds_read_b64_tr_b16 a[98:99], v216 offset:8704\n"
    "ds_read_b64_tr_b16 a[100:101], v217 offset:8192\n"
    "ds_read_b64_tr_b16 a[102:103], v217 offset:8704\n"
    "ds_read_b64_tr_b16 a[104:105], v216 offset:8448\n"
    "ds_read_b64_tr_b16 a[106:107], v216 offset:8960\n"
    "ds_read_b64_tr_b16 a[108:109], v217 offset:8448\n"
    "ds_read_b64_tr_b16 a[110:111], v217 offset:8960\n"
    "ds_read_b64_tr_b16 a[112:113], v216 offset:12288\n"
    "ds_read_b64_tr_b16 a[114:115], v216 offset:12800\n"
    "ds_read_b64_tr_b16 a[116:117], v217 offset:12288\n"
    "ds_read_b64_tr_b16 a[118:119], v217 offset:12800\n"
    "ds_read_b64_tr_b16 a[120:121], v216 offset:12544\n"
    "ds_read_b64_tr_b16 a[122:123], v216 offset:13056\n"
    "ds_read_b64_tr_b16 a[124:125], v217 offset:12544\n"
    "ds_read_b64_tr_b16 a[126:127], v217 offset:13056\n"
    "ds_read_b64_tr_b16 a[128:129], v218 offset:0\n"
    "ds_read_b64_tr_b16 a[130:131], v218 offset:512\n"
    "ds_read_b64_tr_b16 a[132:133], v219 offset:0\n"
    "ds_read_b64_tr_b16 a[134:135], v219 offset:512\n"
    "ds_read_b64_tr_b16 a[136:137], v218 offset:256\n"
    "ds_read_b64_tr_b16 a[138:139], v218 offset:768\n"
    "ds_read_b64_tr_b16 a[140:141], v219 offset:256\n"
    "ds_read_b64_tr_b16 a[142:143], v219 offset:768\n"
    "ds_read_b64_tr_b16 a[144:145], v218 offset:4096\n"
    "ds_read_b64_tr_b16 a[146:147], v218 offset:4608\n"
    "ds_read_b64_tr_b16 a[148:149], v219 offset:4096\n"
    "ds_read_b64_tr_b16 a[150:151], v219 offset:4608\n"
    "ds_read_b64_tr_b16 a[152:153], v218 offset:4352\n"
    "ds_read_b64_tr_b16 a[154:155], v218 offset:4864\n"
    "ds_read_b64_tr_b16 a[156:157], v219 offset:4352\n"
    "ds_read_b64_tr_b16 a[158:159], v219 offset:4864\n"
    "s_lshl_b32 s73, s30, 10\n"
    "v_add_u32_e64 v216, s73, v224\n"
    "v_and_b32 v217, 0xf0, v224\n"
    "v_and_b32 v16, 0x100, v224\n"
    "v_lshl_or_b32 v217, v16, 1, v217\n"
    "v_and_b32 v16, 0x200, v224\n"
    "v_lshl_or_b32 v217, v16, 3, v217\n"
    "v_add_u32 v217, s75, v217\n"
    "s_add_u32 s96, s96, 64\n"
    "s_mov_b32 s91, 0\n"
    "v_cmp_ne_u64 vcc, s[94:95], v[252:253]\n"
    "s_cbranch_vccz pl3_ok\n"
    "s_waitcnt vmcnt(0)\n"
    "v_mov_b32 v220, s94\n"
    "v_mov_b32 v221, s95\n"
    "v_mov_b32 v222, 0\n"
    "s_mov_b64 exec, 1\n"
    "buffer_store_dwordx2 v[220:221], v222, s[60:63], s96 offen sc1\n"
    "s_mov_b64 exec, -1\n"
    "s_mov_b32 s91, 1\n"
    "s_mov_b32 s90, 32768\n"
    "pl3:\n"
    "buffer_load_dwordx2 v[220:221], v225, s[60:63], s97 offen sc1\n"
    "s_waitcnt vmcnt(0)\n"
    "v_cmp_ne_u64 vcc, s[94:95], v[220:221]\n"
    "s_cbranch_vccz pl3_ok\n"
    "s_sub_u32 s90, s90, 1\n"
    "s_cbranch_scc1 pl3_ok\n"
    "s_sleep 2\n"
    "s_branch pl3\n"
    "pl3_ok:\n"
    "s_add_u32 s73, s87, 0\n"
    "buffer_load_dwordx4 v[0:3], v224, s[60:63], s73 offen offset:0 sc1\n"
    "buffer_load_dwordx4 v[4:7], v224, s[60:63], s73 offen offset:1024 sc1\n"
    "s_add_u32 s73, s87, 4096\n"
    "buffer_load_dwordx4 v[8:11], v224, s[60:63], s73 offen offset:0 sc1\n"
    "buffer_load_dwordx4 v[12:15], v224, s[60:63], s73 offen offset:1024 sc1\n"
    "s_add_u32 s73, s87, 8192\n"
    "buffer_load_dwordx4 v[16:19], v224, s[60:63], s73 offen offset:0 sc1\n"
    "buffer_load_dwordx4 v[20:23], v224, s[60:63], s73 offen offset:1024 sc1\n"
    "s_add_u32 s73, s87, 12288\n"
    "buffer_load_dwordx4 v[24:27], v224, s[60:63], s73 offen offset:0 sc1\n"
    "buffer_load_dwordx4 v[28:31], v224, s[60:63], s73 offen offset:1024 sc1\n"
    "s_add_u32 s73, s87, 16384\n"
    "buffer_load_dwordx4 v[32:35], v224, s[60:63], s73 offen offset:0 sc1\n"
    "buffer_load_dwordx4 v[36:39], v224, s[60:63], s73 offen offset:1024 sc1\n"
    "s_add_u32 s73, s87, 20480\n"
    "buffer_load_dwordx4 v[40:43], v224, s[60:63], s73 offen offset:0 sc1\n"
    "buffer_load_dwordx4 v[44:47], v224, s[60:63], s73 offen offset:1024 sc1\n"
    "s_waitcnt vmcnt(12) lgkmcnt(0)\n"
    "s_cmp_lg_u32 s91, 0\n"
    "s_cbranch_scc1 fb_done\n"
    "v_mov_b32 v220, s94\n"
    "v_mov_b32 v221, s95\n"
    "v_mov_b32 v222, 0\n"
    "s_mov_b64 exec, 1\n"
    "buffer_store_dwordx2 v[220:221], v222, s[60:63], s96 offen sc1\n"
    "s_mov_b64 exec, -1\n"
    "fb_done:\n"
    "s_add_u32 s97, s97, 64\n"
    "s_waitcnt vmcnt(11)\n"
    "v_mfma_f32_16x16x32_bf16 v[64:67], v[144:147], v[0:3], 0\n"
    "v_mfma_f32_16x16x32_bf16 v[68:71], v[148:151], v[0:3], 0\n"
    "s_add_u32 s73, s87, 24576\n"
    "buffer_load_dwordx4 v[48:51], v224, s[60:63], s73 offen offset:0 sc1\n"
    "v_mfma_f32_16x16x32_bf16 v[96:99], v[152:155], v[0:3], 0\n"
    "v_mfma_f32_16x16x32_bf16 v[100:103], v[156:159], v[0:3], 0\n"
    "buffer_load_dwordx4 v[52:55], v224, s[60:63], s73 offen offset:1024 sc1\n"
    "s_waitcnt vmcnt(12)\n"
    "v_mfma_f32_16x16x32_bf16 v[72:75], v[144:147], v[4:7], 0\n"
    "v_mfma_f32_16x16x32_bf16 v[76:79], v[148:151], v[4:7], 0\n"
    "s_add_u32 s73, s87, 28672\n"
    "buffer_load_dwordx4 v[56:59], v224, s[60:63], s73 offen offset:0 sc1\n"
    "v_mfma_f32_16x16x32_bf16 v[104:107], v[152:155], v[4:7], 0\n"
    "v_mfma_f32_16x16x32_bf16 v[108:111], v[156:159], v[4:7], 0\n"
    "buffer_load_dwordx4 v[60:63], v224, s[60:63], s73 offen offset:1024 sc1\n"
    "s_waitcnt vmcnt(13)\n"
    "v_mfma_f32_16x16x32_bf16 v[64:67], v[160:163], v[8:11], v[64:67]\n"
    "v_mfma_f32_16x16x32_bf16 v[68:71], v[164:167], v[8:11], v[68:71]\n"
    "s_add_u32 s73, s87, 32768\n"
    "buffer_load_dwordx4 v[128:131], v224, s[60:63], s73 offen offset:0 sc1\n"
    "v_mfma_f32_16x16x32_bf16 v[96:99], v[168:171], v[8:11], v[96:99]\n"
    "v_mfma_f32_16x16x32_bf16 v[100:103], v[172:175], v[8:11], v[100:103]\n"
    "buffer_load_dwordx4 v[132:135], v224, s[60:63], s73 offen offset:1024 sc1\n"
    "s_waitcnt vmcnt(14)\n"
    "v_mfma_f32_16x16x32_bf16 v[72:75], v[160:163], v[12:15], v[72:75]\n"
    "v_mfma_f32_16x16x32_bf16 v[76:79], v[164:167], v[12:15], v[76:79]\n"
    "s_add_u32 s73, s87, 36864\n"
    "buffer_load_dwordx4 v[136:139], v224, s[60:63], s73 offen offset:0 sc1\n"
    "v_mfma_f32_16x16x32_bf16 v[104:107], v[168:171], v[12:15], v[104:107]\n"
    "v_mfma_f32_16x16x32_bf16 v[108:111], v[172:175], v[12:15], v[108:111]\n"
    "buffer_load_dwordx4 v[140:143], v224, s[60:63], s73 offen offset:1024 sc1\n"
    "s_waitcnt vmcnt(15)\n"
    "v_mfma_f32_16x16x32_bf16 v[64:67], v[176:179], v[16:19], v[64:67]\n"
    "v_mfma_f32_16x16x32_bf16 v[68:71], v[180:183], v[16:19], v[68:71]\n"
    "s_add_u32 s73, s87, 40960\n"
    "buffer_load_dwordx4 v[0:3], v224, s[60:63], s73 offen offset:0 sc1\n"
    "v_mfma_f32_16x16x32_bf16 v[96:99], v[184:187], v[16:19], v[96:99]\n"
    "v_mfma_f32_16x16x32_bf16 v[100:103], v[188:191], v[16:19], v[100:103]\n"
    "buffer_load_dwordx4 v[4:7], v224, s[60:63], s73 offen offset:1024 sc1\n"
    "s_waitcnt vmcnt(16)\n"
    "v_mfma_f32_16x16x32_bf16 v[72:75], v[176:179], v[20:23], v[72:75]\n"
    "v_mfma_f32_16x16x32_bf16 v[76:79], v[180:183], v[20:23], v[76:79]\n"
    "s_add_u32 s73, s87, 45056\n"
    "buffer_load_dwordx4 v[8:11], v224, s[60:63], s73 offen offset:0 sc1\n"
    "v_mfma_f32_16x16x32_bf16 v[104:107], v[184:187], v[20:23], v[104:107]\n"
    "v_mfma_f32_16x16x32_bf16 v[108:111], v[188:191], v[20:23], v[108:111]\n"
    "buffer_load_dwordx4 v[12:15], v224, s[60:63], s73 offen offset:1024 sc1\n"
    "s_waitcnt vmcnt(17)\n"
    "v_mfma_f32_16x16x32_bf16 v[64:67], v[192:195], v[24:27], v[64:67]\n"
    "v_mfma_f32_16x16x32_bf16 v[68:71], v[196:199], v[24:27], v[68:71]\n"
    "s_add_u32 s73, s87, 49152\n"
    "buffer_load_dwordx4 v[16:19], v224, s[60:63], s73 offen offset:0 sc1\n"
    "v_mfma_f32_16x16x32_bf16 v[96:99], v[200:203], v[24:27], v[96:99]\n"
    "v_mfma_f32_16x16x32_bf16 v[100:103], v[204:207], v[24:27], v[100:103]\n"
    "buffer_load_dwordx4 v[20:23], v224, s[60:63], s73 offen offset:1024 sc1\n"
    "s_waitcnt vmcnt(18)\n"
    "v_mfma_f32_16x16x32_bf16 v[72:75], v[192:195], v[28:31], v[72:75]\n"
    "v_mfma_f32_16x16x32_bf16 v[76:79], v[196:199], v[28:31], v[76:79]\n"
    "v_mfma_f32_16x16x32_bf16 v[104:107], v[200:203], v[28:31], v[104:107]\n"
    "v_mfma_f32_16x16x32_bf16 v[108:111], v[204:207], v[28:31], v[108:111]\n"
    "s_add_u32 s73, s87, 53248\n"
    "buffer_load_dwordx4 v[24:27], v224, s[60:63], s73 offen offset:0 sc1\n"
    "s_waitcnt vmcnt(18)\n"
    "v_mfma_f32_16x16x32_bf16 v[64:67], a[0:3], v[32:35], v[64:67]\n"
    "v_mfma_f32_16x16x32_bf16 v[68:71], a[4:7], v[32:35], v[68:71]\n"
    "v_mfma_f32_16x16x32_bf16 v[96:99], a[8:11], v[32:35], v[96:99]\n"
    "v_mfma_f32_16x16x32_bf16 v[100:103], a[12:15], v[32:35], v[100:103]\n"
    "buffer_load_dwordx4 v[28:31], v224, s[60:63], s73 offen offset:1024 sc1\n"
    "s_waitcnt vmcnt(18)\n"
    "v_mfma_f32_16x16x32_bf16 v[72:75], a[0:3], v[36:39], v[72:75]\n"
    "v_mfma_f32_16x16x32_bf16 v[76:79], a[4:7], v[36:39], v[76:79]\n"
    "v_mfma_f32_16x16x32_bf16 v[104:107], a[8:11], v[36:39], v[104:107]\n"
    "v_mfma_f32_16x16x32_bf16 v[108:111], a[12:15], v[36:39], v[108:111]\n"
    "s_waitcnt vmcnt(17)\n"
    "v_mfma_f32_16x16x32_bf16 v[64:67], a[16:19], v[40:43], v[64:67]\n"
    "v_mfma_f32_16x16x32_bf16 v[68:71], a[20:23], v[40:43], v[68:71]\n"
    "v_mfma_f32_16x16x32_bf16 v[96:99], a[24:27], v[40:43], v[96:99]\n"
    "v_mfma_f32_16x16x32_bf16 v[100:103], a[28:31], v[40:43], v[100:103]\n"
    "s_waitcnt vmcnt(16)\n"
    "v_mfma_f32_16x16x32_bf16 v[72:75], a[16:19], v[44:47], v[72:75]\n"
    "v_mfma_f32_16x16x32_bf16 v[76:79], a[20:23], v[44:47], v[76:79]\n"
    "v_mfma_f32_16x16x32_bf16 v[104:107], a[24:27], v[44:47], v[104:107]\n"
    "v_mfma_f32_16x16x32_bf16 v[108:111], a[28:31], v[44:47], v[108:111]\n"
    "s_waitcnt vmcnt(15)\n"
    "v_mfma_f32_16x16x32_bf16 v[64:67], a[32:35], v[48:51], v[64:67]\n"
    "v_mfma_f32_16x16x32_bf16 v[68:71], a[36:39], v[48:51], v[68:71]\n"
    "v_mfma_f32_16x16x32_bf16 v[96:99], a[40:43], v[48:51], v[96:99]\n"
    "v_mfma_f32_16x16x32_bf16 v[100:103], a[44:47], v[48:51], v[100:103]\n"
    "s_waitcnt vmcnt(14)\n"
    "v_mfma_f32_16x16x32_bf16 v[72:75], a[32:35], v[52:55], v[72:75]\n"
    "v_mfma_f32_16x16x32_bf16 v[76:79], a[36:39], v[52:55], v[76:79]\n"
    "v_mfma_f32_16x16x32_bf16 v[104:107], a[40:43], v[52:55], v[104:107]\n"
    "v_mfma_f32_16x16x32_bf16 v[108:111], a[44:47], v[52:55], v[108:111]\n"
    "s_waitcnt vmcnt(13)\n"
    "v_mfma_f32_16x16x32_bf16 v[64:67], a[48:51], v[56:59], v[64:67]\n"
    "v_mfma_f32_16x16x32_bf16 v[68:71], a[52:55], v[56:59], v[68:71]\n"
    "v_mfma_f32_16x16x32_bf16 v[96:99], a[56:59], v[56:59], v[96:99]\n"
    "v_mfma_f32_16x16x32_bf16 v[100:103], a[60:63], v[56:59], v[100:103]\n"
    "s_waitcnt vmcnt(12)\n"
    "v_mfma_f32_16x16x32_bf16 v[72:75], a[48:51], v[60:63], v[72:75]\n"
    "v_mfma_f32_16x16x32_bf16 v[76:79], a[52:55], v[60:63], v[76:79]\n"
    "v_mfma_f32_16x16x32_bf16 v[104:107], a[56:59], v[60:63], v[104:107]\n"
    "v_mfma_f32_16x16x32_bf16 v[108:111], a[60:63], v[60:63], v[108:111]\n"
    "s_waitcnt vmcnt(11)\n"
    "v_mfma_f32_16x16x32_bf16 v[64:67], a[64:67], v[128:131], v[64:67]\n"
    "v_mfma_f32_16x16x32_bf16 v[68:71], a[68:71], v[128:131], v[68:71]\n"
    "v_mfma_f32_16x16x32_bf16 v[96:99], a[72:75], v[128:131], v[96:99]\n"
    "v_mfma_f32_16x16x32_bf16 v[100:103], a[76:79], v[128:131], v[100:103]\n"
    "s_waitcnt vmcnt(10)\n"
    "v_mfma_f32_16x16x32_bf16 v[72:75], a[64:67], v[132:135], v[72:75]\n"
    "v_mfma_f32_16x16x32_bf16 v[76:79], a[68:71], v[132:135], v[76:79]\n"
    "v_mfma_f32_16x16x32_bf16 v[104:107], a[72:75], v[132:135], v[104:107]\n"
    "v_mfma_f32_16x16x32_bf16 v[108:111], a[76:79], v[132:135], v[108:111]\n"
    "s_waitcnt vmcnt(9)\n"
    "v_mfma_f32_16x16x32_bf16 v[64:67], a[80:83], v[136:139], v[64:67]\n"
    "v_mfma_f32_16x16x32_bf16 v[68:71], a[84:87], v[136:139], v[68:71]\n"
    "v_mfma_f32_16x16x32_bf16 v[96:99], a[88:91], v[136:139], v[96:99]\n"
    "v_mfma_f32_16x16x32_bf16 v[100:103], a[92:95], v[136:139], v[100:103]\n"
    "s_waitcnt vmcnt(8)\n"
    "v_mfma_f32_16x16x32_bf16 v[72:75], a[80:83], v[140:143], v[72:75]\n"
    "v_mfma_f32_16x16x32_bf16 v[76:79], a[84:87], v[140:143], v[76:79]\n"
    "v_mfma_f32_16x16x32_bf16 v[104:107], a[88:91], v[140:143], v[104:107]\n"
    "v_mfma_f32_16x16x32_bf16 v[108:111], a[92:95], v[140:143], v[108:111]\n"
    "s_waitcnt vmcnt(7)\n"
    "v_mfma_f32_16x16x32_bf16 v[64:67], a[96:99], v[0:3], v[64:67]\n"
    "v_mfma_f32_16x16x32_bf16 v[68:71], a[100:103], v[0:3], v[68:71]\n"
    "v_mfma_f32_16x16x32_bf16 v[96:99], a[104:107], v[0:3], v[96:99]\n"
    "v_mfma_f32_16x16x32_bf16 v[100:103], a[108:111], v[0:3], v[100:103]\n"
    "buffer_load_dwordx2 v[252:253], v225, s[60:63], s97 offen sc1\n"
    "s_waitcnt vmcnt(7)\n"
    "v_mfma_f32_16x16x32_bf16 v[72:75], a[96:99], v[4:7], v[72:75]\n"
    "v_mfma_f32_16x16x32_bf16 v[76:79], a[100:103], v[4:7], v[76:79]\n"
    "v_mfma_f32_16x16x32_bf16 v[104:107], a[104:107], v[4:7], v[104:107]\n"
    "v_mfma_f32_16x16x32_bf16 v[108:111], a[108:111], v[4:7], v[108:111]\n"
    "s_waitcnt vmcnt(6)\n"
    "v_mfma_f32_16x16x32_bf16 v[64:67], a[112:115], v[8:11], v[64:67]\n"
    "v_mfma_f32_16x16x32_bf16 v[68:71], a[116:119], v[8:11], v[68:71]\n"
    "v_mfma_f32_16x16x32_bf16 v[96:99], a[120:123], v[8:11], v[96:99]\n"
    "v_mfma_f32_16x16x32_bf16 v[100:103], a[124:127], v[8:11], v[100:103]\n"
    "s_waitcnt vmcnt(5)\n"
    "v_mfma_f32_16x16x32_bf16 v[72:75], a[112:115], v[12:15], v[72:75]\n"
    "v_mfma_f32_16x16x32_bf16 v[76:79], a[116:119], v[12:15], v[76:79]\n"
    "v_mfma_f32_16x16x32_bf16 v[104:107], a[120:123], v[12:15], v[104:107]\n"
    "v_mfma_f32_16x16x32_bf16 v[108:111], a[124:127], v[12:15], v[108:111]\n"
    "s_waitcnt vmcnt(4)\n"
    "v_mfma_f32_16x16x32_bf16 v[64:67], a[128:131], v[16:19], v[64:67]\n"
    "v_mfma_f32_16x16x32_bf16 v[68:71], a[132:135], v[16:19], v[68:71]\n"
    "v_mfma_f32_16x16x32_bf16 v[96:99], a[136:139], v[16:19], v[96:99]\n"
    "v_mfma_f32_16x16x32_bf16 v[100:103], a[140:143], v[16:19], v[100:103]\n"
    "s_waitcnt vmcnt(3)\n"
    "v_mfma_f32_16x16x32_bf16 v[72:75], a[128:131], v[20:23], v[72:75]\n"
    "v_mfma_f32_16x16x32_bf16 v[76:79], a[132:135], v[20:23], v[76:79]\n"
    "v_mfma_f32_16x16x32_bf16 v[104:107], a[136:139], v[20:23], v[104:107]\n"
    "v_mfma_f32_16x16x32_bf16 v[108:111], a[140:143], v[20:23], v[108:111]\n"
    "s_waitcnt vmcnt(2)\n"
    "v_mfma_f32_16x16x32_bf16 v[64:67], a[144:147], v[24:27], v[64:67]\n"
    "v_mfma_f32_16x16x32_bf16 v[68:71], a[148:151], v[24:27], v[68:71]\n"
    "v_mfma_f32_16x16x32_bf16 v[96:99], a[152:155], v[24:27], v[96:99]\n"
    "v_mfma_f32_16x16x32_bf16 v[100:103], a[156:159], v[24:27], v[100:103]\n"
    "s_waitcnt vmcnt(1)\n"
    "v_mfma_f32_16x16x32_bf16 v[72:75], a[144:147], v[28:31], v[72:75]\n"
    "v_mfma_f32_16x16x32_bf16 v[76:79], a[148:151], v[28:31], v[76:79]\n"
    "v_mfma_f32_16x16x32_bf16 v[104:107], a[152:155], v[28:31], v[104:107]\n"
    "v_mfma_f32_16x16x32_bf16 v[108:111], a[156:159], v[28:31], v[108:111]\n"
    "s_waitcnt vmcnt(0)\n"
    "v_cmp_ne_u64 vcc, s[94:95], v[252:253]\n"
    "s_mov_b32 s91, 0\n"
    "s_cbranch_vccnz late_b\n"
    "s_mov_b32 s91, 1\n"
    "s_add_u32 s73, s87, 0\n"
    "buffer_load_dwordx4 v[0:3], v224, s[60:63], s73 offen offset:2048 sc1\n"
    "buffer_load_dwordx4 v[4:7], v224, s[60:63], s73 offen offset:3072 sc1\n"
    "s_add_u32 s73, s87, 4096\n"
    "buffer_load_dwordx4 v[8:11], v224, s[60:63], s73 offen offset:2048 sc1\n"
    "buffer_load_dwordx4 v[12:15], v224, s[60:63], s73 offen offset:3072 sc1\n"
    "s_add_u32 s73, s87, 8192\n"
    "buffer_load_dwordx4 v[16:19], v224, s[60:63], s73 offen offset:2048 sc1\n"
    "buffer_load_dwordx4 v[20:23], v224, s[60:63], s73 offen offset:3072 sc1\n"
    "s_add_u32 s73, s87, 12288\n"
    "buffer_load_dwordx4 v[24:27], v224, s[60:63], s73 offen offset:2048 sc1\n"
    "buffer_load_dwordx4 v[28:31], v224, s[60:63], s73 offen offset:3072 sc1\n"
    "s_add_u32 s73, s87, 16384\n"
    "buffer_load_dwordx4 v[32:35], v224, s[60:63], s73 offen offset:2048 sc1\n"
    "buffer_load_dwordx4 v[36:39], v224, s[60:63], s73 offen offset:3072 sc1\n"
    "s_add_u32 s73, s87, 20480\n"
    "buffer_load_dwordx4 v[40:43], v224, s[60:63], s73 offen offset:2048 sc1\n"
    "buffer_load_dwordx4 v[44:47], v224, s[60:63], s73 offen offset:3072 sc1\n"
    "s_add_u32 s73, s87, 24576\n"
    "buffer_load_dwordx4 v[48:51], v224, s[60:63], s73 offen offset:2048 sc1\n"
    "buffer_load_dwordx4 v[52:55], v224, s[60:63], s73 offen offset:3072 sc1\n"
    "s_add_u32 s73, s87, 28672\n"
    "buffer_load_dwordx4 v[56:59], v224, s[60:63], s73 offen offset:2048 sc1\n"
    "buffer_load_dwordx4 v[60:63], v224, s[60:63], s73 offen offset:3072 sc1\n"
    "s_add_u32 s73, s87, 32768\n"
    "buffer_load_dwordx4 v[128:131], v224, s[60:63], s73 offen offset:2048 sc1\n"
    "buffer_load_dwordx4 v[132:135], v224, s[60:63], s73 offen offset:3072 sc1\n"
    "s_add_u32 s73, s87, 36864\n"
    "buffer_load_dwordx4 v[136:139], v224, s[60:63], s73 offen offset:2048 sc1\n"
    "buffer_load_dwordx4 v[140:143], v224, s[60:63], s73 offen offset:3072 sc1\n"
    "late_b:\n"
    "s_nop 7\n"
    "ds_write_b128 v216, v[64:67] offset:0\n"
    "ds_write_b128 v216, v[68:71] offset:4096\n"
    "ds_write_b128 v216, v[72:75] offset:8192\n"
    "ds_write_b128 v216, v[76:79] offset:12288\n"
    "ds_write_b128 v216, v[96:99] offset:16384\n"
    "ds_write_b128 v216, v[100:103] offset:20480\n"
    "ds_write_b128 v216, v[104:107] offset:24576\n"
    "ds_write_b128 v216, v[108:111] offset:28672\n"
    "s_waitcnt lgkmcnt(0)\n"
    "s_barrier\n"
    "ds_read_b128 v[64:67], v217 offset:0\n"
    "ds_read_b128 v[68:71], v217 offset:256\n"
    "ds_read_b128 v[72:75], v217 offset:1024\n"
    "ds_read_b128 v[76:79], v217 offset:1280\n"
    "ds_read_b128 v[96:99], v217 offset:2048\n"
    "ds_read_b128 v[100:103], v217 offset:2304\n"
    "ds_read_b128 v[104:107], v217 offset:3072\n"
    "ds_read_b128 v[108:111], v217 offset:3328\n"
    "s_mov_b32 s69, 0\n"
    "s_waitcnt lgkmcnt(4)\n"
    "v_pk_add_f32 v[64:65], v[64:65], v[72:73]\n"
    "v_pk_add_f32 v[66:67], v[66:67], v[74:75]\n"
    "v_pk_add_f32 v[68:69], v[68:69], v[76:77]\n"
    "v_pk_add_f32 v[70:71], v[70:71], v[78:79]\n"
    "s_waitcnt lgkmcnt(0)\n"
    "s_nop 0\n"
    "v_pk_add_f32 v[96:97], v[96:97], v[104:105]\n"
    "v_pk_add_f32 v[98:99], v[98:99], v[106:107]\n"
    "v_pk_add_f32 v[100:101], v[100:101], v[108:109]\n"
    "v_pk_add_f32 v[102:103], v[102:103], v[110:111]\n"
    "v_pk_add_f32 v[64:65], v[64:65], v[96:97]\n"
    "v_pk_add_f32 v[66:67], v[66:67], v[98:99]\n"
    "v_pk_add_f32 v[68:69], v[68:69], v[100:101]\n"
    "v_pk_add_f32 v[70:71], v[70:71], v[102:103]\n"
    "v_pk_fma_f32 v[238:239], v[226:227], v[64:65], v[238:239]\n"
    "v_pk_fma_f32 v[240:241], v[228:229], v[66:67], v[240:241]\n"
    "v_pk_fma_f32 v[242:243], v[230:231], v[68:69], v[242:243]\n"
    "v_pk_fma_f32 v[244:245], v[232:233], v[70:71], v[244:245]\n"
    "v_cvt_pk_bf16_f32 v238, v238, v239\n"
    "v_cvt_pk_bf16_f32 v239, v240, v241\n"
    "v_cvt_pk_bf16_f32 v240, v242, v243\n"
    "v_cvt_pk_bf16_f32 v241, v244, v245\n"
    "buffer_store_dwordx4 v[238:241], v246, s[48:51], s69 offen\n"
    "s_cmp_lg_u32 s91, 0\n"
    "s_cbranch_scc1 have_b\n"
    "s_mov_b32 s90, 32768\n"
    "pl4:\n"
    "buffer_load_dwordx2 v[220:221], v225, s[60:63], s97 offen sc1\n"
    "s_waitcnt vmcnt(0)\n"
    "v_cmp_ne_u64 vcc, s[94:95], v[220:221]\n"
    "s_cbranch_vccz pl4_ok\n"
    "s_sub_u32 s90, s90, 1\n"
    "s_cbranch_scc1 pl4_ok\n"
    "s_sleep 2\n"
    "s_branch pl4\n"
    "pl4_ok:\n"
    "s_add_u32 s73, s87, 0\n"
    "buffer_load_dwordx4 v[0:3], v224, s[60:63], s73 offen offset:2048 sc1\n"
    "buffer_load_dwordx4 v[4:7], v224, s[60:63], s73 offen offset:3072 sc1\n"
    "s_add_u32 s73, s87, 4096\n"
    "buffer_load_dwordx4 v[8:11], v224, s[60:63], s73 offen offset:2048 sc1\n"
    "buffer_load_dwordx4 v[12:15], v224, s[60:63], s73 offen offset:3072 sc1\n"
    "s_add_u32 s73, s87, 8192\n"
    "buffer_load_dwordx4 v[16:19], v224, s[60:63], s73 offen offset:2048 sc1\n"
    "buffer_load_dwordx4 v[20:23], v224, s[60:63], s73 offen offset:3072 sc1\n"
    "s_add_u32 s73, s87, 12288\n"
    "buffer_load_dwordx4 v[24:27], v224, s[60:63], s73 offen offset:2048 sc1\n"
    "buffer_load_dwordx4 v[28:31], v224, s[60:63], s73 offen offset:3072 sc1\n"
    "s_add_u32 s73, s87, 16384\n"
    "buffer_load_dwordx4 v[32:35], v224, s[60:63], s73 offen offset:2048 sc1\n"
    "buffer_load_dwordx4 v[36:39], v224, s[60:63], s73 offen offset:3072 sc1\n"
    "s_add_u32 s73, s87, 20480\n"
    "buffer_load_dwordx4 v[40:43], v224, s[60:63], s73 offen offset:2048 sc1\n"
    "buffer_load_dwordx4 v[44:47], v224, s[60:63], s73 offen offset:3072 sc1\n"
    "s_add_u32 s73, s87, 24576\n"
    "buffer_load_dwordx4 v[48:51], v224, s[60:63], s73 offen offset:2048 sc1\n"
    "buffer_load_dwordx4 v[52:55], v224, s[60:63], s73 offen offset:3072 sc1\n"
    "s_add_u32 s73, s87, 28672\n"
    "buffer_load_dwordx4 v[56:59], v224, s[60:63], s73 offen offset:2048 sc1\n"
    "buffer_load_dwordx4 v[60:63], v224, s[60:63], s73 offen offset:3072 sc1\n"
    "s_add_u32 s73, s87, 32768\n"
    "buffer_load_dwordx4 v[128:131], v224, s[60:63], s73 offen offset:2048 sc1\n"
    "buffer_load_dwordx4 v[132:135], v224, s[60:63], s73 offen offset:3072 sc1\n"
    "s_add_u32 s73, s87, 36864\n"
    "buffer_load_dwordx4 v[136:139], v224, s[60:63], s73 offen offset:2048 sc1\n"
    "buffer_load_dwordx4 v[140:143], v224, s[60:63], s73 offen offset:3072 sc1\n"
    "have_b:\n"
    "s_waitcnt vmcnt(19)\n"
    "v_mfma_f32_16x16x32_bf16 v[80:83], v[144:147], v[0:3], 0\n"
    "v_mfma_f32_16x16x32_bf16 v[84:87], v[148:151], v[0:3], 0\n"
    "v_mfma_f32_16x16x32_bf16 v[112:115], v[152:155], v[0:3], 0\n"
    "v_mfma_f32_16x16x32_bf16 v[116:119], v[156:159], v[0:3], 0\n"
    "s_add_u32 s73, s87, 40960\n"
    "buffer_load_dwordx4 v[0:3], v224, s[60:63], s73 offen offset:2048 sc1\n"
    "s_waitcnt vmcnt(19)\n"
    "v_mfma_f32_16x16x32_bf16 v[88:91], v[144:147], v[4:7], 0\n"
    "v_mfma_f32_16x16x32_bf16 v[92:95], v[148:151], v[4:7], 0\n"
    "v_mfma_f32_16x16x32_bf16 v[120:123], v[152:155], v[4:7], 0\n"
    "v_mfma_f32_16x16x32_bf16 v[124:127], v[156:159], v[4:7], 0\n"
    "buffer_load_dwordx4 v[4:7], v224, s[60:63], s73 offen offset:3072 sc1\n"
    "s_waitcnt vmcnt(19)\n"
    "v_mfma_f32_16x16x32_bf16 v[80:83], v[160:163], v[8:11], v[80:83]\n"
    "v_mfma_f32_16x16x32_bf16 v[84:87], v[164:167], v[8:11], v[84:87]\n"
    "v_mfma_f32_16x16x32_bf16 v[112:115], v[168:171], v[8:11], v[112:115]\n"
    "v_mfma_f32_16x16x32_bf16 v[116:119], v[172:175], v[8:11], v[116:119]\n"
    "s_add_u32 s73, s87, 45056\n"
    "buffer_load_dwordx4 v[8:11], v224, s[60:63], s73 offen offset:2048 sc1\n"
    "s_waitcnt vmcnt(19)\n"
    "v_mfma_f32_16x16x32_bf16 v[88:91], v[160:163], v[12:15], v[88:91]\n"
    "v_mfma_f32_16x16x32_bf16 v[92:95], v[164:167], v[12:15], v[92:95]\n"
    "v_mfma_f32_16x16x32_bf16 v[120:123], v[168:171], v[12:15], v[120:123]\n"
    "v_mfma_f32_16x16x32_bf16 v[124:127], v[172:175], v[12:15], v[124:127]\n"
    "buffer_load_dwordx4 v[12:15], v224, s[60:63], s73 offen offset:3072 sc1\n"
    "s_waitcnt vmcnt(19)\n"
    "v_mfma_f32_16x16x32_bf16 v[80:83], v[176:179], v[16:19], v[80:83]\n"
    "v_mfma_f32_16x16x32_bf16 v[84:87], v[180:183], v[16:19], v[84:87]\n"
    "v_mfma_f32_16x16x32_bf16 v[112:115], v[184:187], v[16:19], v[112:115]\n"
    "v_mfma_f32_16x16x32_bf16 v[116:119], v[188:191], v[16:19], v[116:119]\n"
    "s_add_u32 s73, s87, 49152\n"
    "buffer_load_dwordx4 v[16:19], v224, s[60:63], s73 offen offset:2048 sc1\n"
    "s_waitcnt vmcnt(19)\n"
    "v_mfma_f32_16x16x32_bf16 v[88:91], v[176:179], v[20:23], v[88:91]\n"
    "v_mfma_f32_16x16x32_bf16 v[92:95], v[180:183], v[20:23], v[92:95]\n"
    "v_mfma_f32_16x16x32_bf16 v[120:123], v[184:187], v[20:23], v[120:123]\n"
    "v_mfma_f32_16x16x32_bf16 v[124:127], v[188:191], v[20:23], v[124:127]\n"
    "buffer_load_dwordx4 v[20:23], v224, s[60:63], s73 offen offset:3072 sc1\n"
    "s_waitcnt vmcnt(19)\n"
    "v_mfma_f32_16x16x32_bf16 v[80:83], v[192:195], v[24:27], v[80:83]\n"
    "v_mfma_f32_16x16x32_bf16 v[84:87], v[196:199], v[24:27], v[84:87]\n"
    "v_mfma_f32_16x16x32_bf16 v[112:115], v[200:203], v[24:27], v[112:115]\n"
    "v_mfma_f32_16x16x32_bf16 v[116:119], v[204:207], v[24:27], v[116:119]\n"
    "s_add_u32 s73, s87, 53248\n"
    "buffer_load_dwordx4 v[24:27], v224, s[60:63], s73 offen offset:2048 sc1\n"
    "s_waitcnt vmcnt(19)\n"
    "v_mfma_f32_16x16x32_bf16 v[88:91], v[192:195], v[28:31], v[88:91]\n"
    "v_mfma_f32_16x16x32_bf16 v[92:95], v[196:199], v[28:31], v[92:95]\n"
    "v_mfma_f32_16x16x32_bf16 v[120:123], v[200:203], v[28:31], v[120:123]\n"
    "v_mfma_f32_16x16x32_bf16 v[124:127], v[204:207], v[28:31], v[124:127]\n"
    "buffer_load_dwordx4 v[28:31], v224, s[60:63], s73 offen offset:3072 sc1\n"
    "s_waitcnt vmcnt(19)\n"
    "v_mfma_f32_16x16x32_bf16 v[80:83], a[0:3], v[32:35], v[80:83]\n"
    "v_mfma_f32_16x16x32_bf16 v[84:87], a[4:7], v[32:35], v[84:87]\n"
    "v_mfma_f32_16x16x32_bf16 v[112:115], a[8:11], v[32:35], v[112:115]\n"
    "v_mfma_f32_16x16x32_bf16 v[116:119], a[12:15], v[32:35], v[116:119]\n"
    "s_waitcnt vmcnt(18)\n"
    "v_mfma_f32_16x16x32_bf16 v[88:91], a[0:3], v[36:39], v[88:91]\n"
    "v_mfma_f32_16x16x32_bf16 v[92:95], a[4:7], v[36:39], v[92:95]\n"
    "v_mfma_f32_16x16x32_bf16 v[120:123], a[8:11], v[36:39], v[120:123]\n"
    "v_mfma_f32_16x16x32_bf16 v[124:127], a[12:15], v[36:39], v[124:127]\n"
    "s_waitcnt vmcnt(17)\n"
    "v_mfma_f32_16x16x32_bf16 v[80:83], a[16:19], v[40:43], v[80:83]\n"
    "v_mfma_f32_16x16x32_bf16 v[84:87], a[20:23], v[40:43], v[84:87]\n"
    "v_mfma_f32_16x16x32_bf16 v[112:115], a[24:27], v[40:43], v[112:115]\n"
    "v_mfma_f32_16x16x32_bf16 v[116:119], a[28:31], v[40:43], v[116:119]\n"
    "s_waitcnt vmcnt(16)\n"
    "v_mfma_f32_16x16x32_bf16 v[88:91], a[16:19], v[44:47], v[88:91]\n"
    "v_mfma_f32_16x16x32_bf16 v[92:95], a[20:23], v[44:47], v[92:95]\n"
    "v_mfma_f32_16x16x32_bf16 v[120:123], a[24:27], v[44:47], v[120:123]\n"
    "v_mfma_f32_16x16x32_bf16 v[124:127], a[28:31], v[44:47], v[124:127]\n"
    "s_waitcnt vmcnt(15)\n"
    "v_mfma_f32_16x16x32_bf16 v[80:83], a[32:35], v[48:51], v[80:83]\n"
    "v_mfma_f32_16x16x32_bf16 v[84:87], a[36:39], v[48:51], v[84:87]\n"
    "v_mfma_f32_16x16x32_bf16 v[112:115], a[40:43], v[48:51], v[112:115]\n"
    "v_mfma_f32_16x16x32_bf16 v[116:119], a[44:47], v[48:51], v[116:119]\n"
    "s_waitcnt vmcnt(14)\n"
    "v_mfma_f32_16x16x32_bf16 v[88:91], a[32:35], v[52:55], v[88:91]\n"
    "v_mfma_f32_16x16x32_bf16 v[92:95], a[36:39], v[52:55], v[92:95]\n"
    "v_mfma_f32_16x16x32_bf16 v[120:123], a[40:43], v[52:55], v[120:123]\n"
    "v_mfma_f32_16x16x32_bf16 v[124:127], a[44:47], v[52:55], v[124:127]\n"
    "s_waitcnt vmcnt(13)\n"
    "v_mfma_f32_16x16x32_bf16 v[80:83], a[48:51], v[56:59], v[80:83]\n"
    "v_mfma_f32_16x16x32_bf16 v[84:87], a[52:55], v[56:59], v[84:87]\n"
    "v_mfma_f32_16x16x32_bf16 v[112:115], a[56:59], v[56:59], v[112:115]\n"
    "v_mfma_f32_16x16x32_bf16 v[116:119], a[60:63], v[56:59], v[116:119]\n"
    "s_waitcnt vmcnt(12)\n"
    "v_mfma_f32_16x16x32_bf16 v[88:91], a[48:51], v[60:63], v[88:91]\n"
    "v_mfma_f32_16x16x32_bf16 v[92:95], a[52:55], v[60:63], v[92:95]\n"
    "v_mfma_f32_16x16x32_bf16 v[120:123], a[56:59], v[60:63], v[120:123]\n"
    "v_mfma_f32_16x16x32_bf16 v[124:127], a[60:63], v[60:63], v[124:127]\n"
    "s_waitcnt vmcnt(11)\n"
    "v_mfma_f32_16x16x32_bf16 v[80:83], a[64:67], v[128:131], v[80:83]\n"
    "v_mfma_f32_16x16x32_bf16 v[84:87], a[68:71], v[128:131], v[84:87]\n"
    "v_mfma_f32_16x16x32_bf16 v[112:115], a[72:75], v[128:131], v[112:115]\n"
    "v_mfma_f32_16x16x32_bf16 v[116:119], a[76:79], v[128:131], v[116:119]\n"
    "s_waitcnt vmcnt(10)\n"
    "v_mfma_f32_16x16x32_bf16 v[88:91], a[64:67], v[132:135], v[88:91]\n"
    "v_mfma_f32_16x16x32_bf16 v[92:95], a[68:71], v[132:135], v[92:95]\n"
    "v_mfma_f32_16x16x32_bf16 v[120:123], a[72:75], v[132:135], v[120:123]\n"
    "v_mfma_f32_16x16x32_bf16 v[124:127], a[76:79], v[132:135], v[124:127]\n"
    "s_waitcnt vmcnt(9)\n"
    "v_mfma_f32_16x16x32_bf16 v[80:83], a[80:83], v[136:139], v[80:83]\n"
    "v_mfma_f32_16x16x32_bf16 v[84:87], a[84:87], v[136:139], v[84:87]\n"
    "v_mfma_f32_16x16x32_bf16 v[112:115], a[88:91], v[136:139], v[112:115]\n"
    "v_mfma_f32_16x16x32_bf16 v[116:119], a[92:95], v[136:139], v[116:119]\n"
    "s_waitcnt vmcnt(8)\n"
    "v_mfma_f32_16x16x32_bf16 v[88:91], a[80:83], v[140:143], v[88:91]\n"
    "v_mfma_f32_16x16x32_bf16 v[92:95], a[84:87], v[140:143], v[92:95]\n"
    "v_mfma_f32_16x16x32_bf16 v[120:123], a[88:91], v[140:143], v[120:123]\n"
    "v_mfma_f32_16x16x32_bf16 v[124:127], a[92:95], v[140:143], v[124:127]\n"
    "s_waitcnt vmcnt(7)\n"
    "v_mfma_f32_16x16x32_bf16 v[80:83], a[96:99], v[0:3], v[80:83]\n"
    "v_mfma_f32_16x16x32_bf16 v[84:87], a[100:103], v[0:3], v[84:87]\n"
    "v_mfma_f32_16x16x32_bf16 v[112:115], a[104:107], v[0:3], v[112:115]\n"
    "v_mfma_f32_16x16x32_bf16 v[116:119], a[108:111], v[0:3], v[116:119]\n"
    "s_waitcnt vmcnt(6)\n"
    "v_mfma_f32_16x16x32_bf16 v[88:91], a[96:99], v[4:7], v[88:91]\n"
    "v_mfma_f32_16x16x32_bf16 v[92:95], a[100:103], v[4:7], v[92:95]\n"
    "v_mfma_f32_16x16x32_bf16 v[120:123], a[104:107], v[4:7], v[120:123]\n"
    "v_mfma_f32_16x16x32_bf16 v[124:127], a[108:111], v[4:7], v[124:127]\n"
    "s_waitcnt vmcnt(5)\n"
    "v_mfma_f32_16x16x32_bf16 v[80:83], a[112:115], v[8:11], v[80:83]\n"
    "v_mfma_f32_16x16x32_bf16 v[84:87], a[116:119], v[8:11], v[84:87]\n"
    "v_mfma_f32_16x16x32_bf16 v[112:115], a[120:123], v[8:11], v[112:115]\n"
    "v_mfma_f32_16x16x32_bf16 v[116:119], a[124:127], v[8:11], v[116:119]\n"
    "s_waitcnt vmcnt(4)\n"
    "v_mfma_f32_16x16x32_bf16 v[88:91], a[112:115], v[12:15], v[88:91]\n"
    "v_mfma_f32_16x16x32_bf16 v[92:95], a[116:119], v[12:15], v[92:95]\n"
    "v_mfma_f32_16x16x32_bf16 v[120:123], a[120:123], v[12:15], v[120:123]\n"
    "v_mfma_f32_16x16x32_bf16 v[124:127], a[124:127], v[12:15], v[124:127]\n"
    "s_waitcnt vmcnt(3)\n"
    "v_mfma_f32_16x16x32_bf16 v[80:83], a[128:131], v[16:19], v[80:83]\n"
    "v_mfma_f32_16x16x32_bf16 v[84:87], a[132:135], v[16:19], v[84:87]\n"
    "v_mfma_f32_16x16x32_bf16 v[112:115], a[136:139], v[16:19], v[112:115]\n"
    "v_mfma_f32_16x16x32_bf16 v[116:119], a[140:143], v[16:19], v[116:119]\n"
    "s_waitcnt vmcnt(2)\n"
    "v_mfma_f32_16x16x32_bf16 v[88:91], a[128:131], v[20:23], v[88:91]\n"
    "v_mfma_f32_16x16x32_bf16 v[92:95], a[132:135], v[20:23], v[92:95]\n"
    "v_mfma_f32_16x16x32_bf16 v[120:123], a[136:139], v[20:23], v[120:123]\n"
    "v_mfma_f32_16x16x32_bf16 v[124:127], a[140:143], v[20:23], v[124:127]\n"
    "s_waitcnt vmcnt(1)\n"
    "v_mfma_f32_16x16x32_bf16 v[80:83], a[144:147], v[24:27], v[80:83]\n"
    "v_mfma_f32_16x16x32_bf16 v[84:87], a[148:151], v[24:27], v[84:87]\n"
    "v_mfma_f32_16x16x32_bf16 v[112:115], a[152:155], v[24:27], v[112:115]\n"
    "v_mfma_f32_16x16x32_bf16 v[116:119], a[156:159], v[24:27], v[116:119]\n"
    "s_waitcnt vmcnt(0)\n"
    "v_mfma_f32_16x16x32_bf16 v[88:91], a[144:147], v[28:31], v[88:91]\n"
    "v_mfma_f32_16x16x32_bf16 v[92:95], a[148:151], v[28:31], v[92:95]\n"
    "v_mfma_f32_16x16x32_bf16 v[120:123], a[152:155], v[28:31], v[120:123]\n"
    "v_mfma_f32_16x16x32_bf16 v[124:127], a[156:159], v[28:31], v[124:127]\n"
    "s_nop 11\n"
    "ds_write_b128 v216, v[80:83] offset:32768\n"
    "ds_write_b128 v216, v[84:87] offset:36864\n"
    "ds_write_b128 v216, v[88:91] offset:40960\n"
    "ds_write_b128 v216, v[92:95] offset:45056\n"
    "ds_write_b128 v216, v[112:115] offset:49152\n"
    "ds_write_b128 v216, v[116:119] offset:53248\n"
    "ds_write_b128 v216, v[120:123] offset:57344\n"
    "ds_write_b128 v216, v[124:127] offset:61440\n"
    "s_waitcnt lgkmcnt(0)\n"
    "s_barrier\n"
    "ds_read_b128 v[80:83], v217 offset:32768\n"
    "ds_read_b128 v[84:87], v217 offset:33024\n"
    "ds_read_b128 v[88:91], v217 offset:33792\n"
    "ds_read_b128 v[92:95], v217 offset:34048\n"
    "ds_read_b128 v[112:115], v217 offset:34816\n"
    "ds_read_b128 v[116:119], v217 offset:35072\n"
    "ds_read_b128 v[120:123], v217 offset:35840\n"
    "ds_read_b128 v[124:127], v217 offset:36096\n"
    "s_mov_b32 s69, 32768\n"
    "s_waitcnt lgkmcnt(4)\n"
    "v_pk_add_f32 v[80:81], v[80:81], v[88:89]\n"
    "v_pk_add_f32 v[82:83], v[82:83], v[90:91]\n"
    "v_pk_add_f32 v[84:85], v[84:85], v[92:93]\n"
    "v_pk_add_f32 v[86:87], v[86:87], v[94:95]\n"
    "s_waitcnt lgkmcnt(0)\n"
    "s_nop 0\n"
    "v_pk_add_f32 v[112:113], v[112:113], v[120:121]\n"
    "v_pk_add_f32 v[114:115], v[114:115], v[122:123]\n"
    "v_pk_add_f32 v[116:117], v[116:117], v[124:125]\n"
    "v_pk_add_f32 v[118:119], v[118:119], v[126:127]\n"
    "v_pk_add_f32 v[80:81], v[80:81], v[112:113]\n"
    "v_pk_add_f32 v[82:83], v[82:83], v[114:115]\n"
    "v_pk_add_f32 v[84:85], v[84:85], v[116:117]\n"
    "v_pk_add_f32 v[86:87], v[86:87], v[118:119]\n"
    "v_pk_fma_f32 v[208:209], v[226:227], v[80:81], v[208:209]\n"
    "v_pk_fma_f32 v[210:211], v[228:229], v[82:83], v[210:211]\n"
    "v_pk_fma_f32 v[212:213], v[230:231], v[84:85], v[212:213]\n"
    "v_pk_fma_f32 v[214:215], v[232:233], v[86:87], v[214:215]\n"
    "v_cvt_pk_bf16_f32 v208, v208, v209\n"
    "v_cvt_pk_bf16_f32 v209, v210, v211\n"
    "v_cvt_pk_bf16_f32 v210, v212, v213\n"
    "v_cvt_pk_bf16_f32 v211, v214, v215\n"
    "buffer_store_dwordx4 v[208:211], v246, s[48:51], s69 offen\n"
    "s_endpgm\n"
    "attn_redo:\n"
    "s_mov_b32 s91, 1\n"
    "s_mov_b32 s73, 0\n"
    "v_mov_b32 v210, 0xff800000\n"
    "v_mov_b32 v211, 0xff800000\n"
    "v_mov_b32 v212, 0xff800000\n"
    "v_mov_b32 v213, 0xff800000\n"
    "rd_loop:\n"
    "s_add_u32 s96, s73, 4096\n"
    "buffer_load_dwordx4 a[128:131], v247, s[44:47], s73 offen offset:0\n"
    "buffer_load_dwordx4 a[132:135], v247, s[44:47], s73 offen offset:64\n"
    "buffer_load_dwordx4 a[136:139], v247, s[44:47], s73 offen offset:128\n"
    "buffer_load_dwordx4 a[140:143], v247, s[44:47], s73 offen offset:192\n"
    "buffer_load_dwordx4 a[144:147], v247, s[44:47], s96 offen offset:0\n"
    "buffer_load_dwordx4 a[148:151], v247, s[44:47], s96 offen offset:64\n"
    "buffer_load_dwordx4 a[152:155], v247, s[44:47], s96 offen offset:128\n"
    "buffer_load_dwordx4 a[156:159], v247, s[44:47], s96 offen offset:192\n"
    "s_waitcnt vmcnt(0)\n"
    "v_mfma_f32_16x16x32_bf16 v[64:67], a[128:131], v[0:3], 0\n"
    "v_mfma_f32_16x16x32_bf16 v[64:67], a[132:135], v[4:7], v[64:67]\n"
    "v_mfma_f32_16x16x32_bf16 v[64:67], a[136:139], v[8:11], v[64:67]\n"
    "v_mfma_f32_16x16x32_bf16 v[64:67], a[140:143], v[12:15], v[64:67]\n"
    "v_mfma_f32_16x16x32_bf16 v[72:75], a[128:131], v[16:19], 0\n"
    "v_mfma_f32_16x16x32_bf16 v[72:75], a[132:135], v[20:23], v[72:75]\n"
    "v_mfma_f32_16x16x32_bf16 v[72:75], a[136:139], v[24:27], v[72:75]\n"
    "v_mfma_f32_16x16x32_bf16 v[72:75], a[140:143], v[28:31], v[72:75]\n"
    "v_mfma_f32_16x16x32_bf16 v[80:83], a[128:131], v[32:35], 0\n"
    "v_mfma_f32_16x16x32_bf16 v[80:83], a[132:135], v[36:39], v[80:83]\n"
    "v_mfma_f32_16x16x32_bf16 v[80:83], a[136:139], v[40:43], v[80:83]\n"
    "v_mfma_f32_16x16x32_bf16 v[80:83], a[140:143], v[44:47], v[80:83]\n"
    "v_mfma_f32_16x16x32_bf16 v[88:91], a[128:131], v[48:51], 0\n"
    "v_mfma_f32_16x16x32_bf16 v[88:91], a[132:135], v[52:55], v[88:91]\n"
    "v_mfma_f32_16x16x32_bf16 v[88:91], a[136:139], v[56:59], v[88:91]\n"
    "v_mfma_f32_16x16x32_bf16 v[88:91], a[140:143], v[60:63], v[88:91]\n"
    "v_mfma_f32_16x16x32_bf16 v[68:71], a[144:147], v[0:3], 0\n"
    "v_mfma_f32_16x16x32_bf16 v[68:71], a[148:151], v[4:7], v[68:71]\n"
    "v_mfma_f32_16x16x32_bf16 v[68:71], a[152:155], v[8:11], v[68:71]\n"
    "v_mfma_f32_16x16x32_bf16 v[68:71], a[156:159], v[12:15], v[68:71]\n"
    "v_mfma_f32_16x16x32_bf16 v[76:79], a[144:147], v[16:19], 0\n"
    "v_mfma_f32_16x16x32_bf16 v[76:79], a[148:151], v[20:23], v[76:79]\n"
    "v_mfma_f32_16x16x32_bf16 v[76:79], a[152:155], v[24:27], v[76:79]\n"
    "v_mfma_f32_16x16x32_bf16 v[76:79], a[156:159], v[28:31], v[76:79]\n"
    "v_mfma_f32_16x16x32_bf16 v[84:87], a[144:147], v[32:35], 0\n"
    "v_mfma_f32_16x16x32_bf16 v[84:87], a[148:151], v[36:39], v[84:87]\n"
    "v_mfma_f32_16x16x32_bf16 v[84:87], a[152:155], v[40:43], v[84:87]\n"
    "v_mfma_f32_16x16x32_bf16 v[84:87], a[156:159], v[44:47], v[84:87]\n"
    "v_mfma_f32_16x16x32_bf16 v[92:95], a[144:147], v[48:51], 0\n"
    "v_mfma_f32_16x16x32_bf16 v[92:95], a[148:151], v[52:55], v[92:95]\n"
    "v_mfma_f32_16x16x32_bf16 v[92:95], a[152:155], v[56:59], v[92:95]\n"
    "v_mfma_f32_16x16x32_bf16 v[92:95], a[156:159], v[60:63], v[92:95]\n"
    "s_nop 15\n"
    "s_nop 0\n"
    "v_max3_f32 v210, v210, v64, v65\n"
    "v_max3_f32 v210, v210, v66, v67\n"
    "v_max3_f32 v210, v210, v68, v69\n"
    "v_max3_f32 v210, v210, v70, v71\n"
    "v_max3_f32 v211, v211, v72, v73\n"
    "v_max3_f32 v211, v211, v74, v75\n"
    "v_max3_f32 v211, v211, v76, v77\n"
    "v_max3_f32 v211, v211, v78, v79\n"
    "v_max3_f32 v212, v212, v80, v81\n"
    "v_max3_f32 v212, v212, v82, v83\n"
    "v_max3_f32 v212, v212, v84, v85\n"
    "v_max3_f32 v212, v212, v86, v87\n"
    "v_max3_f32 v213, v213, v88, v89\n"
    "v_max3_f32 v213, v213, v90, v91\n"
    "v_max3_f32 v213, v213, v92, v93\n"
    "v_max3_f32 v213, v213, v94, v95\n"
    "s_add_u32 s73, s73, 8192\n"
    "s_cmp_lg_u32 s73, 131072\n"
    "s_cbranch_scc1 rd_loop\n"
    "v_mov_b32 v176, v210\n"
    "v_mov_b32 v177, v211\n"
    "v_mov_b32 v178, v212\n"
    "v_mov_b32 v179, v213\n"
    "v_permlane32_swap_b32 v210, v176\n"
    "v_permlane32_swap_b32 v211, v177\n"
    "v_permlane32_swap_b32 v212, v178\n"
    "v_permlane32_swap_b32 v213, v179\n"
    "v_max_f32 v210, v210, v176\n"
    "v_max_f32 v211, v211, v177\n"
    "v_max_f32 v212, v212, v178\n"
    "v_max_f32 v213, v213, v179\n"
    "v_mov_b32 v176, v210\n"
    "v_mov_b32 v177, v211\n"
    "v_mov_b32 v178, v212\n"
    "v_mov_b32 v179, v213\n"
    "v_permlane16_swap_b32 v210, v176\n"
    "v_permlane16_swap_b32 v211, v177\n"
    "v_permlane16_swap_b32 v212, v178\n"
    "v_permlane16_swap_b32 v213, v179\n"
    "v_max_f32 v210, v210, v176\n"
    "v_max_f32 v211, v211, v177\n"
    "v_max_f32 v212, v212, v178\n"
    "v_max_f32 v213, v213, v179\n"
    "v_sub_f32 v210, 0, v210\n"
    "v_sub_f32 v211, 0, v211\n"
    "v_sub_f32 v212, 0, v212\n"
    "v_sub_f32_e64 v213, 0, v213\n"
    "s_mov_b32 s86, 0\n"
    "s_mov_b32 s87, 0\n"
    "s_waitcnt vmcnt(0) lgkmcnt(0)\n"
    "s_barrier\n"
    "s_add_u32 m0, s88, 0\n"
    "s_nop 0\n"
    "buffer_load_dwordx4 v234, s[44:47], s86 offen lds\n"
    "buffer_load_dwordx4 v235, s[44:47], s86 offen offset:1024 lds\n"
    "s_add_u32 s86, s86, 8192\n"
    "s_add_u32 m0, s88, 8192\n"
    "s_nop 0\n"
    "buffer_load_dwordx4 v234, s[44:47], s86 offen lds\n"
    "buffer_load_dwordx4 v235, s[44:47], s86 offen offset:1024 lds\n"
    "s_add_u32 s86, s86, 8192\n"
    "s_add_u32 m0, s89, 0\n"
    "s_nop 0\n"
    "buffer_load_dwordx4 v236, s[48:51], s87 offen lds\n"
    "buffer_load_dwordx4 v233, s[48:51], s87 offen offset:1024 lds\n"
    "s_add_u32 s87, s87, 8192\n"
    "s_add_u32 m0, s89, 8192\n"
    "s_nop 0\n"
    "buffer_load_dwordx4 v236, s[48:51], s87 offen lds\n"
    "buffer_load_dwordx4 v233, s[48:51], s87 offen offset:1024 lds\n"
    "s_add_u32 s87, s87, 8192\n"
    "s_waitcnt vmcnt(0)\n"
    "s_barrier\n"
    "ds_read_b128 a[128:131], v196 offset:0\n"
    "ds_read_b128 a[132:135], v197 offset:0\n"
    "ds_read_b128 a[136:139], v198 offset:0\n"
    "ds_read_b128 a[140:143], v199 offset:0\n"
    "ds_read_b128 a[144:147], v196 offset:4096\n"
    "ds_read_b128 a[148:151], v197 offset:4096\n"
    "ds_read_b128 a[152:155], v198 offset:4096\n"
    "ds_read_b128 a[156:159], v199 offset:4096\n"
    "s_waitcnt lgkmcnt(0)\n"
    "s_barrier\n"
    "s_add_u32 m0, s88, 0\n"
    "s_nop 0\n"
    "buffer_load_dwordx4 v234, s[44:47], s86 offen lds\n"
    "buffer_load_dwordx4 v235, s[44:47], s86 offen offset:1024 lds\n"
    "s_add_u32 s86, s86, 8192\n"
    "v_mfma_f32_16x16x32_bf16 v[64:67], a[128:131], v[0:3], 0\n"
    "v_mfma_f32_16x16x32_bf16 v[64:67], a[132:135], v[4:7], v[64:67]\n"
    "v_mfma_f32_16x16x32_bf16 v[64:67], a[136:139], v[8:11], v[64:67]\n"
    "v_mfma_f32_16x16x32_bf16 v[64:67], a[140:143], v[12:15], v[64:67]\n"
    "v_mfma_f32_16x16x32_bf16 v[72:75], a[128:131], v[16:19], 0\n"
    "v_mfma_f32_16x16x32_bf16 v[72:75], a[132:135], v[20:23], v[72:75]\n"
    "v_mfma_f32_16x16x32_bf16 v[72:75], a[136:139], v[24:27], v[72:75]\n"
    "v_mfma_f32_16x16x32_bf16 v[72:75], a[140:143], v[28:31], v[72:75]\n"
    "v_mfma_f32_16x16x32_bf16 v[80:83], a[128:131], v[32:35], 0\n"
    "v_mfma_f32_16x16x32_bf16 v[80:83], a[132:135], v[36:39], v[80:83]\n"
    "v_mfma_f32_16x16x32_bf16 v[80:83], a[136:139], v[40:43], v[80:83]\n"
    "v_mfma_f32_16x16x32_bf16 v[80:83], a[140:143], v[44:47], v[80:83]\n"
    "v_mfma_f32_16x16x32_bf16 v[88:91], a[128:131], v[48:51], 0\n"
    "v_mfma_f32_16x16x32_bf16 v[88:91], a[132:135], v[52:55], v[88:91]\n"
    "v_mfma_f32_16x16x32_bf16 v[88:91], a[136:139], v[56:59], v[88:91]\n"
    "v_mfma_f32_16x16x32_bf16 v[88:91], a[140:143], v[60:63], v[88:91]\n"
    "v_mfma_f32_16x16x32_bf16 v[68:71], a[144:147], v[0:3], 0\n"
    "v_mfma_f32_16x16x32_bf16 v[68:71], a[148:151], v[4:7], v[68:71]\n"
    "v_mfma_f32_16x16x32_bf16 v[68:71], a[152:155], v[8:11], v[68:71]\n"
    "v_mfma_f32_16x16x32_bf16 v[68:71], a[156:159], v[12:15], v[68:71]\n"
    "v_mfma_f32_16x16x32_bf16 v[76:79], a[144:147], v[16:19], 0\n"
    "v_mfma_f32_16x16x32_bf16 v[76:79], a[148:151], v[20:23], v[76:79]\n"
    "v_mfma_f32_16x16x32_bf16 v[76:79], a[152:155], v[24:27], v[76:79]\n"
    "v_mfma_f32_16x16x32_bf16 v[76:79], a[156:159], v[28:31], v[76:79]\n"
    "v_mfma_f32_16x16x32_bf16 v[84:87], a[144:147], v[32:35], 0\n"
    "v_mfma_f32_16x16x32_bf16 v[84:87], a[148:151], v[36:39], v[84:87]\n"
    "v_mfma_f32_16x16x32_bf16 v[84:87], a[152:155], v[40:43], v[84:87]\n"
    "v_mfma_f32_16x16x32_bf16 v[84:87], a[156:159], v[44:47], v[84:87]\n"
    "v_mfma_f32_16x16x32_bf16 v[92:95], a[144:147], v[48:51], 0\n"
    "v_mfma_f32_16x16x32_bf16 v[92:95], a[148:151], v[52:55], v[92:95]\n"
    "v_mfma_f32_16x16x32_bf16 v[92:95], a[152:155], v[56:59], v[92:95]\n"
    "v_mfma_f32_16x16x32_bf16 v[92:95], a[156:159], v[60:63], v[92:95]\n"
    "s_branch attn_re\n"
    "k_end:\n"
    :
    : [ka] "s"(__builtin_amdgcn_kernarg_segment_ptr()), [bx] "s"(blockIdx.x), [tid] "v"(threadIdx.x)
    : "v0", "v1", "v2", "v3", "v4", "v5", "v6", "v7", "v8", "v9", "v10", "v11", "v12", "v13", "v14", "v15", "v16", "v17", "v18", "v19", "v20", "v21", "v22", "v23", "v24", "v25", "v26", "v27", "v28", "v29", "v30", "v31", "v32", "v33", "v34", "v35", "v36", "v37", "v38", "v39", "v40", "v41", "v42", "v43", "v44", "v45", "v46", "v47", "v48", "v49", "v50", "v51", "v52", "v53", "v54", "v55", "v56", "v57", "v58", "v59", "v60", "v61", "v62", "v63", "v64", "v65", "v66", "v67", "v68", "v69", "v70", "v71", "v72", "v73", "v74", "v75", "v76", "v77", "v78", "v79", "v80", "v81", "v82", "v83", "v84", "v85", "v86", "v87", "v88", "v89", "v90", "v91", "v92", "v93", "v94", "v95", "v96", "v97", "v98", "v99", "v100", "v101", "v102", "v103", "v104", "v105", "v106", "v107", "v108", "v109", "v110", "v111", "v112", "v113", "v114", "v115", "v116", "v117", "v118", "v119", "v120", "v121", "v122", "v123", "v124", "v125", "v126", "v127", "v128", "v129", "v130", "v131", "v132", "v133", "v134", "v135", "v136", "v137", "v138", "v139", "v140", "v141", "v142", "v143", "v144", "v145", "v146", "v147", "v148", "v149", "v150", "v151", "v152", "v153", "v154", "v155", "v156", "v157", "v158", "v159", "v160", "v161", "v162", "v163", "v164", "v165", "v166", "v167", "v168", "v169", "v170", "v171", "v172", "v173", "v174", "v175", "v176", "v177", "v178", "v179", "v180", "v181", "v182", "v183", "v184", "v185", "v186", "v187", "v188", "v189", "v190", "v191", "v192", "v193", "v194", "v195", "v196", "v197", "v198", "v199", "v200", "v201", "v202", "v203", "v204", "v205", "v206", "v207", "v208", "v209", "v210", "v211", "v212", "v213", "v214", "v215", "v216", "v217", "v218", "v219", "v220", "v221", "v222", "v223", "v224", "v225", "v226", "v227", "v228", "v229", "v230", "v231", "v232", "v233", "v234", "v235", "v236", "v237", "v238", "v239", "v240", "v241", "v242", "v243", "v244", "v245", "v246", "v247", "v248", "v249", "v250", "v251", "v252", "v253", "v254", "a0", "a1", "a2", "a3", "a4", "a5", "a6", "a7", "a8", "a9", "a10", "a11", "a12", "a13", "a14", "a15", "a16", "a17", "a18", "a19", "a20", "a21", "a22", "a23", "a24", "a25", "a26", "a27", "a28", "a29", "a30", "a31", "a32", "a33", "a34", "a35", "a36", "a37", "a38", "a39", "a40", "a41", "a42", "a43", "a44", "a45", "a46", "a47", "a48", "a49", "a50", "a51", "a52", "a53", "a54", "a55", "a56", "a57", "a58", "a59", "a60", "a61", "a62", "a63", "a64", "a65", "a66", "a67", "a68", "a69", "a70", "a71", "a72", "a73", "a74", "a75", "a76", "a77", "a78", "a79", "a80", "a81", "a82", "a83", "a84", "a85", "a86", "a87", "a88", "a89", "a90", "a91", "a92", "a93", "a94", "a95", "a96", "a97", "a98", "a99", "a100", "a101", "a102", "a103", "a104", "a105", "a106", "a107", "a108", "a109", "a110", "a111", "a112", "a113", "a114", "a115", "a116", "a117", "a118", "a119", "a120", "a121", "a122", "a123", "a124", "a125", "a126", "a127", "a128", "a129", "a130", "a131", "a132", "a133", "a134", "a135", "a136", "a137", "a138", "a139", "a140", "a141", "a142", "a143", "a144", "a145", "a146", "a147", "a148", "a149", "a150", "a151", "a152", "a153", "a154", "a155", "a156", "a157", "a158", "a159", "a160", "a161", "a162", "a163", "a164", "a165", "a166", "a167", "a168", "a169", "a170", "a171", "a172", "a173", "a174", "a175", "a176", "a177", "a178", "a179", "a180", "a181", "a182", "a183", "a184", "a185", "a186", "a187", "a188", "a189", "a190", "a191", "a192", "a193", "a194", "a195", "a196", "a197", "a198", "a199", "a200", "a201", "a202", "a203", "a204", "a205", "a206", "a207", "a208", "a209", "a210", "a211", "a212", "a213", "a214", "a215", "a216", "a217", "a218", "a219", "a220", "a221", "a222", "a223", "a224", "a225", "a226", "a227", "a228", "a229", "a230", "a231", "a232", "a233", "a234", "a235", "a236", "a237", "a238", "a239", "a240", "a241", "a242", "a243", "a244", "a245", "a246", "a247", "a248", "a249", "a250", "a251", "a252", "a253", "a254", "a255", "s3", "s4", "s5", "s6", "s7", "s8", "s9", "s10", "s11", "s12", "s13", "s14", "s15", "s16", "s17", "s18", "s19", "s20", "s21", "s22", "s23", "s24", "s25", "s26", "s27", "s28", "s29", "s30", "s31", "s33", "s34", "s35", "s36", "s37", "s38", "s39", "s40", "s41", "s42", "s43", "s44", "s45", "s46", "s47", "s48", "s49", "s50", "s51", "s52", "s53", "s54", "s55", "s56", "s57", "s58", "s59", "s60", "s61", "s62", "s63", "s64", "s65", "s66", "s67", "s68", "s69", "s70", "s71", "s72", "s73", "s74", "s75", "s76", "s77", "s78", "s79", "s80", "s81", "s82", "s83", "s84", "s85", "s86", "s87", "s88", "s89", "s90", "s91", "s92", "s93", "s94", "s95", "s96", "s97", "s98", "s99", "vcc", "scc", "memory");
}

extern "C" void kernel_launch(void* const* d_in, const int* in_sizes, int n_in, void* d_out, int out_size, void* d_ws, size_t ws_size, hipStream_t stream) {
  (void)hipFuncSetAttribute((const void*)k_main, hipFuncAttributeMaxDynamicSharedMemorySize, 163840);
  k_main<<<256, 256, 163840, stream>>>((const char*)d_in[0], (const char*)d_in[1], (const char*)d_in[2], (const char*)d_in[3], (const char*)d_in[4],
                                       (const char*)d_in[6], (const char*)d_in[7], (const char*)d_in[8], (const char*)d_in[9],
                                       (const char*)d_in[10], (const char*)d_in[11], (char*)d_ws, (char*)d_out);
}
```
